```python
import torch

def _softmax(x, axis=-1):
    return torch.softmax(x, dim=axis)
def _gelu(x):
    return torch.nn.functional.gelu(x, approximate="tanh")
def _silu(x):
    return torch.nn.functional.silu(x)
def _sigmoid(x):
    return torch.sigmoid(x)
def _layernorm(x, axis=-1, eps=1e-5):
    m = x.mean(dim=axis, keepdim=True)
    v = x.var(dim=axis, keepdim=True, unbiased=False)
    return (x - m) / torch.sqrt(v + eps)
def _scatter_add(tgt, idx, src, axis=0):
    return tgt.clone().index_add_(axis, idx, src)
def _segment_sum(src, seg, n):
    out = torch.zeros((n,) + src.shape[1:], dtype=src.dtype)
    return out.index_add_(0, seg, src)
def _pad(x, pw):
    flat = tuple(v for pair in reversed(pw) for v in pair)
    return torch.nn.functional.pad(x, flat)

kernel_name = 'interleave_layers__softmax_full_mha__mixer_token_mlp__prompt'

def setup_inputs():

    _w = (lambda a: (lambda t: t.to(torch.bfloat16) if t.is_floating_point() else t)(torch.as_tensor(a)))
    def _A_setup():
        B, S, D = 1, 2048, 512
        return {
            "x": _w((lambda *s: torch.randn(*s, dtype=torch.bfloat16) * 0.05)(B, S, D)),
            "wqkv": _w((lambda *s: torch.randn(*s, dtype=torch.bfloat16) * 0.05)(D, 3 * D)),
            "wo": _w((lambda *s: torch.randn(*s, dtype=torch.bfloat16) * 0.05)(D, D)),
        }
    _A_in = _A_setup()

    def _B_setup():
        B, S, D, F = 1, 2048, 512, 1792
        return {
            "x": _w((lambda *s: torch.randn(*s, dtype=torch.bfloat16) * 0.05)(B, S, D)),
            "w1": _w((lambda *s: torch.randn(*s, dtype=torch.bfloat16) * 0.8)(S, F) * (S ** -0.5)),
            "b1": _w((lambda *s: torch.randn(*s, dtype=torch.bfloat16) * 12.8)(F) * 0.02),
            "w2": _w((lambda *s: torch.randn(*s, dtype=torch.bfloat16) * 0.2)(F, S) * (F ** -0.5)),
            "b2": _w((lambda *s: torch.randn(*s, dtype=torch.bfloat16) * 3.2)(S) * 0.02),
        }
    _B_in = _B_setup()

    return {
        "A_x": _A_in["x"],
        "A_wqkv": _A_in["wqkv"],
        "A_wo": _A_in["wo"],
        "B_x": _B_in["x"],
        "B_w1": _B_in["w1"],
        "B_b1": _B_in["b1"],
        "B_w2": _B_in["w2"],
        "B_b2": _B_in["b2"],
    }

def reference(A_x, A_wqkv, A_wo, B_x, B_w1, B_b1, B_w2, B_b2):
    def _A_ref(x, wqkv, wo):
        B, S, D = x.shape
        H = 32; Dh = D // H
        qkv = (lambda x, *d: x.permute(*d))((x @ wqkv).reshape(B, S, 3, H, Dh), 2, 0, 3, 1, 4)
        q, k, v = qkv[0], qkv[1], qkv[2]
        scale = Dh ** -0.5
        scores = torch.einsum("bhqd,bhkd->bhqk", q, k) * scale
        p = _softmax(scores, axis=-1)
        o = torch.einsum("bhqk,bhkd->bhqd", p, v)
        return (lambda x, *d: x.permute(*d))(o, 0, 2, 1, 3).reshape(B, S, D) @ wo

    def _B_ref(x, w1, b1, w2, b2):
        B, S, D = x.shape
        h = _layernorm(x, axis=-1)
        u = torch.einsum("bsd,sf->bdf", h, w1) + b1
        u = _gelu(u)
        y = torch.einsum("bdf,fs->bds", u, w2) + b2
        return (lambda x, *d: x.permute(*d))(y, 0, 2, 1)

    x = A_x
    x = x + _A_ref(_layernorm(x, axis=-1), A_wqkv, A_wo)
    x = x + _B_ref(_layernorm(x, axis=-1), B_w1, B_b1, B_w2, B_b2)
    x = x + _A_ref(_layernorm(x, axis=-1), A_wqkv, A_wo)
    x = x + _B_ref(_layernorm(x, axis=-1), B_w1, B_b1, B_w2, B_b2)
    return x
```

```cpp
#include <hip/hip_runtime.h>
#include <hip/hip_bf16.h>
#include <stdint.h>

#define STAMPS 0

typedef __attribute__((ext_vector_type(16))) float f32x16;
typedef __attribute__((ext_vector_type(8))) float f32x8;
typedef __attribute__((ext_vector_type(4))) float f32x4;
typedef __attribute__((ext_vector_type(2))) float f32x2;
typedef __attribute__((ext_vector_type(16))) __bf16 bf16x16;
typedef __attribute__((ext_vector_type(8))) __bf16 bf16x8;
typedef __attribute__((ext_vector_type(2))) __bf16 bf16x2;
typedef __attribute__((ext_vector_type(4))) unsigned u32x4;
typedef __attribute__((ext_vector_type(2))) unsigned u32x2;
typedef __attribute__((ext_vector_type(4))) short s16x4;
typedef __amdgpu_buffer_rsrc_t rsrc_t;
#define LAS __attribute__((address_space(3)))
typedef LAS char* lptr;
#define DI __device__ __forceinline__

constexpr unsigned WS_NONCE = 0x0;
constexpr unsigned WS_F1 = 0x1000;
constexpr unsigned WS_F2 = 0x11000;
constexpr unsigned WS_F3 = 0x19000;
constexpr unsigned WS_F4 = 0x1B000;
constexpr unsigned WS_F5 = 0x1D000;
constexpr unsigned WS_ST = 0x20000;
constexpr unsigned WS_KV = 0x200000;
constexpr unsigned WS_O = 0xA00000;
constexpr unsigned WS_H2 = 0xE00000;
constexpr unsigned WS_U = 0x1200000;
constexpr unsigned WS_X2 = 0x1600000;
constexpr unsigned WS_DBG = 0x1800000;
constexpr unsigned WS_END = 0x1900000;
constexpr unsigned L_WIMG = 0;
constexpr unsigned L_XST = 49152;
constexpr unsigned L_ONES = 131072;
constexpr unsigned L_SCR = 147456;
constexpr unsigned L_TOTAL = 163840;
constexpr int AUX_SC1 = 16;
constexpr unsigned SPIN_CAP = 1u << 16;
constexpr float LN_EPS = 1e-5f;

#define SBAR() __builtin_amdgcn_sched_barrier(0)
#define PIN(x) asm volatile("" : "+v"(x))
#define MFMA(a, b, c) __builtin_amdgcn_mfma_f32_32x32x16_bf16(a, b, c, 0, 0, 0)
#define EX(x) __builtin_amdgcn_exp2f(x)
#define VMWAIT(n) asm volatile("s_waitcnt vmcnt(" #n ")" ::: "memory")
#define BAR() asm volatile("s_waitcnt lgkmcnt(0)\n\ts_barrier" ::: "memory")

template <int N> DI void vm_wait() { asm volatile("s_waitcnt vmcnt(%0)" ::"i"(N) : "memory"); }
template <int N> DI void vm_wait_bar() { asm volatile("s_waitcnt vmcnt(%0) lgkmcnt(0)\n\ts_barrier" ::"i"(N) : "memory"); }
DI rsrc_t mk_rsrc(const void* p, unsigned bytes) { return __builtin_amdgcn_make_buffer_rsrc((void*)p, 0, bytes, 0x00020000); }
template <int N, int LSTEP, int GSTEP> DI void dma(unsigned lds, unsigned voff, rsrc_t rs, unsigned soff) {
    unsigned t;
    asm volatile("s_mov_b32 m0, %1\n\ts_mov_b32 %0, %4\n\tbuffer_load_dwordx4 %2, %3, %0 offen lds\n\t"
                 ".rept %5\n\ts_add_u32 m0, m0, %6\n\ts_add_u32 %0, %0, %7\n\tbuffer_load_dwordx4 %2, %3, %0 offen lds\n\t.endr"
                 : "=&s"(t) : "s"(lds), "v"(voff), "s"(rs), "s"(soff), "i"(N - 1), "i"(LSTEP), "i"(GSTEP) : "memory", "scc");
}
template <int N, int LSTEP, int GSTEP> DI void dma_sc1(unsigned lds, unsigned voff, rsrc_t rs, unsigned soff) {
    unsigned t;
    asm volatile("s_mov_b32 m0, %1\n\ts_mov_b32 %0, %4\n\tbuffer_load_dwordx4 %2, %3, %0 offen sc1 lds\n\t"
                 ".rept %5\n\ts_add_u32 m0, m0, %6\n\ts_add_u32 %0, %0, %7\n\tbuffer_load_dwordx4 %2, %3, %0 offen sc1 lds\n\t.endr"
                 : "=&s"(t) : "s"(lds), "v"(voff), "s"(rs), "s"(soff), "i"(N - 1), "i"(LSTEP), "i"(GSTEP) : "memory", "scc");
}
DI bf16x8 ld_frag(rsrc_t rs, unsigned voff, unsigned soff) { return __builtin_bit_cast(bf16x8, __builtin_amdgcn_raw_buffer_load_b128(rs, voff, soff, AUX_SC1)); }
DI void st_frag(u32x4 v, rsrc_t rs, unsigned voff, unsigned soff) { __builtin_amdgcn_raw_buffer_store_b128(v, rs, voff, soff, AUX_SC1); }
DI bf16x8 lds_frag(lptr p) { return *(LAS const bf16x8*)p; }
DI s16x4 lds_tr(lptr p) { return __builtin_amdgcn_ds_read_tr16_b64_v4i16((LAS s16x4*)p); }
DI bf16x8 tr_frag(lptr p0, lptr p1) {
    const s16x4 a = lds_tr(p0), b = lds_tr(p1);
    return __builtin_bit_cast(bf16x8, __builtin_shufflevector(a, b, 0, 1, 2, 3, 4, 5, 6, 7));
}
DI unsigned pk2(float a, float b) { const f32x2 v = {a, b}; return __builtin_bit_cast(unsigned, __builtin_convertvector(v, bf16x2)); }
DI float bf_lo(unsigned u) { return __uint_as_float(u << 16); }
DI float bf_hi(unsigned u) { return __uint_as_float(u & 0xffff0000u); }
DI float dot2(unsigned a, unsigned b, float c) { return __builtin_amdgcn_fdot2_f32_bf16(__builtin_bit_cast(bf16x2, a), __builtin_bit_cast(bf16x2, b), c, false); }
DI float half_sum2(float a, float b) { const auto r = __builtin_amdgcn_permlane32_swap(__float_as_uint(a), __float_as_uint(b), false, false); return __uint_as_float(r[0]) + __uint_as_float(r[1]); }
DI f32x8 lo8(f32x16 v) { return __builtin_shufflevector(v, v, 0, 1, 2, 3, 4, 5, 6, 7); }
DI f32x8 hi8(f32x16 v) { return __builtin_shufflevector(v, v, 8, 9, 10, 11, 12, 13, 14, 15); }
DI f32x16 smf(bf16x8 a, f32x8 p, f32x16 c, int idx) { return __builtin_amdgcn_smfmac_f32_32x32x32_bf16(a, __builtin_bit_cast(bf16x16, p), c, idx, 0, 0); }
DI int opaque(int v) { asm volatile("" : "+v"(v)); return v; }
DI int sopaque(int v) { asm volatile("" : "+s"(v)); return v; }

DI void wait32(rsrc_t ws, unsigned voff, unsigned soff, unsigned tag) {
    for (unsigned i = 0; i < SPIN_CAP; ++i) {
        const unsigned v = __builtin_amdgcn_raw_buffer_load_b32(ws, voff, soff, AUX_SC1);
        if (__all(v == tag)) break;
        asm volatile("" ::: "memory");
    }
    asm volatile("" ::: "memory");
}
DI void wait64(rsrc_t ws, unsigned voff, unsigned soff, unsigned tlo, unsigned thi) {
    for (unsigned i = 0; i < SPIN_CAP; ++i) {
        const u32x2 v = __builtin_amdgcn_raw_buffer_load_b64(ws, voff, soff, AUX_SC1);
        if (__all(v[0] == tlo && v[1] == thi)) break;
        asm volatile("" ::: "memory");
    }
    asm volatile("" ::: "memory");
}
DI void flag32(rsrc_t ws, unsigned soff, unsigned tag, int lane) {
    VMWAIT(0);
    if (lane == 0) __builtin_amdgcn_raw_buffer_store_b32(tag, ws, 0, soff, AUX_SC1);
    asm volatile("" ::: "memory");
}
DI void flag64(rsrc_t ws, unsigned soff, unsigned tlo, unsigned thi, int lane) {
    VMWAIT(0);
    if (lane == 0) __builtin_amdgcn_raw_buffer_store_b64(u32x2{tlo, thi}, ws, 0, soff, AUX_SC1);
    asm volatile("" ::: "memory");
}

#define STAMP(i) do { } while (0)

DI unsigned wvoff(int lane, unsigned ldw) { return (lane >> 3) * ldw + (((lane & 7) ^ (((lane >> 4) & 1) << 2)) << 4); }
template <int S0, int S1, int SLOT0, int LDW> DI void ring_prefetch(unsigned lb, rsrc_t rw, unsigned voff, unsigned gb) {
#pragma unroll
    for (int s = S0; s < S1; ++s) dma<4, 1024, 8 * LDW>(lb + ((s + SLOT0) & 7) * 16384, voff, rw, gb + s * 64 * LDW);
}
template <int I, int NST, int SLOT0, int LDW> DI void ring_loader(unsigned lb, rsrc_t rw, unsigned voff, unsigned gb) {
    if constexpr (I < NST) {
        constexpr int last = (I == 0) ? 7 : I + 6;
        constexpr int younger = (last < NST - 1 ? last : NST - 1) - I;
        vm_wait_bar<4 * younger>();
        if constexpr (I >= 1 && I + 7 < NST) ring_prefetch<I + 7, I + 8, SLOT0, LDW>(lb, rw, voff, gb);
        ring_loader<I + 1, NST, SLOT0, LDW>(lb, rw, voff, gb);
    }
}
template <int NST, int SLOT0, bool ACT_A, bool BARS, int PD>
DI void gemm_compute(lptr lds, int lane, int kh, rsrc_t ws, unsigned act_soff, f32x16& acc0, f32x16& acc1) {
    constexpr int NK = NST * 4;
    const int hh = lane >> 5, gq = (lane >> 4) & 1, q = (lane >> 2) & 3, p = lane & 3;
    const unsigned a0 = kh * 8192 + (4 * hh + q) * 128 + (((4 * kh + 2 * gq + (p >> 1)) ^ (((q >> 1) & 1) << 2)) << 4) + (p & 1) * 8;
    const lptr b0 = lds + a0, b1 = lds + (a0 ^ 64), b0h = b0 + 65536, b1h = b1 + 65536;
    const unsigned av = lane * 16;
    bf16x8 f[PD];
#pragma unroll
    for (int i = 0; i < PD; ++i) if (i < NK) f[i] = ld_frag(ws, av, act_soff + i * 1024);
    acc0 = f32x16{}; acc1 = f32x16{};
#pragma unroll
    for (int s = 0; s < NST; ++s) {
        if (BARS) BAR();
        const int slot = (s + SLOT0) & 7;
#pragma unroll
        for (int t = 0; t < 4; ++t) {
            const int k = s * 4 + t, o = (slot & 3) * 16384 + t * 2048;
            const lptr p0 = (slot & 4) ? b0h : b0, p1 = (slot & 4) ? b1h : b1;
            const bf16x8 w0 = tr_frag(p0 + o, p0 + o + 1024), w1 = tr_frag(p1 + o, p1 + o + 1024);
            const bf16x8 x = f[k % PD];
            if (ACT_A) { acc0 = MFMA(x, w0, acc0); acc1 = MFMA(x, w1, acc1); }
            else { acc0 = MFMA(w0, x, acc0); acc1 = MFMA(w1, x, acc1); }
            if (k + PD < NK) f[k % PD] = ld_frag(ws, av, act_soff + (k + PD) * 1024);
        }
    }
}
DI void exchange(lptr lds, int lane, int w, int partner, f32x16& acc0, const f32x16& acc1) {
    LAS f32x4* dst = (LAS f32x4*)(lds + L_SCR + w * 4096 + lane * 16);
#pragma unroll
    for (int i = 0; i < 4; ++i) dst[i * 64] = f32x4{acc1[4 * i], acc1[4 * i + 1], acc1[4 * i + 2], acc1[4 * i + 3]};
    BAR();
    LAS const f32x4* src = (LAS const f32x4*)(lds + L_SCR + partner * 4096 + lane * 16);
#pragma unroll
    for (int i = 0; i < 4; ++i) { const f32x4 v = src[i * 64]; acc0[4 * i] += v[0]; acc0[4 * i + 1] += v[1]; acc0[4 * i + 2] += v[2]; acc0[4 * i + 3] += v[3]; }
}

struct QkvAcc { f32x16 qk, v; float sum, sq; };
DI void qkv_step(QkvAcc& a, bf16x8 x, lptr pqk, lptr pv, int o0, int o1) {
    const bf16x8 wqk = tr_frag(pqk + o0, pqk + o1), wv = tr_frag(pv + o0, pv + o1);
    const u32x4 xu = __builtin_bit_cast(u32x4, x);
#pragma unroll
    for (int i = 0; i < 4; ++i) { a.sq = dot2(xu[i], xu[i], a.sq); a.sum = dot2(xu[i], 0x3f803f80u, a.sum); }
    a.qk = MFMA(wqk, x, a.qk);
    a.v = MFMA(x, wv, a.v);
}
DI void colsum_partial(lptr lds, int lane, int w) {
    const int hh = lane >> 5, gq = (lane >> 4) & 1, q = (lane >> 2) & 3, p = lane & 3;
    const lptr pb = lds + L_WIMG + w * 6144 + hh * 768 + q * 32 + p * 8;
    float cq = 0.f, cv = 0.f;
#pragma unroll
    for (int t = 0; t < 4; ++t) {
        const u32x4 a = __builtin_bit_cast(u32x4, tr_frag(pb + gq * 128 + t * 1536, pb + gq * 128 + t * 1536 + 384));
        const u32x4 b = __builtin_bit_cast(u32x4, tr_frag(pb + 256 + t * 1536, pb + 256 + t * 1536 + 384));
#pragma unroll
        for (int i = 0; i < 4; ++i) { cq = dot2(a[i], 0x3f803f80u, cq); cv = dot2(b[i], 0x3f803f80u, cv); }
    }
    const float c = half_sum2(cq, cv);
    if (lane < 48) *(LAS float*)(lds + L_SCR + lane * 32 + w * 4) = c;
}
DI void colsum_final(lptr lds, int lane) {
    if (lane < 48) {
        const f32x4 a = *(LAS const f32x4*)(lds + L_SCR + lane * 32), b = *(LAS const f32x4*)(lds + L_SCR + lane * 32 + 16);
        *(LAS float*)(lds + L_SCR + 2048 + lane * 4) = ((a[0] + a[1]) + (a[2] + a[3])) + ((b[0] + b[1]) + (b[2] + b[3]));
    }
}

#define ATT_TILE(C, N, KOFF, VOFF) do { \
        N = MFMA(kf, qf, cneg); SBAR(); kf = lds_frag(kp + (KOFF)); \
        C[3] = EX(C[3]); C[4] = EX(C[4]); C[5] = EX(C[5]); C[6] = EX(C[6]); C[7] = EX(C[7]); C[8] = EX(C[8]); C[9] = EX(C[9]); PIN(C); SBAR(); \
        o = smf(v0, lo8(C), o, idx); SBAR(); v0 = lds_frag(vp + (VOFF)); \
        C[10] = EX(C[10]); C[11] = EX(C[11]); C[12] = EX(C[12]); C[13] = EX(C[13]); C[14] = EX(C[14]); C[15] = EX(C[15]); \
        N[0] = EX(N[0]); N[1] = EX(N[1]); PIN(C); PIN(N); SBAR(); \
        o = smf(v1, hi8(C), o, idx); SBAR(); v1 = lds_frag(vp + (VOFF) + 512); \
        N[2] = EX(N[2]); PIN(N); SBAR(); \
    } while (0)

__global__ __launch_bounds__(512, 2) void fused_fwd(const void* __restrict__ xin, const void* __restrict__ wqkv, const void* __restrict__ wo,
                                                    const void* __restrict__ w1, const unsigned short* __restrict__ b1, const void* __restrict__ w2,
                                                    const unsigned short* __restrict__ b2, unsigned short* __restrict__ out, char* wsp) {
    __shared__ __attribute__((aligned(16))) char lds_raw[L_TOTAL];
    const lptr lds = (lptr)lds_raw;
    const unsigned lds0 = (unsigned)(uintptr_t)lds;
    const int w_ = __builtin_amdgcn_readfirstlane(threadIdx.x >> 6);
#define PHASE_IDS \
    const int lane = opaque(threadIdx.x & 63), l31 = lane & 31, hh = lane >> 5; \
    const int w = sopaque(w_), bid = sopaque(blockIdx.x), g = bid & 7, r = bid >> 3; \
    const int st64 = g * 4 + (r >> 3), dt64 = r & 7; \
    const int dt1 = (g & 1) * 4 + (r & 3), ft1 = (g >> 1) * 7 + (r >> 2); \
    const int own_s32 = st64 * 2 + (w & 1), own_d32 = dt64 * 2 + ((w >> 1) & 1); \
    (void)l31; (void)hh; (void)dt1; (void)ft1; (void)own_s32; (void)own_d32;
    const bool w1_on = (blockIdx.x >> 3) < 28;
    const rsrc_t ws = mk_rsrc(wsp, WS_END);

    unsigned xres[8] = {};
    unsigned tag_lo, tag_hi;
    u32x2 nonce = {};

#pragma unroll 1
    for (int L = 0; L < 2; ++L) {
        bf16x8 qf;
        {
            PHASE_IDS
            const int gq = (lane >> 4) & 1, q = (lane >> 2) & 3, p = lane & 3;
            const unsigned kv_soff = WS_KV + (L * 32 + r) * 131072;
            STAMP(0);
            if (w >= 4) {
                const rsrc_t rw = mk_rsrc(wqkv, 512 * 3072);
#pragma unroll
                for (int ty = 0; ty < 3; ++ty) {
                    const int o = ty * 1024 + lane * 16, rg = o / 384, rem = o % 384;
                    const unsigned voff = (4 * rg + ((rem >> 5) & 3)) * 3072 + (rem >> 7) * 1024 + (rem & 16);
                    dma<4, 3072, 98304>(lds0 + L_WIMG + (w - 4) * 12288 + ty * 1024, voff, rw, (w - 4) * 393216 + r * 32);
                }
            }
            QkvAcc a; a.qk = f32x16{}; a.v = f32x16{}; a.sum = 0.f; a.sq = 0.f;
            if (L == 0) {
                const rsrc_t rx = mk_rsrc(xin, 2048 * 1024);
                const unsigned tokb = (g * 256 + w * 32) * 1024, stg = L_XST + w * 12288;
                const unsigned vA = (lane >> 3) * 1024 + (((lane & 7) ^ (lane >> 4)) << 4), vB = vA ^ 64;
#define XCHUNK(c) do { dma<2, 2048, 16384>(lds0 + stg + ((c) % 3) * 4096, vA, rx, tokb + (c) * 128); \
                       dma<2, 2048, 16384>(lds0 + stg + ((c) % 3) * 4096 + 1024, vB, rx, tokb + 8192 + (c) * 128); } while (0)
                XCHUNK(0); XCHUNK(1); XCHUNK(2);
                nonce = __builtin_amdgcn_raw_buffer_load_b64(ws, 0, WS_NONCE, AUX_SC1);
                if (w < 4) {
#pragma unroll
                    for (int i = 0; i < 4; ++i) {
                        const u32x2 v = __builtin_amdgcn_raw_buffer_load_b64(rx, l31 * 1024 + hh * 8, own_s32 * 32768 + own_d32 * 64 + i * 16, 0);
                        xres[2 * i] = v[0]; xres[2 * i + 1] = v[1];
                    }
#pragma unroll
                    for (int ll = 0; ll < 2; ++ll)
                        __builtin_amdgcn_raw_buffer_store_b64(u32x2{0u, 0u}, ws, lane * 8, WS_ST + ((ll * 64 + own_s32) * 16 + own_d32) * 512, AUX_SC1);
                    const int f32t = ft1 * 2 + (w >> 1), d1 = dt1 * 2 + (w & 1), Ll = lane & 1;
                    unsigned fa = WS_F3 + ((Ll * 16 + own_d32) * 2 + (own_s32 >> 5)) * 128 + (own_s32 & 31) * 4;
                    if (lane >= 2) fa = WS_F4 + ((Ll * 16 + d1) * 2 + (f32t >= 28)) * 128 + (f32t % 28) * 4;
                    if (lane == 4) fa = WS_F5 + own_s32 * 128 + own_d32 * 4;
                    if (lane < 2 || lane == 4 || (lane < 4 && w1_on)) __builtin_amdgcn_raw_buffer_store_b32(0u, ws, fa, 0, AUX_SC1);
                }
                vm_wait_bar<12>();
                colsum_partial(lds, lane, w);
                const lptr pqk = lds + L_WIMG + hh * 768 + gq * 128 + q * 32 + p * 8, pv = lds + L_WIMG + hh * 768 + 256 + q * 32 + p * 8;
                lptr px[4];
#pragma unroll
                for (int t = 0; t < 4; ++t) px[t] = lds + stg + l31 * 128 + (((2 * t + hh) ^ ((l31 >> 1) & 7)) << 4);
#pragma unroll
                for (int c = 0; c < 8; ++c) {
                    if (c <= 5) vm_wait<8>(); else if (c == 6) vm_wait<4>(); else vm_wait<0>();
                    bf16x8 xf[4];
#pragma unroll
                    for (int t = 0; t < 4; ++t) xf[t] = lds_frag(px[t] + (c % 3) * 4096);
                    if (c + 3 < 8) { asm volatile("s_waitcnt lgkmcnt(0)" : "+v"(xf[0]), "+v"(xf[1]), "+v"(xf[2]), "+v"(xf[3]) :: "memory"); XCHUNK(c + 3); }
#pragma unroll
                    for (int t = 0; t < 4; ++t) qkv_step(a, xf[t], pqk, pv, (4 * c + t) * 1536, (4 * c + t) * 1536 + 384);
                    if (c == 1) { BAR(); if (w == 0) colsum_final(lds, lane); }
                }
#undef XCHUNK
            } else {
                wait32(ws, (lane & 15) * 4, WS_F5 + (g * 8 + w) * 128, 1u);
                STAMP(1);
                bf16x8 xf[32];
#pragma unroll
                for (int t = 0; t < 32; ++t) xf[t] = ld_frag(ws, lane * 16, WS_X2 + ((g * 8 + w) * 32 + t) * 1024);
                BAR();
                colsum_partial(lds, lane, w);
                const lptr pqk = lds + L_WIMG + hh * 384 + gq * 128 + q * 32 + p * 8, pv = lds + L_WIMG + hh * 384 + 256 + q * 32 + p * 8;
#pragma unroll
                for (int t = 0; t < 32; ++t) {
                    qkv_step(a, xf[t], pqk, pv, t * 1536, t * 1536 + 768);
                    if (t == 7) { BAR(); if (w == 0) colsum_final(lds, lane); }
                }
            }
            STAMP(2);
            BAR();
            const float tot = half_sum2(a.sum, a.sq);
            const auto sw = __builtin_amdgcn_permlane32_swap(__float_as_uint(tot), __float_as_uint(tot), false, false);
            const float mean = __uint_as_float(sw[0]) * (1.f / 512.f);
            const float var = __builtin_fmaxf(__uint_as_float(sw[1]) * (1.f / 512.f) - mean * mean, 0.f);
            const float rstd = __builtin_amdgcn_rsqf(var + LN_EPS), mr = mean * rstd;
            const lptr sst = lds + L_SCR + 4096 + w * 256;
            *(LAS float*)(sst + lane * 4) = hh ? mr : rstd;
            float qk[16];
#pragma unroll
            for (int i = 0; i < 4; ++i) {
                const f32x4 cs = *(LAS const f32x4*)(lds + L_SCR + 2048 + (8 * i + 4 * hh) * 4);
#pragma unroll
                for (int j = 0; j < 4; ++j) qk[4 * i + j] = a.qk[4 * i + j] * rstd - mr * cs[j];
            }
            const u32x4 kpk = {pk2(qk[8], qk[9]), pk2(qk[10], qk[11]), pk2(qk[12], qk[13]), pk2(qk[14], qk[15])};
            const unsigned koff = (hh * 32 + ((l31 & 19) | ((l31 & 4) << 1) | ((l31 & 8) >> 1))) * 16;
            st_frag(kpk, ws, koff, kv_soff + (g * 8 + w) * 2048);
            *(LAS u32x4*)(lds + w * 1024 + koff) = kpk;
            const float csv = *(LAS const float*)(lds + L_SCR + 2048 + 128 + (lane & 15) * 4);
            float vv[16];
#pragma unroll
            for (int i = 0; i < 4; ++i) {
                const f32x4 rs = *(LAS const f32x4*)(sst + (8 * i + 4 * hh) * 4), ms = *(LAS const f32x4*)(sst + 128 + (8 * i + 4 * hh) * 4);
#pragma unroll
                for (int j = 0; j < 4; ++j) vv[4 * i + j] = a.v[4 * i + j] * rs[j] - ms[j] * csv;
            }
            u32x4 vpk;
#pragma unroll
            for (int i = 0; i < 4; ++i) { const unsigned lo = pk2(vv[2 * i], vv[2 * i + 1]), hi = pk2(vv[8 + 2 * i], vv[9 + 2 * i]); vpk[i] = gq ? hi : lo; }
            const unsigned voff = gq * 512 + hh * 256 + (lane & 15) * 16;
            st_frag(vpk, ws, voff, kv_soff + (g * 8 + w) * 2048 + 1024);
            *(LAS u32x4*)(lds + 8192 + w * 1024 + voff) = vpk;
            *(LAS u32x4*)(lds + L_ONES + w * 1024 + lane * 16) = u32x4{0x3f803f80u, 0x3f803f80u, 0x3f803f80u, 0x3f803f80u};
            constexpr float QS = 0.25f * 1.4426950408889634f;
            qf = __builtin_bit_cast(bf16x8, u32x4{pk2(qk[0] * QS, qk[1] * QS), pk2(qk[2] * QS, qk[3] * QS), pk2(qk[4] * QS, qk[5] * QS), pk2(qk[6] * QS, qk[7] * QS)});
            BAR();
            STAMP(3);
        }
        {
            PHASE_IDS
            const int gq = (lane >> 4) & 1;
            const unsigned kv_soff = WS_KV + (L * 32 + r) * 131072;
            {
                unsigned long long z = ((unsigned long long)__builtin_amdgcn_readfirstlane(nonce[1]) << 32) | (unsigned)__builtin_amdgcn_readfirstlane(nonce[0]);
                z = z * 0x9E3779B97F4A7C15ull + 0x632BE59BD9B4E019ull;
                z ^= z >> 29; z *= 0xBF58476D1CE4E5B9ull; z ^= z >> 32;
                tag_lo = ((unsigned)z & ~1u) + L; tag_hi = (unsigned)(z >> 32);
            }
            const int idx = opaque(0xDDDD);
            lptr kp = lds + lane * 16;
            lptr vp = lds + (gq ? L_ONES : 8192) + hh * 256 + (lane & 15) * 16;
            const int vinc = gq ? 0 : 16384;
            bf16x8 kf = lds_frag(kp), v0 = lds_frag(vp), v1 = lds_frag(vp + 512);
            f32x16 sA = MFMA(kf, qf, f32x16{}), sB, cneg, o = {};
            {
                float m = __builtin_fmaxf(sA[0], sA[1]);
#pragma unroll
                for (int i = 2; i < 16; i += 2) m = __builtin_fmaxf(__builtin_fmaxf(m, sA[i]), sA[i + 1]);
                const auto sw = __builtin_amdgcn_permlane32_swap(__float_as_uint(m), __float_as_uint(m), false, false);
                m = -__builtin_fmaxf(__uint_as_float(sw[0]), __uint_as_float(sw[1]));
#pragma unroll
                for (int i = 0; i < 16; ++i) cneg[i] = m;
                PIN(cneg);
            }
            sA = MFMA(kf, qf, cneg);
            kf = lds_frag(kp + 1024);
            sA[0] = EX(sA[0]); sA[1] = EX(sA[1]); sA[2] = EX(sA[2]);
#pragma unroll
            for (int c = 0; c < 8; ++c) {
                ATT_TILE(sA, sB, 2048, 1024);
                ATT_TILE(sB, sA, 3072, 2048);
                if (c == 0) flag64(ws, WS_F1 + ((L * 32 + r) * 8 + w) * 128 + g * 8, tag_lo, tag_hi, lane);
                ATT_TILE(sA, sB, 4096, 3072);
                ATT_TILE(sB, sA, 5120, 4096);
                if (c == 0) {
                    STAMP(4);
                    wait64(ws, (lane & 7) * 8, WS_F1 + ((L * 32 + r) * 8 + w) * 128, tag_lo, tag_hi);
                    STAMP(5);
#pragma unroll
                    for (int j = 1; j < 8; ++j) {
                        dma_sc1<2, 8192, 1024>(lds0 + j * 16384 + w * 1024, lane * 16, ws, kv_soff + (((g + j) & 7) * 8 + w) * 2048);
                    }
                }
                ATT_TILE(sA, sB, 6144, 5120);
                ATT_TILE(sB, sA, 7168, 6144);
                if (c == 0) vm_wait_bar<12>(); else if (c == 1) vm_wait_bar<10>(); else if (c == 2) vm_wait_bar<8>(); else if (c == 3) vm_wait_bar<6>();
                else if (c == 4) vm_wait_bar<4>(); else if (c == 5) vm_wait_bar<10>(); else if (c == 6) vm_wait_bar<8>();
                if (c == 0) STAMP(6);
                if (c == 4) dma<8, 1024, 8192>(lds0 + w * 8192, wvoff(lane, 1024), mk_rsrc(wo, 512 * 1024), ((w & 1) * 256 + (w >> 1) * 64) * 1024 + dt64 * 128);
                if (c < 7) {
                    ATT_TILE(sA, sB, 16384, 7168);
                    vp += vinc;
                    ATT_TILE(sB, sA, 16384 + 1024, 0);
                    kp += 16384;
                } else {
                    ATT_TILE(sA, sB, 0, 7168);
                    sB[3] = EX(sB[3]); sB[4] = EX(sB[4]); sB[5] = EX(sB[5]); sB[6] = EX(sB[6]); sB[7] = EX(sB[7]);
                    o = smf(v0, lo8(sB), o, idx);
#pragma unroll
                    for (int i = 8; i < 16; ++i) sB[i] = EX(sB[i]);
                    o = smf(v1, hi8(sB), o, idx);
                }
            }
            STAMP(7);
            float chk = __builtin_fabsf(o[8]);
#pragma unroll
            for (int i = 0; i < 8; ++i) chk += __builtin_fabsf(o[i]);
            if (__builtin_expect(__any(!(chk < 1e36f)), 0)) {
                float m = -__builtin_inff();
                o = f32x16{};
                const u32x4 one4 = {0x3f803f80u, 0x3f803f80u, 0x3f803f80u, 0x3f803f80u};
#pragma unroll 1
                for (int t = 0; t < 64; ++t) {
                    const bf16x8 kk = ld_frag(ws, lane * 16, kv_soff + t * 2048);
                    u32x4 a0 = __builtin_amdgcn_raw_buffer_load_b128(ws, hh * 256 + (lane & 15) * 16, kv_soff + t * 2048 + 1024, AUX_SC1);
                    u32x4 a1 = __builtin_amdgcn_raw_buffer_load_b128(ws, hh * 256 + (lane & 15) * 16, kv_soff + t * 2048 + 1536, AUX_SC1);
                    if (gq) { a0 = one4; a1 = one4; }
                    f32x16 s = MFMA(kk, qf, f32x16{});
                    float tm = m;
#pragma unroll
                    for (int i = 0; i < 16; ++i) tm = __builtin_fmaxf(tm, s[i]);
                    const auto sw = __builtin_amdgcn_permlane32_swap(__float_as_uint(tm), __float_as_uint(tm), false, false);
                    tm = __builtin_fmaxf(__uint_as_float(sw[0]), __uint_as_float(sw[1]));
                    const float al = EX(m - tm);
                    m = tm;
#pragma unroll
                    for (int i = 0; i < 16; ++i) { o[i] *= al; s[i] = EX(s[i] - m); }
                    o = smf(__builtin_bit_cast(bf16x8, a0), lo8(s), o, idx);
                    o = smf(__builtin_bit_cast(bf16x8, a1), hi8(s), o, idx);
                }
            }
            const float il = 1.f / o[8];
            st_frag(u32x4{pk2(o[0] * il, o[1] * il), pk2(o[2] * il, o[3] * il), pk2(o[4] * il, o[5] * il), pk2(o[6] * il, o[7] * il)}, ws, lane * 16,
                    WS_O + L * 0x200000 + ((g * 8 + w) * 32 + r) * 1024);
            flag64(ws, WS_F2 + ((L * 64 + g * 8 + w) * 2 + (r >> 4)) * 128 + (r & 15) * 8, tag_lo, tag_hi, lane);
            BAR();
            STAMP(8);
        }
        if (w_ >= 4) {
            PHASE_IDS
            const int lw = w - 4;
            const rsrc_t r1 = mk_rsrc(w1, 2048 * 3584), r2 = mk_rsrc(w2, 1792 * 4096);
            const unsigned v1o = wvoff(lane, 3584), v2o = wvoff(lane, 4096), lb = lds0 + lw * 4096;
            const unsigned g1 = ((lw >> 1) * 1024 + (lw & 1) * 32) * 3584 + ft1 * 128, g2 = ((lw >> 1) * 896 + (lw & 1) * 32) * 4096 + st64 * 128;
            if (w1_on) ring_prefetch<0, 4, 4, 3584>(lb, r1, v1o, g1);
            BAR();
            if (w1_on) {
                ring_prefetch<4, 8, 4, 3584>(lb, r1, v1o, g1);
                ring_loader<0, 16, 4, 3584>(lb, r1, v1o, g1);
                BAR();
            }
            ring_prefetch<0, 8, 4, 4096>(lb, r2, v2o, g2);
            ring_loader<0, 14, 4, 4096>(lb, r2, v2o, g2);
            BAR();
        } else {
            f32x16 acc0, acc1;
            {
                PHASE_IDS
                const int kh = w >> 1;
                wait64(ws, (lane & 15) * 8, WS_F2 + ((L * 64 + own_s32) * 2 + kh) * 128, tag_lo, tag_hi);
                STAMP(9);
                gemm_compute<4, 0, false, false, 16>(lds, lane, kh, ws, WS_O + L * 0x200000 + (own_s32 * 32 + kh * 16) * 1024, acc0, acc1);
                STAMP(10);
                exchange(lds, lane, w, w ^ 2, acc0, acc1);
                float xv[16], sum = 0.f, sq = 0.f;
#pragma unroll
                for (int i = 0; i < 8; ++i) {
                    xres[i] = pk2(bf_lo(xres[i]) + acc0[2 * i], bf_hi(xres[i]) + acc0[2 * i + 1]);
                    xv[2 * i] = bf_lo(xres[i]); xv[2 * i + 1] = bf_hi(xres[i]);
                    sum += xv[2 * i] + xv[2 * i + 1];
                    sq = __builtin_fmaf(xv[2 * i], xv[2 * i], __builtin_fmaf(xv[2 * i + 1], xv[2 * i + 1], sq));
                }
                const unsigned st_soff = WS_ST + (L * 64 + own_s32) * 8192;
                __builtin_amdgcn_raw_buffer_store_b64(u32x2{__float_as_uint(half_sum2(sum, sq)), (unsigned)(L + 1)}, ws, lane * 8, st_soff + own_d32 * 512, AUX_SC1);
                STAMP(11);
                float tot = 0.f;
                for (unsigned it = 0; it < SPIN_CAP; ++it) {
                    u32x2 gv[16];
#pragma unroll
                    for (int i = 0; i < 16; ++i) gv[i] = __builtin_amdgcn_raw_buffer_load_b64(ws, lane * 8, st_soff + i * 512, AUX_SC1);
                    bool ok = true; tot = 0.f;
#pragma unroll
                    for (int i = 0; i < 16; ++i) { ok = ok && (gv[i][1] == (unsigned)(L + 1)); tot += __uint_as_float(gv[i][0]); }
                    if (__all(ok)) break;
                    asm volatile("" ::: "memory");
                }
                STAMP(12);
                const auto sw = __builtin_amdgcn_permlane32_swap(__float_as_uint(tot), __float_as_uint(tot), false, false);
                const float mean = __uint_as_float(sw[0]) * (1.f / 512.f);
                const float var = __builtin_fmaxf(__uint_as_float(sw[1]) * (1.f / 512.f) - mean * mean, 0.f);
                const float sc = __builtin_amdgcn_rsqf(var * (1.f + LN_EPS) + LN_EPS * LN_EPS), ms = mean * sc;
                const lptr tp = lds + L_SCR + (w ^ 2) * 4096;
                u32x4 h0, h1;
#pragma unroll
                for (int i = 0; i < 4; ++i) {
                    h0[i] = pk2(xv[2 * i] * sc - ms, xv[2 * i + 1] * sc - ms);
                    h1[i] = pk2(xv[8 + 2 * i] * sc - ms, xv[9 + 2 * i] * sc - ms);
                }
                *(LAS u32x4*)(tp + l31 * 64 + hh * 32) = h0;
                *(LAS u32x4*)(tp + l31 * 64 + hh * 32 + 16) = h1;
                const lptr rp = tp + (4 * hh + ((lane >> 2) & 3)) * 64 + ((lane >> 4) & 1) * 32 + (lane & 3) * 8;
                const unsigned hv = (hh * 32 + ((l31 & 3) | ((l31 & 12) << 1) | ((l31 & 16) >> 2))) * 16;
#pragma unroll
                for (int t = 0; t < 2; ++t)
                    st_frag(__builtin_bit_cast(u32x4, tr_frag(rp + t * 1024, rp + t * 1024 + 512)), ws, hv, WS_H2 + L * 0x200000 + (own_d32 * 128 + own_s32 * 2 + t) * 1024);
                flag32(ws, WS_F3 + ((L * 16 + own_d32) * 2 + (own_s32 >> 5)) * 128 + (own_s32 & 31) * 4, L + 1, lane);
                if (L == 0 && bid == 0 && w == 0 && lane == 0)
                    __builtin_amdgcn_raw_buffer_store_b64(u32x2{nonce[0] + 1u, nonce[1] + (nonce[0] == 0xffffffffu)}, ws, 0, WS_NONCE, AUX_SC1);
                STAMP(13);
            }
            if (w1_on) {
                PHASE_IDS
                const int kh = w >> 1, d32 = dt1 * 2 + (w & 1), f32t = ft1 * 2 + kh;
                u32x2 bb[4];
#pragma unroll
                for (int i = 0; i < 4; ++i) bb[i] = *(const u32x2*)(b1 + f32t * 32 + 8 * i + 4 * hh);
                wait32(ws, (lane & 31) * 4, WS_F3 + ((L * 16 + d32) * 2 + kh) * 128, L + 1);
                STAMP(14);
                gemm_compute<16, 4, false, true, 16>(lds, lane, kh, ws, WS_H2 + L * 0x200000 + (d32 * 128 + kh * 64) * 1024, acc0, acc1);
                STAMP(15);
                exchange(lds, lane, w, w ^ 2, acc0, acc1);
                float u[16];
#pragma unroll
                for (int i = 0; i < 16; ++i) {
                    const unsigned bw = bb[i >> 2][(i >> 1) & 1];
                    const float z = acc0[i] + ((i & 1) ? bf_hi(bw) : bf_lo(bw));
                    const float e = EX(z * __builtin_fmaf(z * z, -0.10294324f, -2.30220818f));
                    u[i] = z * __builtin_amdgcn_rcpf(1.f + e);
                }
#pragma unroll
                for (int t = 0; t < 2; ++t)
                    st_frag(u32x4{pk2(u[8 * t], u[8 * t + 1]), pk2(u[8 * t + 2], u[8 * t + 3]), pk2(u[8 * t + 4], u[8 * t + 5]), pk2(u[8 * t + 6], u[8 * t + 7])}, ws, lane * 16,
                            WS_U + L * 0x200000 + (d32 * 112 + f32t * 2 + t) * 1024);
                flag32(ws, WS_F4 + ((L * 16 + d32) * 2 + (f32t >= 28)) * 128 + (f32t % 28) * 4, L + 1, lane);
                STAMP(16);
            }
            {
                PHASE_IDS
                const int kh = w & 1;
                const float bias = __uint_as_float((unsigned)b2[own_s32 * 32 + l31] << 16);
                wait32(ws, (l31 < 28 ? l31 : 0) * 4, WS_F4 + ((L * 16 + own_d32) * 2 + kh) * 128, L + 1);
                STAMP(17);
                gemm_compute<14, 4, true, true, 16>(lds, lane, kh, ws, WS_U + L * 0x200000 + (own_d32 * 112 + kh * 56) * 1024, acc0, acc1);
                STAMP(18);
                exchange(lds, lane, w, w ^ 1, acc0, acc1);
#pragma unroll
                for (int i = 0; i < 8; ++i) xres[i] = pk2(bf_lo(xres[i]) + (acc0[2 * i] + bias), bf_hi(xres[i]) + (acc0[2 * i + 1] + bias));
                if (L == 0) {
#pragma unroll
                    for (int t = 0; t < 2; ++t)
                        st_frag(u32x4{xres[4 * t], xres[4 * t + 1], xres[4 * t + 2], xres[4 * t + 3]}, ws, lane * 16, WS_X2 + (own_s32 * 32 + own_d32 * 2 + t) * 1024);
                    flag32(ws, WS_F5 + own_s32 * 128 + own_d32 * 4, 1u, lane);
                } else {
                    char* op = (char*)out + (own_s32 * 32 + l31) * 1024 + own_d32 * 64 + hh * 16;
#pragma unroll
                    for (int t = 0; t < 2; ++t) {
                        const auto s0 = __builtin_amdgcn_permlane32_swap(xres[4 * t], xres[4 * t + 2], false, false);
                        const auto s1 = __builtin_amdgcn_permlane32_swap(xres[4 * t + 1], xres[4 * t + 3], false, false);
                        *(u32x4*)(op + t * 32) = u32x4{s0[0], s1[0], s0[1], s1[1]};
                    }
                }
                STAMP(19);
            }
        }
    }
}

extern "C" void kernel_launch(void* const* d_in, const int* in_sizes, int n_in, void* d_out, int out_size, void* d_ws, size_t ws_size,
                              hipStream_t stream) {
    fused_fwd<<<256, 512, 0, stream>>>(d_in[0], d_in[1], d_in[2], d_in[4], (const unsigned short*)d_in[5], d_in[6], (const unsigned short*)d_in[7],
                                       (unsigned short*)d_out, (char*)d_ws);
}
```

```cpp
#include <hip/hip_runtime.h>
#include <hip/hip_bf16.h>
#include <stdint.h>

#define WARM1 8
#define WARM2 4
#define AN0 6
#define APD 12
#define XN0 6
#define XPD 12
#define CPRIO 2
#define APRIO 1
#define NTLD 0
#define STAMPS 0

typedef __attribute__((ext_vector_type(16))) float f32x16;
typedef __attribute__((ext_vector_type(8))) float f32x8;
typedef __attribute__((ext_vector_type(4))) float f32x4;
typedef __attribute__((ext_vector_type(2))) float f32x2;
typedef __attribute__((ext_vector_type(16))) __bf16 bf16x16;
typedef __attribute__((ext_vector_type(8))) __bf16 bf16x8;
typedef __attribute__((ext_vector_type(2))) __bf16 bf16x2;
typedef __attribute__((ext_vector_type(4))) unsigned u32x4;
typedef __attribute__((ext_vector_type(2))) unsigned u32x2;
typedef __attribute__((ext_vector_type(4))) short s16x4;
typedef __amdgpu_buffer_rsrc_t rsrc_t;
#define LAS __attribute__((address_space(3)))
typedef LAS char* lptr;
#define DI __device__ __forceinline__

constexpr unsigned WS_NONCE = 0x0;
constexpr unsigned WS_F0 = 0x1000;
constexpr unsigned WS_F5 = 0x2000;
constexpr unsigned WS_C1 = 0x10000;
constexpr unsigned WS_C2 = 0x20000;
constexpr unsigned WS_C3 = 0x28000;
constexpr unsigned WS_C4 = 0x2C000;
constexpr unsigned WS_C5 = 0x30000;
constexpr unsigned WS_C6 = 0x34000;
constexpr unsigned WS_ST = 0x40000;
constexpr unsigned WS_SP = 0x140000;
constexpr unsigned WS_KV = 0x200000;
constexpr unsigned WS_O = 0xA00000;
constexpr unsigned WS_H2 = 0xE00000;
constexpr unsigned WS_U = 0x1200000;
constexpr unsigned WS_X2 = 0x1600000;
constexpr unsigned WS_DBG = 0x1800000;
constexpr unsigned WS_END = 0x1A00000;
constexpr unsigned L_WIMG = 16384;
constexpr unsigned L_ONES = 131072;
constexpr unsigned L_CSP = 135168;
constexpr unsigned L_CSF = L_CSP + 1536;
constexpr unsigned L_PROG = L_CSP + 1792;
constexpr unsigned L_FILL = L_PROG + 16;
constexpr unsigned L_SST = L_CSP + 2048;
constexpr unsigned L_REG = 139264;
constexpr unsigned L_SCR = 147456;
constexpr unsigned L_TOTAL = 163840;
constexpr int AUX_SC1 = 16;
constexpr unsigned OOB = 0x80000000u;
constexpr unsigned SPIN_CAP = 1u << 16;
constexpr float LN_EPS = 1e-5f;

#define SBAR() __builtin_amdgcn_sched_barrier(0)
#define PIN(x) asm volatile("" : "+v"(x))
#define MFMA(a, b, c) __builtin_amdgcn_mfma_f32_32x32x16_bf16(a, b, c, 0, 0, 0)
#define EX(x) __builtin_amdgcn_exp2f(x)
#define VMWAIT(n) asm volatile("s_waitcnt vmcnt(" #n ")" ::: "memory")
#define BAR() asm volatile("s_waitcnt lgkmcnt(0)\n\ts_barrier" ::: "memory")

template <int N> DI void vm_wait() { asm volatile("s_waitcnt vmcnt(%0)" ::"i"(N) : "memory"); }
template <int N> DI void vm_wait_bar() { asm volatile("s_waitcnt vmcnt(%0) lgkmcnt(0)\n\ts_barrier" ::"i"(N) : "memory"); }
DI rsrc_t mk_rsrc(const void* p, unsigned bytes) { asm volatile("" : "+s"(p)); return __builtin_amdgcn_make_buffer_rsrc((void*)p, 0, bytes, 0x00020000); }
template <int N, int LSTEP, int GSTEP> DI void dma(unsigned lds, unsigned voff, rsrc_t rs, unsigned soff) {
    unsigned t;
    asm volatile("s_mov_b32 m0, %1\n\ts_mov_b32 %0, %4\n\ts_nop 2\n\tbuffer_load_dwordx4 %2, %3, %0 offen lds\n\t"
                 ".rept %5\n\ts_add_u32 m0, m0, %6\n\ts_add_u32 %0, %0, %7\n\tbuffer_load_dwordx4 %2, %3, %0 offen lds\n\t.endr"
                 : "=&s"(t) : "s"(lds), "v"(voff), "s"(rs), "s"(soff), "i"(N - 1), "i"(LSTEP), "i"(GSTEP) : "memory", "scc");
}
#define NTS ""
template <int N, int LSTEP, int GSTEP> DI void dma_sc1(unsigned lds, unsigned voff, rsrc_t rs, unsigned soff) {
    unsigned t;
    asm volatile("s_mov_b32 m0, %1\n\ts_mov_b32 %0, %4\n\ts_nop 2\n\tbuffer_load_dwordx4 %2, %3, %0 offen sc1 " NTS " lds\n\t"
                 ".rept %5\n\ts_add_u32 m0, m0, %6\n\ts_add_u32 %0, %0, %7\n\tbuffer_load_dwordx4 %2, %3, %0 offen sc1 " NTS " lds\n\t.endr"
                 : "=&s"(t) : "s"(lds), "v"(voff), "s"(rs), "s"(soff), "i"(N - 1), "i"(LSTEP), "i"(GSTEP) : "memory", "scc");
}
DI bf16x8 ld_frag(rsrc_t rs, unsigned voff, unsigned soff) { return __builtin_bit_cast(bf16x8, __builtin_amdgcn_raw_buffer_load_b128(rs, voff, soff, AUX_SC1 | (NTLD ? 2 : 0))); }
DI void st_frag(u32x4 v, rsrc_t rs, unsigned voff, unsigned soff) { __builtin_amdgcn_raw_buffer_store_b128(v, rs, voff, soff, AUX_SC1); }
DI bf16x8 lds_frag(lptr p) { return *(LAS const bf16x8*)p; }
DI s16x4 lds_tr(lptr p) { return __builtin_amdgcn_ds_read_tr16_b64_v4i16((LAS s16x4*)p); }
DI bf16x8 tr_frag(lptr p0, lptr p1) {
    const s16x4 a = lds_tr(p0), b = lds_tr(p1);
    return __builtin_bit_cast(bf16x8, __builtin_shufflevector(a, b, 0, 1, 2, 3, 4, 5, 6, 7));
}
DI unsigned pk2(float a, float b) { const f32x2 v = {a, b}; return __builtin_bit_cast(unsigned, __builtin_convertvector(v, bf16x2)); }
DI float bf_lo(unsigned u) { return __uint_as_float(u << 16); }
DI float bf_hi(unsigned u) { return __uint_as_float(u & 0xffff0000u); }
DI float dot2(unsigned a, unsigned b, float c) { return __builtin_amdgcn_fdot2_f32_bf16(__builtin_bit_cast(bf16x2, a), __builtin_bit_cast(bf16x2, b), c, false); }
DI float half_sum2(float a, float b) { const auto r = __builtin_amdgcn_permlane32_swap(__float_as_uint(a), __float_as_uint(b), false, false); return __uint_as_float(r[0]) + __uint_as_float(r[1]); }
DI f32x8 lo8(f32x16 v) { return __builtin_shufflevector(v, v, 0, 1, 2, 3, 4, 5, 6, 7); }
DI f32x8 hi8(f32x16 v) { return __builtin_shufflevector(v, v, 8, 9, 10, 11, 12, 13, 14, 15); }
DI f32x16 smf(bf16x8 a, f32x8 p, f32x16 c, int idx) { return __builtin_amdgcn_smfmac_f32_32x32x32_bf16(a, __builtin_bit_cast(bf16x16, p), c, idx, 0, 0); }
DI int opaque(int v) { asm volatile("" : "+v"(v)); return v; }
DI int sopaque(int v) { asm volatile("" : "+s"(v)); return v; }

#define PSLEEP 0
#define POLL_PAUSE() do { if (PSLEEP) __builtin_amdgcn_s_sleep(PSLEEP); asm volatile("" ::: "memory"); } while (0)
DI unsigned wait_cnt(rsrc_t ws, unsigned soff, unsigned target, int lane) {
    unsigned i;
#pragma unroll 1
    for (i = 0; i < SPIN_CAP; ++i) {
        const unsigned v = __builtin_amdgcn_raw_ptr_buffer_atomic_add_i32(0, ws, lane == 0 ? 0u : OOB, soff, AUX_SC1);
        if ((unsigned)__builtin_amdgcn_readfirstlane(v) >= target) break;
        POLL_PAUSE();
    }
    asm volatile("" ::: "memory");
    return i;
}
template <int NREP> DI void arrive(rsrc_t ws, unsigned soff, int lane) {
    __builtin_amdgcn_raw_ptr_buffer_atomic_add_i32(1, ws, lane < NREP ? lane * 128u : OOB, soff, AUX_SC1);
    asm volatile("" ::: "memory");
}
template <int NREP> DI void publish(rsrc_t ws, unsigned soff, int lane) { VMWAIT(0); arrive<NREP>(ws, soff, lane); }
DI unsigned wait64(rsrc_t ws, unsigned voff, unsigned soff, unsigned tlo, unsigned thi) {
    unsigned i;
#pragma unroll 1
    for (i = 0; i < SPIN_CAP; ++i) {
        const u32x2 v = __builtin_amdgcn_raw_buffer_load_b64(ws, voff, soff, AUX_SC1);
        if (__all(((v[0] ^ tlo) | (v[1] ^ thi)) == 0)) break;
        POLL_PAUSE();
    }
    asm volatile("" ::: "memory");
    return i;
}

#define ISTAMP(i) do { } while (0)
#define NOTE(i, v) (void)(v)
#define STAMP(i) do { } while (0)

DI unsigned wvoff(int lane, unsigned ldw) { return (lane >> 3) * ldw + (((lane & 7) ^ (((lane >> 4) & 1) << 2)) << 4); }
DI void lds_post(lptr p, unsigned v) { *(volatile LAS unsigned*)p = v; }
DI unsigned lds_min2(lptr p) {
    const u32x2 v = *(volatile LAS u32x2*)p;
    const unsigned a = __builtin_amdgcn_readfirstlane(v[0]), b = __builtin_amdgcn_readfirstlane(v[1]);
    return a < b ? a : b;
}
template <int SLEEP> DI void lds_need(lptr p, unsigned& have, unsigned need) {
#pragma unroll 1
    for (unsigned i = 0; have < need && i < (SPIN_CAP << 4); ++i) { have = lds_min2(p); if (SLEEP && have < need) __builtin_amdgcn_s_sleep(SLEEP); }
    asm volatile("" ::: "memory");
}
template <int S0, int S1, int SLOT0, int LDW> DI void ring_prefetch(unsigned lb, u32x4 (&rg)[8][4], rsrc_t rw, unsigned voff, unsigned gb, lptr fill, unsigned base) {
#pragma unroll
    for (int s = S0; s < S1; ++s) {
        const unsigned so = gb + s * (64 * LDW);
#pragma unroll
        for (int h = 0; h < 2; ++h) {
            if (s < 8) dma<2, 1024, 8 * LDW>(lb + ((s + SLOT0) & 7) * 16384 + h * 2048, voff, rw, so + h * 16 * LDW);
            else {
#pragma unroll
                for (int p = 2 * h; p < 2 * h + 2; ++p)
                    asm volatile("s_nop 4\n\tbuffer_load_dwordx4 %0, %1, %2, %3 offen" : "=v"(rg[s & 7][p]) : "v"(voff), "s"(rw), "s"(so + p * 8 * LDW) : "memory");
            }
            vm_wait<4>();
        }
        if (s >= 1 && s <= 8) lds_post(fill, base + s);
    }
}
template <int NST, int SLOT0> DI void ring_refill(lptr lp, u32x4 (&rg)[8][4], lptr prog, lptr fill, unsigned base) {
    unsigned have = 0;
#pragma unroll
    for (int s = 8; s < NST; ++s) {
        lds_need<1>(prog, have, base + s - 7);
        if (s == NST - 1) asm volatile("s_waitcnt vmcnt(0)" : "+v"(rg[s & 7][0]), "+v"(rg[s & 7][1]), "+v"(rg[s & 7][2]), "+v"(rg[s & 7][3]) :: "memory");
        else asm volatile("" : "+v"(rg[s & 7][0]), "+v"(rg[s & 7][1]), "+v"(rg[s & 7][2]), "+v"(rg[s & 7][3]) :: "memory");
#pragma unroll
        for (int p = 0; p < 4; ++p) *(LAS u32x4*)(lp + ((s + SLOT0) & 7) * 16384 + p * 1024) = rg[s & 7][p];
        asm volatile("" ::: "memory");
        lds_post(fill, base + s + 1);
    }
}
template <int NST, int SLOT0, bool ACT_A, bool RING>
DI void gemm_compute(lptr lds, int lane, int kh, int ah, rsrc_t ws, unsigned act_soff, unsigned base, f32x16& acc0, f32x16& acc1, unsigned* istv = nullptr, int ib = 0) {
    constexpr int NK = NST * 4, LA = 2;
    const unsigned av = lane * 16;
    ISTAMP(ib);
    bf16x8 f[NK], w0[NK], w1[NK];
#define ALOAD(i) f[i] = ld_frag(ws, av + ((i) & 3) * 1024, act_soff + ((i) >> 2) * 4096)
#pragma unroll
    for (int i = 0; i < AN0; ++i) ALOAD(i);
    SBAR();
    const int hh = lane >> 5, gq = (lane >> 4) & 1, q = (lane >> 2) & 3, p = lane & 3;
    const unsigned a0 = kh * 8192 + (4 * hh + q) * 128 + (((4 * kh + 2 * gq + (p >> 1)) ^ (((q >> 1) & 1) << 2)) << 4) + (p & 1) * 8;
    const lptr b0 = lds + a0, b1 = lds + (a0 ^ 64), b0h = b0 + 65536, b1h = b1 + 65536;
    const lptr fill = lds + L_FILL + kh * 8, prog = lds + L_PROG + kh * 8 + ah * 4;
    unsigned have = 0;
#define WREAD(dst, pl, ph, k) do { const int sl_ = (((k) >> 2) + SLOT0) & 7, o_ = (sl_ & 3) * 16384 + ((k) & 3) * 2048; \
        const lptr p_ = (sl_ & 4) ? ph : pl; dst[k] = tr_frag(p_ + o_, p_ + o_ + 1024); } while (0)
    acc0 = f32x16{}; acc1 = f32x16{};
    ISTAMP(ib + 1);
    if (RING) lds_need<0>(fill, have, base + 1);
    ISTAMP(ib + 2);
#pragma unroll
    for (int k = 0; k < LA; ++k) { WREAD(w1, b1, b1h, k); WREAD(w0, b0, b0h, k); }
    int nl = AN0;
#pragma unroll
    for (int k = 0; k < NK; ++k) {
        if (RING && ((k + LA) & 3) == 0 && k + LA < NK) lds_need<0>(fill, have, base + ((k + LA) >> 2) + 1);
        SBAR();
        acc1 = ACT_A ? MFMA(f[k], w1[k], acc1) : MFMA(w1[k], f[k], acc1);
        SBAR();
        if (k + LA < NK) WREAD(w1, b1, b1h, k + LA);
        if (nl < NK && nl < k + APD) { ALOAD(nl); ++nl; }
        SBAR();
        acc0 = ACT_A ? MFMA(f[k], w0[k], acc0) : MFMA(w0[k], f[k], acc0);
        SBAR();
        if (RING && (k & 3) == 3 && (k >> 2) + 8 < NST) lds_post(prog, base + (k >> 2) + 1);
        if (k + LA < NK) WREAD(w0, b0, b0h, k + LA);
        if (nl < NK && nl < k + 1 + APD) { ALOAD(nl); ++nl; }
        if (k == 0) ISTAMP(ib + 3);
        if ((k & 7) == 7) ISTAMP(ib + 4 + (k >> 3));
    }
    SBAR();
#undef WREAD
#undef ALOAD
}
DI void exchange(lptr lds, int lane, int w, int partner, f32x16& acc0, const f32x16& acc1) {
    LAS f32x4* dst = (LAS f32x4*)(lds + L_SCR + w * 4096 + lane * 16);
#pragma unroll
    for (int i = 0; i < 4; ++i) dst[i * 64] = f32x4{acc1[4 * i], acc1[4 * i + 1], acc1[4 * i + 2], acc1[4 * i + 3]};
    BAR();
    LAS const f32x4* src = (LAS const f32x4*)(lds + L_SCR + partner * 4096 + lane * 16);
#pragma unroll
    for (int i = 0; i < 4; ++i) { const f32x4 v = src[i * 64]; acc0[4 * i] += v[0]; acc0[4 * i + 1] += v[1]; acc0[4 * i + 2] += v[2]; acc0[4 * i + 3] += v[3]; }
}

struct QkvAcc { f32x16 qk, v; };
struct WFrag { bf16x8 qk, v; };
DI void qkv_step(QkvAcc& a, bf16x8 x, const WFrag& f, WFrag& n, bool more, lptr pqk, lptr pv, int o0, int o1) {
    SBAR(); a.qk = MFMA(f.qk, x, a.qk); SBAR();
    if (more) n.qk = tr_frag(pqk + o0, pqk + o1);
    SBAR(); a.v = MFMA(x, f.v, a.v); SBAR();
    if (more) n.v = tr_frag(pv + o0, pv + o1);
}
DI u32x2 row_partial(const unsigned (&x)[8]) {
    float sum = 0.f, sq = 0.f;
#pragma unroll
    for (int i = 0; i < 8; ++i) { const float a = bf_lo(x[i]), b = bf_hi(x[i]); sum += a + b; sq = __builtin_fmaf(a, a, __builtin_fmaf(b, b, sq)); }
    const unsigned t = __float_as_uint(half_sum2(sum, sq));
    const auto sw = __builtin_amdgcn_permlane32_swap(t, t, false, false);
    return u32x2{sw[0], sw[1]};
}
DI f32x4 record_total(const u32x4 (&p)[4]) {
    f32x4 t;
#pragma unroll
    for (int j = 0; j < 4; ++j) {
        const unsigned v = __float_as_uint((__uint_as_float(p[0][j]) + __uint_as_float(p[1][j])) + (__uint_as_float(p[2][j]) + __uint_as_float(p[3][j])));
        const auto a = __builtin_amdgcn_permlane32_swap(v, v, false, false);
        const unsigned u = __float_as_uint(__uint_as_float(a[0]) + __uint_as_float(a[1]));
        const auto b = __builtin_amdgcn_permlane16_swap(u, u, false, false);
        t[j] = __uint_as_float(b[0]) + __uint_as_float(b[1]);
    }
    return t;
}
DI void colsum_partial(lptr lds, int lane, int w) {
    const int hh = lane >> 5, gq = (lane >> 4) & 1, q = (lane >> 2) & 3, p = lane & 3;
    const lptr pb = lds + L_WIMG + w * 6144 + hh * 768 + q * 32 + p * 8;
    float cq = 0.f, cv = 0.f;
#pragma unroll
    for (int t = 0; t < 4; ++t) {
        const u32x4 a = __builtin_bit_cast(u32x4, tr_frag(pb + gq * 128 + t * 1536, pb + gq * 128 + t * 1536 + 384));
        const u32x4 b = __builtin_bit_cast(u32x4, tr_frag(pb + 256 + t * 1536, pb + 256 + t * 1536 + 384));
#pragma unroll
        for (int i = 0; i < 4; ++i) { cq = dot2(a[i], 0x3f803f80u, cq); cv = dot2(b[i], 0x3f803f80u, cv); }
    }
    const float c = half_sum2(cq, cv);
    *(LAS float*)(lds + L_CSP + (lane < 48 ? lane : lane - 16) * 32 + w * 4) = c;
}
DI void colsum_final(lptr lds, int lane) {
    const int n = lane < 48 ? lane : lane - 16;
    const f32x4 a = *(LAS const f32x4*)(lds + L_CSP + n * 32), b = *(LAS const f32x4*)(lds + L_CSP + n * 32 + 16);
    *(LAS float*)(lds + L_CSF + n * 4) = ((a[0] + a[1]) + (a[2] + a[3])) + ((b[0] + b[1]) + (b[2] + b[3]));
}

#define E1(C, i) C[i] = EX(C[i]);
#define ATT_TILE(KOFF, VOFF) do { \
        E1(sA, 1) E1(sA, 2) E1(sA, 3) E1(sA, 4) E1(sA, 5) PIN(sA); SBAR(); \
        sB = MFMA(kf, qf2, cneg2); SBAR(); kf = lds_frag(kp + (KOFF)); \
        E1(sA, 6) E1(sA, 7) E1(sA, 8) E1(sA, 9) PIN(sA); SBAR(); \
        o = smf(v0, lo8(sA), o, idx); SBAR(); \
        E1(sA, 10) E1(sA, 11) E1(sA, 12) E1(sA, 13) E1(sA, 14) E1(sA, 15) E1(sB, 0) PIN(sA); PIN(sB); SBAR(); \
        o = smf(v1, hi8(sA), o, idx); SBAR(); \
        E1(sB, 1) E1(sB, 2) E1(sB, 3) E1(sB, 4) E1(sB, 5) PIN(sB); SBAR(); \
        sA = MFMA(kf, qf, cneg); SBAR(); \
        E1(sB, 6) E1(sB, 7) E1(sB, 8) E1(sB, 9) PIN(sB); SBAR(); \
        o2 = smf(v0, lo8(sB), o2, idx); SBAR(); v0 = lds_frag(vp + (VOFF)); \
        E1(sB, 10) E1(sB, 11) E1(sB, 12) E1(sB, 13) E1(sB, 14) E1(sB, 15) E1(sA, 0) PIN(sA); PIN(sB); SBAR(); \
        o2 = smf(v1, hi8(sB), o2, idx); SBAR(); v1 = lds_frag(vp + (VOFF) + 512); \
    } while (0)
DI f32x16 neg_row_max(f32x16 s) {
    float m = __builtin_fmaxf(s[0], s[1]);
#pragma unroll
    for (int i = 2; i < 16; i += 2) m = __builtin_fmaxf(__builtin_fmaxf(m, s[i]), s[i + 1]);
    const auto sw = __builtin_amdgcn_permlane32_swap(__float_as_uint(m), __float_as_uint(m), false, false);
    m = -__builtin_fmaxf(__uint_as_float(sw[0]), __uint_as_float(sw[1]));
    f32x16 c;
#pragma unroll
    for (int i = 0; i < 16; ++i) c[i] = m;
    PIN(c);
    return c;
}

__global__ __launch_bounds__(512, 2) void fused_fwd(const void* __restrict__ xin, const void* __restrict__ wqkv, const void* __restrict__ wo,
                                                    const void* __restrict__ w1, const unsigned short* __restrict__ b1, const void* __restrict__ w2,
                                                    const unsigned short* __restrict__ b2, unsigned short* __restrict__ out, char* wsp) {
    __shared__ __attribute__((aligned(16))) char lds_raw[L_TOTAL];
    const lptr lds = (lptr)lds_raw;
    const unsigned lds0 = (unsigned)(uintptr_t)lds;
    const int w_ = __builtin_amdgcn_readfirstlane(threadIdx.x >> 6);
#define PHASE_IDS \
    const int lane = opaque(threadIdx.x & 63), l31 = lane & 31, hh = lane >> 5; \
    const int w = sopaque(w_), bid = sopaque(blockIdx.x), g = bid & 7, r = bid >> 3; \
    const int st64 = g * 4 + (r >> 3), dt64 = r & 7; \
    const int dt1 = (g & 1) * 4 + (r & 3), ft1 = (g >> 1) * 7 + (r >> 2); \
    const int own_s32 = st64 * 2 + (w & 1), own_d32 = dt64 * 2 + ((w >> 1) & 1); \
    (void)l31; (void)hh; (void)dt1; (void)ft1; (void)own_s32; (void)own_d32;
    const bool w1_on = (blockIdx.x >> 3) < 28;
    const rsrc_t ws = mk_rsrc(wsp, WS_END);

#define ISTARG(b)
    unsigned xres[8] = {};
    unsigned tag_lo = 0, tag_hi = 0;
    u32x2 nonce = {};
    u32x4 wc[12];
#define UNDEF_WC() do { _Pragma("unroll") for (int i_ = 0; i_ < 12; ++i_) asm volatile("" : "=v"(wc[i_])); } while (0)
    UNDEF_WC();

#pragma unroll 1
    for (int L = 0; L < 2; ++L) {
        bf16x8 qf;
        {
            PHASE_IDS
            const int gq = (lane >> 4) & 1, q = (lane >> 2) & 3, p = lane & 3;
            const unsigned kv_soff = WS_KV + (L * 32 + r) * 131072;
            STAMP(0);
            QkvAcc a; a.qk = f32x16{}; a.v = f32x16{};
            u32x4 sp[4];
            float rstd, mr, csv;
            f32x4 cs[4], rs[4], ms[4];
#define LN_PREP() do { \
                const f32x4 tot = record_total(sp); \
                const float m0 = tot[0] * (1.f / 512.f), m1 = tot[2] * (1.f / 512.f); \
                const float r0 = __builtin_amdgcn_rsqf(__builtin_fmaxf(tot[1] * (1.f / 512.f) - m0 * m0, 0.f) + LN_EPS); \
                const float r1 = __builtin_amdgcn_rsqf(__builtin_fmaxf(tot[3] * (1.f / 512.f) - m1 * m1, 0.f) + LN_EPS); \
                const lptr sst = lds + L_SST + w * 256; \
                *(LAS f32x2*)(sst + hh * 128 + (lane & 15) * 8) = hh ? f32x2{m0 * r0, m1 * r1} : f32x2{r0, r1}; \
                rstd = *(LAS const float*)(sst + l31 * 4); mr = *(LAS const float*)(sst + 128 + l31 * 4); \
                _Pragma("unroll") for (int i = 0; i < 4; ++i) { \
                    cs[i] = *(LAS const f32x4*)(lds + L_CSF + (8 * i + 4 * hh) * 4); \
                    rs[i] = *(LAS const f32x4*)(sst + (8 * i + 4 * hh) * 4); ms[i] = *(LAS const f32x4*)(sst + 128 + (8 * i + 4 * hh) * 4); } \
                csv = *(LAS const float*)(lds + L_CSF + 128 + (lane & 15) * 4); \
                SBAR(); } while (0)
            const unsigned sp_soff = WS_SP + (L * 64 + g * 8 + w) * 4096;
            if (L == 0) {
                if (w >= 4) {
                    const rsrc_t rw = mk_rsrc(wqkv, 512 * 3072);
#pragma unroll
                    for (int ty = 0; ty < 3; ++ty) {
                        const int o = ty * 1024 + lane * 16, rg = o / 384, rem = o % 384;
                        const unsigned voff = (4 * rg + ((rem >> 5) & 3)) * 3072 + (rem >> 7) * 1024 + (rem & 16);
                        dma<4, 3072, 98304>(lds0 + L_WIMG + (w - 4) * 12288 + ty * 1024, voff, rw, (w - 4) * 393216 + r * 32);
                    }
                }
                const rsrc_t rx = mk_rsrc(xin, 2048 * 1024);
                nonce = __builtin_amdgcn_raw_buffer_load_b64(ws, 0, WS_NONCE, AUX_SC1);
                if (w < 4) {
#pragma unroll
                    for (int i = 0; i < 4; ++i) {
                        const u32x2 v = __builtin_amdgcn_raw_buffer_load_b64(rx, l31 * 1024 + hh * 8, own_s32 * 32768 + own_d32 * 64 + i * 16, 0);
                        xres[2 * i] = v[0]; xres[2 * i + 1] = v[1];
                    }
                }
                const unsigned tokb = (g * 256 + w * 32) * 1024, stg = w < 5 ? 65536 + w * 12288 : (w == 5 ? 0 : L_REG + (w - 6) * 12288);
                const unsigned vA = (lane >> 3) * 1024 + (((lane & 7) ^ (lane >> 4)) << 4), vB = vA ^ 64;
#define XCHUNK(c) do { dma<2, 2048, 16384>(lds0 + stg + ((c) % 3) * 4096, vA, rx, tokb + (c) * 128); \
                       dma<2, 2048, 16384>(lds0 + stg + ((c) % 3) * 4096 + 1024, vB, rx, tokb + 8192 + (c) * 128); } while (0)
                XCHUNK(0); XCHUNK(1); XCHUNK(2);
                vm_wait<12>();
                {
                    unsigned long long z = ((unsigned long long)__builtin_amdgcn_readfirstlane(nonce[1]) << 32) | (unsigned)__builtin_amdgcn_readfirstlane(nonce[0]);
                    z = z * 0x9E3779B97F4A7C15ull + 0x632BE59BD9B4E019ull;
                    z ^= z >> 29; z *= 0xBF58476D1CE4E5B9ull; z ^= z >> 32;
                    tag_lo = (unsigned)z; tag_hi = (unsigned)(z >> 32);
                }
                if (w < 4) {
                    __builtin_amdgcn_raw_buffer_store_b64(row_partial(xres), ws, hh ? OOB : l31 * 8u, WS_SP + (own_s32 * 16 + own_d32) * 256, AUX_SC1);
                    __builtin_amdgcn_raw_buffer_store_b64(u32x2{}, ws, l31 * 8 + hh * 0x40000, WS_ST + (own_s32 * 16 + own_d32) * 256, AUX_SC1);
                    const unsigned c1a = opaque(WS_C1 + (r * 8 + g) * 128 + lane * 32768), c3a = opaque(WS_C3 + bid * 256 + (lane - 3) * 128);
                    const unsigned c2a = sopaque(WS_C2 + (((r >> 4) * 64 + g * 8) * 2 + (r & 15)) * 128);
                    const unsigned ca = lane < 2 ? c1a : (lane == 2 ? c2a : c3a);
                    __builtin_amdgcn_raw_buffer_store_b32(0u, ws, (w == 0 && lane < 5) ? ca : OOB, 0, AUX_SC1);
                    VMWAIT(0);
                    const unsigned f5a = sopaque(WS_F5 + own_s32 * 128 + own_d32 * 8), f0a = sopaque(w == 0 ? WS_F0 + r * 128 + g * 8 : OOB);
                    __builtin_amdgcn_raw_buffer_store_b64(u32x2{tag_lo, tag_hi}, ws, lane == 0 ? f5a : (lane == 1 ? f0a : OOB), 0, AUX_SC1);
                }
                BAR();
                STAMP(1);
                if (w >= 4) {
#pragma unroll
                    for (int i = 0; i < 12; ++i) wc[i] = *(LAS const u32x4*)(lds + L_WIMG + (w - 4) * 12288 + i * 1024 + lane * 16);
                }
                colsum_partial(lds, lane, w);
                const lptr pqk = lds + L_WIMG + hh * 768 + gq * 128 + q * 32 + p * 8, pv = lds + L_WIMG + hh * 768 + 256 + q * 32 + p * 8;
                lptr px[4];
#pragma unroll
                for (int t = 0; t < 4; ++t) px[t] = lds + stg + l31 * 128 + (((2 * t + hh) ^ ((l31 >> 1) & 7)) << 4);
                WFrag wf[2];
                wf[0].qk = tr_frag(pqk, pqk + 384); wf[0].v = tr_frag(pv, pv + 384);
#pragma unroll
                for (int c = 0; c < 8; ++c) {
                    if (c <= 5) vm_wait<8>(); else if (c == 6) vm_wait<4>(); else vm_wait<0>();
                    bf16x8 xf[4];
#pragma unroll
                    for (int t = 0; t < 4; ++t) xf[t] = lds_frag(px[t] + (c % 3) * 4096);
                    if (c + 3 < 8) { asm volatile("s_waitcnt lgkmcnt(0)" : "+v"(xf[0]), "+v"(xf[1]), "+v"(xf[2]), "+v"(xf[3]) :: "memory"); XCHUNK(c + 3); }
                    if (c == 7) {
                        const unsigned f0a = opaque(WS_F0 + r * 128 + (lane & 7) * 8), f5a = opaque(WS_F5 + (g * 8 + w) * 128 + (lane & 15) * 8);
                        wait64(ws, hh ? f0a : f5a, 0, tag_lo, tag_hi);
#pragma unroll
                        for (int i = 0; i < 4; ++i) sp[i] = __builtin_amdgcn_raw_buffer_load_b128(ws, lane * 16 + i * 1024, sp_soff, AUX_SC1);
                        SBAR();
                    }
#pragma unroll
                    for (int t = 0; t < 4; ++t) {
                        const int k = 4 * c + t;
                        qkv_step(a, xf[t], wf[k & 1], wf[(k + 1) & 1], k < 31, pqk, pv, (k + 1) * 1536, (k + 1) * 1536 + 384);
                    }
                    if (c == 1) { BAR(); if (w == 0) colsum_final(lds, lane); }
                    if (c == 3) BAR();
                }
                LN_PREP();
#undef XCHUNK
            } else {
                if (w >= 4) {
#pragma unroll
                    for (int i = 0; i < 12; ++i) *(LAS u32x4*)(lds + L_WIMG + (w - 4) * 12288 + i * 1024 + lane * 16) = wc[i];
                }
                BAR();
                wait_cnt(ws, WS_C5 + ((g * 8 + w) * 2 + (r & 1)) * 128, 16, lane);
                STAMP(1);
                ISTAMP(32);
                bf16x8 xf[32];
                const unsigned x_soff = WS_X2 + (g * 8 + w) * 32768;
#define XLOAD(k) xf[k] = ld_frag(ws, lane * 16 + ((k) & 3) * 1024, x_soff + ((k) >> 2) * 4096)
#pragma unroll
                for (int i = 0; i < 4; ++i) sp[i] = __builtin_amdgcn_raw_buffer_load_b128(ws, lane * 16 + i * 1024, sp_soff, AUX_SC1);
#pragma unroll
                for (int k = 0; k < XN0; ++k) XLOAD(k);
                SBAR();
                ISTAMP(33);
                LN_PREP();
                ISTAMP(34);
                const lptr pv = lds + L_WIMG + hh * 384 + q * 32 + p * 8 + 256, pqk = pv + gq * 128 - 256;
                WFrag wf[2];
                wf[0].qk = tr_frag(pqk, pqk + 768); wf[0].v = tr_frag(pv, pv + 768);
                int nl = XN0;
#pragma unroll
                for (int k = 0; k < 32; ++k) {
                    qkv_step(a, xf[k], wf[k & 1], wf[(k + 1) & 1], k < 31, pqk, pv, (k + 1) * 1536, (k + 1) * 1536 + 768);
                    if (nl < 32 && nl < k + XPD) { XLOAD(nl); ++nl; }
                    if (nl < 32 && nl < k + 1 + XPD) { XLOAD(nl); ++nl; }
                    if (k == 0) ISTAMP(35);
                    if ((k & 3) == 3) ISTAMP(36 + (k >> 2));
                }
#undef XLOAD
                UNDEF_WC();
            }
            STAMP(2);
            float qk[16], vv[16];
#pragma unroll
            for (int i = 0; i < 16; ++i) qk[i] = a.qk[i] * rstd - mr * cs[i >> 2][i & 3];
            const u32x4 kpk = {pk2(qk[8], qk[9]), pk2(qk[10], qk[11]), pk2(qk[12], qk[13]), pk2(qk[14], qk[15])};
            const unsigned koff = (hh * 32 + ((l31 & 19) | ((l31 & 4) << 1) | ((l31 & 8) >> 1))) * 16;
            st_frag(kpk, ws, koff, kv_soff + (g * 8 + w) * 2048);
#pragma unroll
            for (int i = 0; i < 16; ++i) vv[i] = a.v[i] * rs[i >> 2][i & 3] - ms[i >> 2][i & 3] * csv;
            u32x4 vpk;
#pragma unroll
            for (int i = 0; i < 4; ++i) { const unsigned lo = pk2(vv[2 * i], vv[2 * i + 1]), hi = pk2(vv[8 + 2 * i], vv[9 + 2 * i]); vpk[i] = gq ? hi : lo; }
            const unsigned voff = gq * 512 + hh * 256 + (lane & 15) * 16;
            st_frag(vpk, ws, voff, kv_soff + (g * 8 + w) * 2048 + 1024);
            constexpr float QS = 0.25f * 1.4426950408889634f;
            const u32x4 qpk = {pk2(qk[0] * QS, qk[1] * QS), pk2(qk[2] * QS, qk[3] * QS), pk2(qk[4] * QS, qk[5] * QS), pk2(qk[6] * QS, qk[7] * QS)};
            qf = __builtin_bit_cast(bf16x8, qpk);
            if (L == 0) {
                BAR();
                if (w == 0) *(LAS unsigned*)(lds + L_PROG + (lane & 7) * 4) = 0u;
                const unsigned one = (lane & 15) ? 0u : 0x3f803f80u;
                *(LAS u32x4*)(lds + L_ONES + w * 512 + hh * 256 + (lane & 15) * 16) = u32x4{one, one, one, one};
            }
            *(LAS u32x4*)(lds + w * 1024 + koff) = kpk;
            *(LAS u32x4*)(lds + 8192 + w * 1024 + voff) = vpk;
            *(LAS u32x4*)(lds + L_REG + w * 3072 + lane * 16) = qpk;
            BAR();
            STAMP(3);
        }
        {
            PHASE_IDS
            const int gq = (lane >> 4) & 1;
            const unsigned kv_soff = WS_KV + (L * 32 + r) * 131072;
            const int idx = opaque(0xDDDD);
            lptr kp = lds + (w & 1) * 4096 + lane * 16;
            lptr vp = lds + (gq ? L_ONES : 8192 + (w & 1) * 4096) + hh * 256 + (lane & 15) * 16;
            const int vinc = gq ? 0 : 16384;
            const bf16x8 qf2 = lds_frag(lds + L_REG + (w ^ 1) * 3072 + lane * 16), k0 = lds_frag(lds + lane * 16);
            bf16x8 kf = lds_frag(kp), v0 = lds_frag(vp), v1 = lds_frag(vp + 512);
            f32x16 sA = MFMA(k0, qf, f32x16{}), sB = MFMA(k0, qf2, f32x16{}), o = {}, o2 = {};
            const f32x16 cneg = neg_row_max(sA), cneg2 = neg_row_max(sB);
#define KVCHUNK(j) dma_sc1<2, 8192, 1024>(lds0 + (j) * 16384 + w * 1024, lane * 16, ws, kv_soff + (((g + (j)) & 7) * 8 + w) * 2048)
            sA = MFMA(kf, qf, cneg);
            publish<1>(ws, WS_C1 + ((L * 32 + r) * 8 + w) * 128, lane);
            sA[0] = EX(sA[0]);
#define PRIO(n) do { if (APRIO) __builtin_amdgcn_s_setprio(n); } while (0)
#pragma unroll
            for (int c = 0; c < 8; ++c) {
                if (c == 0 || c == 1 || c == 3 || c == 4 || c == 7) PRIO(2);
                ATT_TILE(1024, 1024);
                if (c == 0) {
                    STAMP(4);
                    wait_cnt(ws, WS_C1 + ((L * 32 + r) * 8 + w) * 128, 8, lane);
                    STAMP(5);
                    KVCHUNK(1);
                }
                if (c == 0 || c == 1 || c == 3 || c == 4) PRIO(1);
                ATT_TILE(2048, 2048);
                if (c == 0) { KVCHUNK(2); KVCHUNK(3); }
                if (c == 0 || c == 1 || c == 3 || c == 4) PRIO(0); else if (c == 7) PRIO(1);
                ATT_TILE(3072, 3072);
                if (c == 0) { vm_wait_bar<4>(); KVCHUNK(4); KVCHUNK(5); STAMP(6); }
                if (c == 1) { vm_wait_bar<4>(); KVCHUNK(6); KVCHUNK(7); }
                if (c == 3) vm_wait_bar<0>();
                if (c == 4) {
                    BAR();
                    dma<8, 1024, 8192>(lds0 + w * 8192, wvoff(lane, 1024), mk_rsrc(wo, 512 * 1024), ((w & 1) * 256 + (w >> 1) * 64) * 1024 + dt64 * 128);
                }
                if (c == 5 && WARM1) dma<8, 0, 8 * 3584>(lds0 + L_REG + w * 3072, wvoff(lane, 3584), mk_rsrc(w1, 2048 * 3584), ((r & 3) * 512 + w * 64) * 3584 + (w1_on ? ft1 : 0) * 128);
                if (c == 6 && WARM2) dma<4, 0, 8 * 4096>(lds0 + L_REG + w * 3072, wvoff(lane, 4096), mk_rsrc(w2, 1792 * 4096), (dt64 * 224 + (w < 7 ? w : 0) * 32) * 4096 + st64 * 128);
                if (c < 7) { if (c != 2 && c < 5) PRIO(3); vp += vinc; ATT_TILE(16384, 0); kp += 16384; }
                else { PRIO(0); ATT_TILE(0, 0); }
            }
            STAMP(7);
            VMWAIT(0);
            STAMP(20);
            {
                const lptr mine = lds + L_REG + w * 3072 + lane * 16, other = lds + L_REG + (w ^ 1) * 3072 + lane * 16;
                *(LAS f32x4*)mine = f32x4{o2[0], o2[1], o2[2], o2[3]};
                *(LAS f32x4*)(mine + 1024) = f32x4{o2[4], o2[5], o2[6], o2[7]};
                *(LAS float*)(mine + 2048) = o2[8];
                BAR();
                STAMP(21);
                const f32x4 a0 = *(LAS const f32x4*)other, a1 = *(LAS const f32x4*)(other + 1024);
                o[8] += *(LAS const float*)(other + 2048);
#pragma unroll
                for (int i = 0; i < 4; ++i) { o[i] += a0[i]; o[4 + i] += a1[i]; }
            }
            float chk = __builtin_fabsf(o[8]);
#pragma unroll
            for (int i = 0; i < 8; ++i) chk += __builtin_fabsf(o[i]);
            if (__builtin_expect(__any(!(chk < 1e36f)), 0)) {
                float m = -__builtin_inff();
                o = f32x16{};
                const u32x4 one4 = {0x3f803f80u, 0x3f803f80u, 0x3f803f80u, 0x3f803f80u};
#pragma unroll 1
                for (int t = 0; t < 64; ++t) {
                    const bf16x8 kk = ld_frag(ws, lane * 16, kv_soff + t * 2048);
                    u32x4 a0 = __builtin_amdgcn_raw_buffer_load_b128(ws, hh * 256 + (lane & 15) * 16, kv_soff + t * 2048 + 1024, AUX_SC1);
                    u32x4 a1 = __builtin_amdgcn_raw_buffer_load_b128(ws, hh * 256 + (lane & 15) * 16, kv_soff + t * 2048 + 1536, AUX_SC1);
                    a0 = gq ? one4 : a0; a1 = gq ? one4 : a1;
                    f32x16 s = MFMA(kk, qf, f32x16{});
                    float tm = m;
#pragma unroll
                    for (int i = 0; i < 16; ++i) tm = __builtin_fmaxf(tm, s[i]);
                    const auto sw = __builtin_amdgcn_permlane32_swap(__float_as_uint(tm), __float_as_uint(tm), false, false);
                    tm = __builtin_fmaxf(__uint_as_float(sw[0]), __uint_as_float(sw[1]));
                    const float al = EX(m - tm);
                    m = tm;
#pragma unroll
                    for (int i = 0; i < 16; ++i) { o[i] *= al; s[i] = EX(s[i] - m); }
                    o = smf(__builtin_bit_cast(bf16x8, a0), lo8(s), o, idx);
                    o = smf(__builtin_bit_cast(bf16x8, a1), hi8(s), o, idx);
                }
            }
            const float il = __builtin_amdgcn_rcpf(__uint_as_float(__builtin_amdgcn_permlane32_swap(__float_as_uint(o[8]), __float_as_uint(o[8]), false, false)[0]));
            st_frag(u32x4{pk2(o[0] * il, o[1] * il), pk2(o[2] * il, o[3] * il), pk2(o[4] * il, o[5] * il), pk2(o[6] * il, o[7] * il)}, ws, lane * 16,
                    WS_O + L * 0x200000 + ((g * 8 + w) * 32 + r) * 1024);
            STAMP(22);
            publish<1>(ws, WS_C2 + ((L * 64 + g * 8 + w) * 2 + (r >> 4)) * 128, lane);
            STAMP(8);
        }
        if (w_ < 4) {
            f32x16 acc0, acc1;
            __builtin_amdgcn_s_setprio(CPRIO);
            {
                PHASE_IDS
                const int kh = w >> 1;
                NOTE(26, wait_cnt(ws, WS_C2 + ((L * 64 + own_s32) * 2 + kh) * 128, 16, lane));
                STAMP(9);
                gemm_compute<4, 0, false, false>(lds, lane, kh, 0, ws, WS_O + L * 0x200000 + (own_s32 * 32 + kh * 16) * 1024, 0, acc0, acc1);
                STAMP(10);
                exchange(lds, lane, w, w ^ 2, acc0, acc1);
#pragma unroll
                for (int i = 0; i < 8; ++i) xres[i] = pk2(bf_lo(xres[i]) + acc0[2 * i], bf_hi(xres[i]) + acc0[2 * i + 1]);
                const unsigned st_soff = WS_ST + (L * 64 + own_s32) * 4096, tg = L + 1;
                {
                    const u32x2 rp = row_partial(xres);
                    __builtin_amdgcn_raw_buffer_store_b64(u32x2{rp[0], (rp[1] & ~3u) | tg}, ws, hh ? OOB : l31 * 8u, st_soff + own_d32 * 256, AUX_SC1);
                    arrive<1>(ws, WS_C6 + (L * 64 + own_s32) * 128, lane);
                }
                STAMP(11);
                NOTE(30, wait_cnt(ws, WS_C6 + (L * 64 + own_s32) * 128, 16, lane));
                u32x4 gv[4];
                unsigned it;
#pragma unroll 1
                for (it = 0; it < SPIN_CAP; ++it) {
                    unsigned bad = 0;
#pragma unroll
                    for (int i = 0; i < 4; ++i) gv[i] = __builtin_amdgcn_raw_buffer_load_b128(ws, lane * 16 + i * 1024, st_soff, AUX_SC1);
#pragma unroll
                    for (int i = 0; i < 4; ++i) bad |= (gv[i][1] ^ tg) | (gv[i][3] ^ tg);
                    if (__all((bad & 3) == 0)) break;
                    POLL_PAUSE();
                }
                NOTE(27, it);
                STAMP(12);
                const lptr tp = lds + L_SCR + (w ^ 2) * 4096;
                {
                    const f32x4 tot = record_total(gv);
                    const float m0 = tot[0] * (1.f / 512.f), m1 = tot[2] * (1.f / 512.f);
                    const float r0 = __builtin_amdgcn_rsqf(__builtin_fmaxf(tot[1] * (1.f / 512.f) - m0 * m0, 0.f) * (1.f + LN_EPS) + LN_EPS * LN_EPS);
                    const float r1 = __builtin_amdgcn_rsqf(__builtin_fmaxf(tot[3] * (1.f / 512.f) - m1 * m1, 0.f) * (1.f + LN_EPS) + LN_EPS * LN_EPS);
                    *(LAS f32x4*)(tp + 2048 + (lane & 15) * 16) = f32x4{r0, m0 * r0, r1, m1 * r1};
                }
                const f32x2 sm = *(LAS const f32x2*)(tp + 2048 + l31 * 8);
                const float sc = sm[0], ms = sm[1];
                float xv[16];
#pragma unroll
                for (int i = 0; i < 8; ++i) { xv[2 * i] = bf_lo(xres[i]); xv[2 * i + 1] = bf_hi(xres[i]); }
                u32x4 h0, h1;
#pragma unroll
                for (int i = 0; i < 4; ++i) {
                    h0[i] = pk2(xv[2 * i] * sc - ms, xv[2 * i + 1] * sc - ms);
                    h1[i] = pk2(xv[8 + 2 * i] * sc - ms, xv[9 + 2 * i] * sc - ms);
                }
                *(LAS u32x4*)(tp + l31 * 64 + hh * 32) = h0;
                *(LAS u32x4*)(tp + l31 * 64 + hh * 32 + 16) = h1;
                const lptr rp = tp + (4 * hh + ((lane >> 2) & 3)) * 64 + ((lane >> 4) & 1) * 32 + (lane & 3) * 8;
                const unsigned hv = (hh * 32 + ((l31 & 3) | ((l31 & 12) << 1) | ((l31 & 16) >> 2))) * 16;
#pragma unroll
                for (int t = 0; t < 2; ++t)
                    st_frag(__builtin_bit_cast(u32x4, tr_frag(rp + t * 1024, rp + t * 1024 + 512)), ws, hv, WS_H2 + L * 0x200000 + (own_d32 * 128 + own_s32 * 2 + t) * 1024);
                STAMP(23);
                publish<2>(ws, WS_C3 + ((L * 16 + own_d32) * 2 + (own_s32 >> 5)) * 256, lane);
                if (L == 0 && bid == 0 && w == 0)
                    __builtin_amdgcn_raw_buffer_store_b64(u32x2{nonce[0] + 1u, nonce[1] + (nonce[0] == 0xffffffffu)}, ws, lane == 0 ? 0u : OOB, WS_NONCE, AUX_SC1);
                STAMP(13);
            }
            if (w1_on) {
                PHASE_IDS
                const int kh = w >> 1, d32 = dt1 * 2 + (w & 1), f32t = ft1 * 2 + kh;
                u32x2 bb[4];
#pragma unroll
                for (int i = 0; i < 4; ++i) bb[i] = *(const u32x2*)(b1 + f32t * 32 + 8 * i + 4 * hh);
                NOTE(28, wait_cnt(ws, WS_C3 + ((L * 16 + d32) * 2 + kh) * 256 + (g & 2) * 64, 32, lane));
                STAMP(14);
                gemm_compute<16, 4, false, true>(lds, lane, kh, w & 1, ws, WS_H2 + L * 0x200000 + (d32 * 128 + kh * 64) * 1024, L * 32, acc0, acc1 ISTARG(0));
                STAMP(15);
                exchange(lds, lane, w, w ^ 2, acc0, acc1);
                float u[16];
#pragma unroll
                for (int i = 0; i < 16; ++i) {
                    const unsigned bw = bb[i >> 2][(i >> 1) & 1];
                    const float z = acc0[i] + ((i & 1) ? bf_hi(bw) : bf_lo(bw));
                    const float e = EX(z * __builtin_fmaf(z * z, -0.10294324f, -2.30220818f));
                    u[i] = z * __builtin_amdgcn_rcpf(1.f + e);
                }
#pragma unroll
                for (int t = 0; t < 2; ++t)
                    st_frag(u32x4{pk2(u[8 * t], u[8 * t + 1]), pk2(u[8 * t + 2], u[8 * t + 3]), pk2(u[8 * t + 4], u[8 * t + 5]), pk2(u[8 * t + 6], u[8 * t + 7])}, ws, lane * 16,
                            WS_U + L * 0x200000 + (d32 * 112 + f32t * 2 + t) * 1024);
                STAMP(24);
                publish<2>(ws, WS_C4 + ((L * 16 + d32) * 2 + ((f32t + 4) >> 5)) * 256, lane);
                STAMP(16);
            }
            {
                PHASE_IDS
                const int kh = w & 1;
                const float bias = __uint_as_float((unsigned)b2[own_s32 * 32 + l31] << 16);
                NOTE(29, wait_cnt(ws, WS_C4 + ((L * 16 + own_d32) * 2 + kh) * 256 + (g & 1) * 128, 28, lane));
                STAMP(17);
                gemm_compute<14, 4, true, true>(lds, lane, kh, w >> 1, ws, WS_U + L * 0x200000 + (own_d32 * 112 + kh * 56) * 1024, L * 32 + 16, acc0, acc1 ISTARG(16));
                STAMP(18);
                exchange(lds, lane, w, w ^ 1, acc0, acc1);
#pragma unroll
                for (int i = 0; i < 8; ++i) xres[i] = pk2(bf_lo(xres[i]) + (acc0[2 * i] + bias), bf_hi(xres[i]) + (acc0[2 * i + 1] + bias));
                if (L == 0) {
#pragma unroll
                    for (int t = 0; t < 2; ++t)
                        st_frag(u32x4{xres[4 * t], xres[4 * t + 1], xres[4 * t + 2], xres[4 * t + 3]}, ws, lane * 16, WS_X2 + (own_s32 * 32 + own_d32 * 2 + t) * 1024);
                    __builtin_amdgcn_raw_buffer_store_b64(row_partial(xres), ws, hh ? OOB : l31 * 8u, WS_SP + ((64 + own_s32) * 16 + own_d32) * 256, AUX_SC1);
                    STAMP(25);
                    publish<2>(ws, WS_C5 + own_s32 * 256, lane);
                } else {
                    char* op = (char*)out + (own_s32 * 32 + l31) * 1024 + own_d32 * 64 + hh * 16;
#pragma unroll
                    for (int t = 0; t < 2; ++t) {
                        const auto s0 = __builtin_amdgcn_permlane32_swap(xres[4 * t], xres[4 * t + 2], false, false);
                        const auto s1 = __builtin_amdgcn_permlane32_swap(xres[4 * t + 1], xres[4 * t + 3], false, false);
                        *(u32x4*)(op + t * 32) = u32x4{s0[0], s1[0], s0[1], s1[1]};
                    }
                }
                STAMP(19);
            }
            __builtin_amdgcn_s_setprio(0);
            UNDEF_WC();
        } else {
            PHASE_IDS
            const int lw = w - 4;
            const rsrc_t r1 = mk_rsrc(w1, 2048 * 3584), r2 = mk_rsrc(w2, 1792 * 4096);
            const unsigned v1o = wvoff(lane, 3584), v2o = wvoff(lane, 4096), lb = lds0 + lw * 4096;
            const unsigned g1 = ((lw >> 1) * 1024 + (lw & 1) * 32) * 3584 + ft1 * 128, g2 = ((lw >> 1) * 896 + (lw & 1) * 32) * 4096 + st64 * 128;
            const lptr lp = lds + lw * 4096 + lane * 16, fill = lds + L_FILL + lw * 4, prog = lds + L_PROG + (lw >> 1) * 8;
            u32x4 rg[8][4];
            if (w1_on) {
                ring_prefetch<0, 4, 4, 3584>(lb, rg, r1, v1o, g1, fill, L * 32);
                BAR();
                ring_prefetch<4, 16, 4, 3584>(lb, rg, r1, v1o, g1, fill, L * 32);
                ring_refill<16, 4>(lp, rg, prog, fill, L * 32);
            }
            BAR();
            ring_prefetch<0, 14, 4, 4096>(lb, rg, r2, v2o, g2, fill, L * 32 + 16);
            ring_refill<14, 4>(lp, rg, prog, fill, L * 32 + 16);
            BAR();
        }
    }
}

extern "C" void kernel_launch(void* const* d_in, const int* in_sizes, int n_in, void* d_out, int out_size, void* d_ws, size_t ws_size,
                              hipStream_t stream) {
    fused_fwd<<<256, 512, 0, stream>>>(d_in[0], d_in[1], d_in[2], d_in[4], (const unsigned short*)d_in[5], d_in[6], (const unsigned short*)d_in[7],
                                       (unsigned short*)d_out, (char*)d_ws);
}
```

```cpp
#include <hip/hip_runtime.h>
#include <hip/hip_bf16.h>
#include <stdint.h>

#define WARM1 8
#define WARM2 4
#define THR 4
#define ROT 0
#define XPD 24
#define EARLY 0
#define THR1 4
#define THR2 4
#define CPRIO 2
#define APRIO 1
#define NTLD 0
#define STAMPS 0

typedef __attribute__((ext_vector_type(16))) float f32x16;
typedef __attribute__((ext_vector_type(8))) float f32x8;
typedef __attribute__((ext_vector_type(4))) float f32x4;
typedef __attribute__((ext_vector_type(2))) float f32x2;
typedef __attribute__((ext_vector_type(16))) __bf16 bf16x16;
typedef __attribute__((ext_vector_type(8))) __bf16 bf16x8;
typedef __attribute__((ext_vector_type(2))) __bf16 bf16x2;
typedef __attribute__((ext_vector_type(4))) unsigned u32x4;
typedef __attribute__((ext_vector_type(2))) unsigned u32x2;
typedef __attribute__((ext_vector_type(4))) short s16x4;
typedef __amdgpu_buffer_rsrc_t rsrc_t;
#define LAS __attribute__((address_space(3)))
typedef LAS char* lptr;
#define DI __device__ __forceinline__

constexpr unsigned WS_NONCE = 0x0;
constexpr unsigned WS_F0 = 0x1000;
constexpr unsigned WS_F5 = 0x2000;
constexpr unsigned WS_C1 = 0x10000;
constexpr unsigned WS_C2 = 0x20000;
constexpr unsigned WS_C3 = 0x28000;
constexpr unsigned WS_C4 = 0x2C000;
constexpr unsigned WS_C5 = 0x30000;
constexpr unsigned WS_C6 = 0x34000;
constexpr unsigned WS_ST = 0x40000;
constexpr unsigned WS_SP = 0x140000;
constexpr unsigned WS_KV = 0x200000;
constexpr unsigned WS_O = 0xA00000;
constexpr unsigned WS_H2 = 0xE00000;
constexpr unsigned WS_U = 0x1200000;
constexpr unsigned WS_X2 = 0x1600000;
constexpr unsigned WS_DBG = 0x1800000;
constexpr unsigned WS_END = 0x1A00000;
constexpr unsigned L_WIMG = 16384;
constexpr unsigned L_ONES = 131072;
constexpr unsigned L_CSP = 135168;
constexpr unsigned L_CSF = L_CSP + 1536;
constexpr unsigned L_SST = L_CSP + 2048;
constexpr unsigned L_REG = 139264;
constexpr unsigned L_SCR = 147456;
constexpr unsigned L_TOTAL = 163840;
constexpr int AUX_SC1 = 16;
constexpr unsigned OOB = 0x80000000u;
constexpr unsigned SPIN_CAP = 1u << 16;
constexpr float LN_EPS = 1e-5f;

#define SBAR() __builtin_amdgcn_sched_barrier(0)
#define PIN(x) asm volatile("" : "+v"(x))
#define MFMA(a, b, c) __builtin_amdgcn_mfma_f32_32x32x16_bf16(a, b, c, 0, 0, 0)
#define EX(x) __builtin_amdgcn_exp2f(x)
#define VMWAIT(n) asm volatile("s_waitcnt vmcnt(" #n ")" ::: "memory")
#define BAR() asm volatile("s_waitcnt lgkmcnt(0)\n\ts_barrier" ::: "memory")

template <int N> DI void vm_wait() { asm volatile("s_waitcnt vmcnt(%0)" ::"i"(N) : "memory"); }
template <int N> DI void vm_wait_bar() { asm volatile("s_waitcnt vmcnt(%0) lgkmcnt(0)\n\ts_barrier" ::"i"(N) : "memory"); }
DI rsrc_t mk_rsrc(const void* p, unsigned bytes) { return __builtin_amdgcn_make_buffer_rsrc((void*)p, 0, bytes, 0x00020000); }
template <int N, int LSTEP, int GSTEP> DI void dma(unsigned lds, unsigned voff, rsrc_t rs, unsigned soff) {
    unsigned t;
    asm volatile("s_mov_b32 m0, %1\n\ts_mov_b32 %0, %4\n\tbuffer_load_dwordx4 %2, %3, %0 offen lds\n\t"
                 ".rept %5\n\ts_add_u32 m0, m0, %6\n\ts_add_u32 %0, %0, %7\n\tbuffer_load_dwordx4 %2, %3, %0 offen lds\n\t.endr"
                 : "=&s"(t) : "s"(lds), "v"(voff), "s"(rs), "s"(soff), "i"(N - 1), "i"(LSTEP), "i"(GSTEP) : "memory", "scc");
}
#define NTS ""
template <int N, int LSTEP, int GSTEP> DI void dma_sc1(unsigned lds, unsigned voff, rsrc_t rs, unsigned soff) {
    unsigned t;
    asm volatile("s_mov_b32 m0, %1\n\ts_mov_b32 %0, %4\n\tbuffer_load_dwordx4 %2, %3, %0 offen sc1 " NTS " lds\n\t"
                 ".rept %5\n\ts_add_u32 m0, m0, %6\n\ts_add_u32 %0, %0, %7\n\tbuffer_load_dwordx4 %2, %3, %0 offen sc1 " NTS " lds\n\t.endr"
                 : "=&s"(t) : "s"(lds), "v"(voff), "s"(rs), "s"(soff), "i"(N - 1), "i"(LSTEP), "i"(GSTEP) : "memory", "scc");
}
DI bf16x8 ld_frag(rsrc_t rs, unsigned voff, unsigned soff) { return __builtin_bit_cast(bf16x8, __builtin_amdgcn_raw_buffer_load_b128(rs, voff, soff, AUX_SC1 | (NTLD ? 2 : 0))); }
DI void st_frag(u32x4 v, rsrc_t rs, unsigned voff, unsigned soff) { __builtin_amdgcn_raw_buffer_store_b128(v, rs, voff, soff, AUX_SC1); }
DI bf16x8 lds_frag(lptr p) { return *(LAS const bf16x8*)p; }
DI s16x4 lds_tr(lptr p) { return __builtin_amdgcn_ds_read_tr16_b64_v4i16((LAS s16x4*)p); }
DI bf16x8 tr_frag(lptr p0, lptr p1) {
    const s16x4 a = lds_tr(p0), b = lds_tr(p1);
    return __builtin_bit_cast(bf16x8, __builtin_shufflevector(a, b, 0, 1, 2, 3, 4, 5, 6, 7));
}
DI unsigned pk2(float a, float b) { const f32x2 v = {a, b}; return __builtin_bit_cast(unsigned, __builtin_convertvector(v, bf16x2)); }
DI float bf_lo(unsigned u) { return __uint_as_float(u << 16); }
DI float bf_hi(unsigned u) { return __uint_as_float(u & 0xffff0000u); }
DI float dot2(unsigned a, unsigned b, float c) { return __builtin_amdgcn_fdot2_f32_bf16(__builtin_bit_cast(bf16x2, a), __builtin_bit_cast(bf16x2, b), c, false); }
DI float half_sum2(float a, float b) { const auto r = __builtin_amdgcn_permlane32_swap(__float_as_uint(a), __float_as_uint(b), false, false); return __uint_as_float(r[0]) + __uint_as_float(r[1]); }
DI f32x8 lo8(f32x16 v) { return __builtin_shufflevector(v, v, 0, 1, 2, 3, 4, 5, 6, 7); }
DI f32x8 hi8(f32x16 v) { return __builtin_shufflevector(v, v, 8, 9, 10, 11, 12, 13, 14, 15); }
DI f32x16 smf(bf16x8 a, f32x8 p, f32x16 c, int idx) { return __builtin_amdgcn_smfmac_f32_32x32x32_bf16(a, __builtin_bit_cast(bf16x16, p), c, idx, 0, 0); }
DI int opaque(int v) { asm volatile("" : "+v"(v)); return v; }
DI int sopaque(int v) { asm volatile("" : "+s"(v)); return v; }

#define PSLEEP 0
#define POLL_PAUSE() do { if (PSLEEP) __builtin_amdgcn_s_sleep(PSLEEP); asm volatile("" ::: "memory"); } while (0)
DI unsigned wait_cnt(rsrc_t ws, unsigned soff, unsigned target, int lane) {
    unsigned i;
#pragma unroll 1
    for (i = 0; i < SPIN_CAP; ++i) {
        const unsigned v = __builtin_amdgcn_raw_ptr_buffer_atomic_add_i32(0, ws, lane == 0 ? 0u : OOB, soff, AUX_SC1);
        if ((unsigned)__builtin_amdgcn_readfirstlane(v) >= target) break;
        POLL_PAUSE();
    }
    asm volatile("" ::: "memory");
    return i;
}
template <int NREP> DI void arrive(rsrc_t ws, unsigned soff, int lane) {
    __builtin_amdgcn_raw_ptr_buffer_atomic_add_i32(1, ws, lane < NREP ? lane * 128u : OOB, soff, AUX_SC1);
    asm volatile("" ::: "memory");
}
template <int NREP> DI void publish(rsrc_t ws, unsigned soff, int lane) { VMWAIT(0); arrive<NREP>(ws, soff, lane); }
DI unsigned wait64(rsrc_t ws, unsigned voff, unsigned soff, unsigned tlo, unsigned thi) {
    unsigned i;
#pragma unroll 1
    for (i = 0; i < SPIN_CAP; ++i) {
        const u32x2 v = __builtin_amdgcn_raw_buffer_load_b64(ws, voff, soff, AUX_SC1);
        if (__all(((v[0] ^ tlo) | (v[1] ^ thi)) == 0)) break;
        POLL_PAUSE();
    }
    asm volatile("" ::: "memory");
    return i;
}

#define ISTAMP(i) do { } while (0)
#define NOTE(i, v) (void)(v)
#define STAMP(i) do { } while (0)

DI unsigned wvoff(int lane, unsigned ldw) { return (lane >> 3) * ldw + (((lane & 7) ^ (((lane >> 4) & 1) << 2)) << 4); }
DI int rot(int s, int rho, int n) { const int v = s + rho; return v >= n ? v - n : v; }
template <int S0, int S1, int NST, int SLOT0, int LDW, int TH> DI void ring_prefetch(unsigned lb, rsrc_t rw, unsigned voff, unsigned gb, int rho) {
#pragma unroll
    for (int s = S0; s < S1; ++s) {
        if constexpr (TH >= 0) {
            const unsigned so = gb + rot(s, rho, NST) * (64 * LDW);
            dma<2, 1024, 8 * LDW>(lb + ((s + SLOT0) & 7) * 16384, voff, rw, so);
            vm_wait<TH>();
            dma<2, 1024, 8 * LDW>(lb + ((s + SLOT0) & 7) * 16384 + 2048, voff, rw, so + 16 * LDW);
            vm_wait<TH>();
        } else {
            dma<4, 1024, 8 * LDW>(lb + ((s + SLOT0) & 7) * 16384, voff, rw, gb + rot(s, rho, NST) * (64 * LDW));
        }
    }
}
template <int S0, int S1, int NST, int LDW, int TH> DI void reg_prefetch(u32x4 (&rg)[8][4], rsrc_t rw, unsigned voff, unsigned gb, int rho) {
#pragma unroll
    for (int s = S0; s < S1; ++s) {
        const unsigned so = gb + rot(s, rho, NST) * (64 * LDW);
#pragma unroll
        for (int p = 0; p < 4; ++p) {
            asm volatile("buffer_load_dwordx4 %0, %1, %2, %3 offen" : "=v"(rg[s - 8][p]) : "v"(voff), "s"(rw), "s"(so + p * 8 * LDW) : "memory");
            if constexpr (TH >= 0) if (p & 1) vm_wait<TH>();
        }
    }
}
template <int G0, int G1, int SLOT0, int WAIT> DI void reg_to_ring(lptr lp, u32x4 (&rg)[8][4]) {
#pragma unroll
    for (int s = G0; s < G1; ++s) {
        asm volatile("s_waitcnt vmcnt(%4)" : "+v"(rg[s - 8][0]), "+v"(rg[s - 8][1]), "+v"(rg[s - 8][2]), "+v"(rg[s - 8][3]) : "i"(WAIT) : "memory");
#pragma unroll
        for (int p = 0; p < 4; ++p) *(LAS u32x4*)(lp + ((s + SLOT0) & 7) * 16384 + p * 1024) = rg[s - 8][p];
    }
}
template <int NST, int SLOT0, bool COUNTED> DI void ring_loader(lptr lp, u32x4 (&rg)[8][4]) {
    vm_wait_bar<COUNTED ? 4 * (NST - 4) : 0>();
    vm_wait_bar<COUNTED ? 4 * (NST - 8) : 0>();
    reg_to_ring<8, 12, SLOT0, COUNTED ? 4 * (NST - 12) : 0>(lp, rg);
    BAR();
    reg_to_ring<12, NST, SLOT0, 0>(lp, rg);
    BAR();
}
template <int NST, int SLOT0, bool ACT_A, bool BARS, int PS>
DI void gemm_compute(lptr lds, int lane, int kh, rsrc_t ws, unsigned act_soff, int rho, f32x16& acc0, f32x16& acc1, unsigned* istv = nullptr, int ib = 0) {
    const unsigned av = lane * 16;
    ISTAMP(ib);
    bf16x8 f[PS][4];
#pragma unroll
    for (int s = 0; s < PS; ++s) {
        const unsigned so = act_soff + rot(s, rho, NST) * 4096;
#pragma unroll
        for (int t = 0; t < 4; ++t) f[s][t] = ld_frag(ws, av + t * 1024, so);
    }
    SBAR();
    const int hh = lane >> 5, gq = (lane >> 4) & 1, q = (lane >> 2) & 3, p = lane & 3;
    const unsigned a0 = kh * 8192 + (4 * hh + q) * 128 + (((4 * kh + 2 * gq + (p >> 1)) ^ (((q >> 1) & 1) << 2)) << 4) + (p & 1) * 8;
    const lptr b0 = lds + a0, b1 = lds + (a0 ^ 64), b0h = b0 + 65536, b1h = b1 + 65536;
    bf16x8 w0[2], w1[2];
#define WREAD(dst, pl, ph, k) do { const int sl_ = (((k) >> 2) + SLOT0) & 7, o_ = (sl_ & 3) * 16384 + ((k) & 3) * 2048; \
        const lptr p_ = (sl_ & 4) ? ph : pl; dst[(k) & 1] = tr_frag(p_ + o_, p_ + o_ + 1024); } while (0)
    acc0 = f32x16{}; acc1 = f32x16{};
    ISTAMP(ib + 1);
    if (BARS) BAR();
    ISTAMP(ib + 2);
    WREAD(w1, b1, b1h, 0); WREAD(w0, b0, b0h, 0);
#pragma unroll
    for (int s = 0; s < NST; ++s) {
        const unsigned so = act_soff + rot(s + PS, rho, NST) * 4096;
#pragma unroll
        for (int t = 0; t < 4; ++t) {
            const int k = 4 * s + t;
            const bf16x8 x = f[s % PS][t];
            if (BARS && (k & 15) == 15 && k + 1 < 4 * NST) BAR();
            SBAR();
            acc1 = ACT_A ? MFMA(x, w1[k & 1], acc1) : MFMA(w1[k & 1], x, acc1);
            SBAR();
            if (k + 1 < 4 * NST) WREAD(w1, b1, b1h, k + 1);
            SBAR();
            acc0 = ACT_A ? MFMA(x, w0[k & 1], acc0) : MFMA(w0[k & 1], x, acc0);
            SBAR();
            if (k + 1 < 4 * NST) WREAD(w0, b0, b0h, k + 1);
            if (s + PS < NST) f[s % PS][t] = ld_frag(ws, av + t * 1024, so);
            if (k == 0) ISTAMP(ib + 3);
        }
        if (s & 1) ISTAMP(ib + 4 + (s >> 1));
    }
    SBAR();
#undef WREAD
}
DI void exchange(lptr lds, int lane, int w, int partner, f32x16& acc0, const f32x16& acc1) {
    LAS f32x4* dst = (LAS f32x4*)(lds + L_SCR + w * 4096 + lane * 16);
#pragma unroll
    for (int i = 0; i < 4; ++i) dst[i * 64] = f32x4{acc1[4 * i], acc1[4 * i + 1], acc1[4 * i + 2], acc1[4 * i + 3]};
    BAR();
    LAS const f32x4* src = (LAS const f32x4*)(lds + L_SCR + partner * 4096 + lane * 16);
#pragma unroll
    for (int i = 0; i < 4; ++i) { const f32x4 v = src[i * 64]; acc0[4 * i] += v[0]; acc0[4 * i + 1] += v[1]; acc0[4 * i + 2] += v[2]; acc0[4 * i + 3] += v[3]; }
}

struct QkvAcc { f32x16 qk, v; };
struct WFrag { bf16x8 qk, v; };
DI void qkv_step(QkvAcc& a, bf16x8 x, const WFrag& f, WFrag& n, bool more, lptr pqk, lptr pv, int o0, int o1) {
    SBAR(); a.qk = MFMA(f.qk, x, a.qk); SBAR();
    if (more) n.qk = tr_frag(pqk + o0, pqk + o1);
    SBAR(); a.v = MFMA(x, f.v, a.v); SBAR();
    if (more) n.v = tr_frag(pv + o0, pv + o1);
}
DI u32x2 row_partial(const unsigned (&x)[8]) {
    float sum = 0.f, sq = 0.f;
#pragma unroll
    for (int i = 0; i < 8; ++i) { const float a = bf_lo(x[i]), b = bf_hi(x[i]); sum += a + b; sq = __builtin_fmaf(a, a, __builtin_fmaf(b, b, sq)); }
    const unsigned t = __float_as_uint(half_sum2(sum, sq));
    const auto sw = __builtin_amdgcn_permlane32_swap(t, t, false, false);
    return u32x2{sw[0], sw[1]};
}
DI f32x4 record_total(const u32x4 (&p)[4]) {
    f32x4 t;
#pragma unroll
    for (int j = 0; j < 4; ++j) {
        const unsigned v = __float_as_uint((__uint_as_float(p[0][j]) + __uint_as_float(p[1][j])) + (__uint_as_float(p[2][j]) + __uint_as_float(p[3][j])));
        const auto a = __builtin_amdgcn_permlane32_swap(v, v, false, false);
        const unsigned u = __float_as_uint(__uint_as_float(a[0]) + __uint_as_float(a[1]));
        const auto b = __builtin_amdgcn_permlane16_swap(u, u, false, false);
        t[j] = __uint_as_float(b[0]) + __uint_as_float(b[1]);
    }
    return t;
}
DI void colsum_partial(lptr lds, int lane, int w) {
    const int hh = lane >> 5, gq = (lane >> 4) & 1, q = (lane >> 2) & 3, p = lane & 3;
    const lptr pb = lds + L_WIMG + w * 6144 + hh * 768 + q * 32 + p * 8;
    float cq = 0.f, cv = 0.f;
#pragma unroll
    for (int t = 0; t < 4; ++t) {
        const u32x4 a = __builtin_bit_cast(u32x4, tr_frag(pb + gq * 128 + t * 1536, pb + gq * 128 + t * 1536 + 384));
        const u32x4 b = __builtin_bit_cast(u32x4, tr_frag(pb + 256 + t * 1536, pb + 256 + t * 1536 + 384));
#pragma unroll
        for (int i = 0; i < 4; ++i) { cq = dot2(a[i], 0x3f803f80u, cq); cv = dot2(b[i], 0x3f803f80u, cv); }
    }
    const float c = half_sum2(cq, cv);
    *(LAS float*)(lds + L_CSP + (lane < 48 ? lane : lane - 16) * 32 + w * 4) = c;
}
DI void colsum_final(lptr lds, int lane) {
    const int n = lane < 48 ? lane : lane - 16;
    const f32x4 a = *(LAS const f32x4*)(lds + L_CSP + n * 32), b = *(LAS const f32x4*)(lds + L_CSP + n * 32 + 16);
    *(LAS float*)(lds + L_CSF + n * 4) = ((a[0] + a[1]) + (a[2] + a[3])) + ((b[0] + b[1]) + (b[2] + b[3]));
}

#define E1(C, i) C[i] = EX(C[i]);
#define ATT_TILE(KOFF, VOFF) do { \
        E1(sA, 1) E1(sA, 2) E1(sA, 3) E1(sA, 4) E1(sA, 5) PIN(sA); SBAR(); \
        sB = MFMA(kf, qf2, cneg2); SBAR(); kf = lds_frag(kp + (KOFF)); \
        E1(sA, 6) E1(sA, 7) E1(sA, 8) E1(sA, 9) PIN(sA); SBAR(); \
        o = smf(v0, lo8(sA), o, idx); SBAR(); \
        E1(sA, 10) E1(sA, 11) E1(sA, 12) E1(sA, 13) E1(sA, 14) E1(sA, 15) E1(sB, 0) PIN(sA); PIN(sB); SBAR(); \
        o = smf(v1, hi8(sA), o, idx); SBAR(); \
        E1(sB, 1) E1(sB, 2) E1(sB, 3) E1(sB, 4) E1(sB, 5) PIN(sB); SBAR(); \
        sA = MFMA(kf, qf, cneg); SBAR(); \
        E1(sB, 6) E1(sB, 7) E1(sB, 8) E1(sB, 9) PIN(sB); SBAR(); \
        o2 = smf(v0, lo8(sB), o2, idx); SBAR(); v0 = lds_frag(vp + (VOFF)); \
        E1(sB, 10) E1(sB, 11) E1(sB, 12) E1(sB, 13) E1(sB, 14) E1(sB, 15) E1(sA, 0) PIN(sA); PIN(sB); SBAR(); \
        o2 = smf(v1, hi8(sB), o2, idx); SBAR(); v1 = lds_frag(vp + (VOFF) + 512); \
    } while (0)
DI f32x16 neg_row_max(f32x16 s) {
    float m = __builtin_fmaxf(s[0], s[1]);
#pragma unroll
    for (int i = 2; i < 16; i += 2) m = __builtin_fmaxf(__builtin_fmaxf(m, s[i]), s[i + 1]);
    const auto sw = __builtin_amdgcn_permlane32_swap(__float_as_uint(m), __float_as_uint(m), false, false);
    m = -__builtin_fmaxf(__uint_as_float(sw[0]), __uint_as_float(sw[1]));
    f32x16 c;
#pragma unroll
    for (int i = 0; i < 16; ++i) c[i] = m;
    PIN(c);
    return c;
}

__global__ __launch_bounds__(512, 2) void fused_fwd(const void* __restrict__ xin, const void* __restrict__ wqkv, const void* __restrict__ wo,
                                                    const void* __restrict__ w1, const unsigned short* __restrict__ b1, const void* __restrict__ w2,
                                                    const unsigned short* __restrict__ b2, unsigned short* __restrict__ out, char* wsp) {
    __shared__ __attribute__((aligned(16))) char lds_raw[L_TOTAL];
    const lptr lds = (lptr)lds_raw;
    const unsigned lds0 = (unsigned)(uintptr_t)lds;
    const int w_ = __builtin_amdgcn_readfirstlane(threadIdx.x >> 6);
#define PHASE_IDS \
    const int lane = opaque(threadIdx.x & 63), l31 = lane & 31, hh = lane >> 5; \
    const int w = sopaque(w_), bid = sopaque(blockIdx.x), g = bid & 7, r = bid >> 3; \
    const int st64 = g * 4 + (r >> 3), dt64 = r & 7; \
    const int dt1 = (g & 1) * 4 + (r & 3), ft1 = (g >> 1) * 7 + (r >> 2); \
    const int own_s32 = st64 * 2 + (w & 1), own_d32 = dt64 * 2 + ((w >> 1) & 1); \
    (void)l31; (void)hh; (void)dt1; (void)ft1; (void)own_s32; (void)own_d32;
#define RHO0 (ROT ? (dt64 & 3) : 0)
#define RHO1 (ROT ? ((2 * (r >> 2) + 4 * (r & 3)) & 15) : 0)
#define RHO2 (ROT ? ((3 * (r >> 3) + 2 * (r & 7)) % 14) : 0)
    const bool w1_on = (blockIdx.x >> 3) < 28;
    const rsrc_t ws = mk_rsrc(wsp, WS_END);

#define ISTARG(b)
    unsigned xres[8] = {};
    unsigned tag_lo = 0, tag_hi = 0;
    u32x2 nonce = {};
    u32x4 wc[12];
#define UNDEF_WC() do { _Pragma("unroll") for (int i_ = 0; i_ < 12; ++i_) asm volatile("" : "=v"(wc[i_])); } while (0)
    UNDEF_WC();

#pragma unroll 1
    for (int L = 0; L < 2; ++L) {
        bf16x8 qf;
        {
            PHASE_IDS
            const int gq = (lane >> 4) & 1, q = (lane >> 2) & 3, p = lane & 3;
            const unsigned kv_soff = WS_KV + (L * 32 + r) * 131072;
            STAMP(0);
            QkvAcc a; a.qk = f32x16{}; a.v = f32x16{};
            u32x4 sp[4];
            float rstd, mr, csv;
            f32x4 cs[4], rs[4], ms[4];
#define LN_PREP() do { \
                const f32x4 tot = record_total(sp); \
                const float m0 = tot[0] * (1.f / 512.f), m1 = tot[2] * (1.f / 512.f); \
                const float r0 = __builtin_amdgcn_rsqf(__builtin_fmaxf(tot[1] * (1.f / 512.f) - m0 * m0, 0.f) + LN_EPS); \
                const float r1 = __builtin_amdgcn_rsqf(__builtin_fmaxf(tot[3] * (1.f / 512.f) - m1 * m1, 0.f) + LN_EPS); \
                const lptr sst = lds + L_SST + w * 256; \
                *(LAS f32x2*)(sst + hh * 128 + (lane & 15) * 8) = hh ? f32x2{m0 * r0, m1 * r1} : f32x2{r0, r1}; \
                rstd = *(LAS const float*)(sst + l31 * 4); mr = *(LAS const float*)(sst + 128 + l31 * 4); \
                _Pragma("unroll") for (int i = 0; i < 4; ++i) { \
                    cs[i] = *(LAS const f32x4*)(lds + L_CSF + (8 * i + 4 * hh) * 4); \
                    rs[i] = *(LAS const f32x4*)(sst + (8 * i + 4 * hh) * 4); ms[i] = *(LAS const f32x4*)(sst + 128 + (8 * i + 4 * hh) * 4); } \
                csv = *(LAS const float*)(lds + L_CSF + 128 + (lane & 15) * 4); \
                SBAR(); } while (0)
            const unsigned sp_soff = WS_SP + (L * 64 + g * 8 + w) * 4096;
            if (L == 0) {
                if (w >= 4) {
                    const rsrc_t rw = mk_rsrc(wqkv, 512 * 3072);
#pragma unroll
                    for (int ty = 0; ty < 3; ++ty) {
                        const int o = ty * 1024 + lane * 16, rg = o / 384, rem = o % 384;
                        const unsigned voff = (4 * rg + ((rem >> 5) & 3)) * 3072 + (rem >> 7) * 1024 + (rem & 16);
                        dma<4, 3072, 98304>(lds0 + L_WIMG + (w - 4) * 12288 + ty * 1024, voff, rw, (w - 4) * 393216 + r * 32);
                    }
                }
                const rsrc_t rx = mk_rsrc(xin, 2048 * 1024);
                nonce = __builtin_amdgcn_raw_buffer_load_b64(ws, 0, WS_NONCE, AUX_SC1);
                if (w < 4) {
#pragma unroll
                    for (int i = 0; i < 4; ++i) {
                        const u32x2 v = __builtin_amdgcn_raw_buffer_load_b64(rx, l31 * 1024 + hh * 8, own_s32 * 32768 + own_d32 * 64 + i * 16, 0);
                        xres[2 * i] = v[0]; xres[2 * i + 1] = v[1];
                    }
                }
                const unsigned tokb = (g * 256 + w * 32) * 1024, stg = w < 5 ? 65536 + w * 12288 : (w == 5 ? 0 : L_REG + (w - 6) * 12288);
                const unsigned vA = (lane >> 3) * 1024 + (((lane & 7) ^ (lane >> 4)) << 4), vB = vA ^ 64;
#define XCHUNK(c) do { dma<2, 2048, 16384>(lds0 + stg + ((c) % 3) * 4096, vA, rx, tokb + (c) * 128); \
                       dma<2, 2048, 16384>(lds0 + stg + ((c) % 3) * 4096 + 1024, vB, rx, tokb + 8192 + (c) * 128); } while (0)
                XCHUNK(0); XCHUNK(1); XCHUNK(2);
                vm_wait<12>();
                {
                    unsigned long long z = ((unsigned long long)__builtin_amdgcn_readfirstlane(nonce[1]) << 32) | (unsigned)__builtin_amdgcn_readfirstlane(nonce[0]);
                    z = z * 0x9E3779B97F4A7C15ull + 0x632BE59BD9B4E019ull;
                    z ^= z >> 29; z *= 0xBF58476D1CE4E5B9ull; z ^= z >> 32;
                    tag_lo = (unsigned)z; tag_hi = (unsigned)(z >> 32);
                }
                if (w < 4) {
                    __builtin_amdgcn_raw_buffer_store_b64(row_partial(xres), ws, hh ? OOB : l31 * 8u, WS_SP + (own_s32 * 16 + own_d32) * 256, AUX_SC1);
                    __builtin_amdgcn_raw_buffer_store_b64(u32x2{}, ws, l31 * 8 + hh * 0x40000, WS_ST + (own_s32 * 16 + own_d32) * 256, AUX_SC1);
                    const unsigned c1a = opaque(WS_C1 + (r * 8 + g) * 128 + lane * 32768), c3a = opaque(WS_C3 + bid * 256 + (lane - 3) * 128);
                    const unsigned c2a = sopaque(WS_C2 + (((r >> 4) * 64 + g * 8) * 2 + (r & 15)) * 128);
                    const unsigned ca = lane < 2 ? c1a : (lane == 2 ? c2a : c3a);
                    __builtin_amdgcn_raw_buffer_store_b32(0u, ws, (w == 0 && lane < 5) ? ca : OOB, 0, AUX_SC1);
                    VMWAIT(0);
                    const unsigned f5a = sopaque(WS_F5 + own_s32 * 128 + own_d32 * 8), f0a = sopaque(w == 0 ? WS_F0 + r * 128 + g * 8 : OOB);
                    __builtin_amdgcn_raw_buffer_store_b64(u32x2{tag_lo, tag_hi}, ws, lane == 0 ? f5a : (lane == 1 ? f0a : OOB), 0, AUX_SC1);
                }
                BAR();
                STAMP(1);
                if (w >= 4) {
#pragma unroll
                    for (int i = 0; i < 12; ++i) wc[i] = *(LAS const u32x4*)(lds + L_WIMG + (w - 4) * 12288 + i * 1024 + lane * 16);
                }
                colsum_partial(lds, lane, w);
                const lptr pqk = lds + L_WIMG + hh * 768 + gq * 128 + q * 32 + p * 8, pv = lds + L_WIMG + hh * 768 + 256 + q * 32 + p * 8;
                lptr px[4];
#pragma unroll
                for (int t = 0; t < 4; ++t) px[t] = lds + stg + l31 * 128 + (((2 * t + hh) ^ ((l31 >> 1) & 7)) << 4);
                WFrag wf[2];
                wf[0].qk = tr_frag(pqk, pqk + 384); wf[0].v = tr_frag(pv, pv + 384);
#pragma unroll
                for (int c = 0; c < 8; ++c) {
                    if (c <= 5) vm_wait<8>(); else if (c == 6) vm_wait<4>(); else vm_wait<0>();
                    bf16x8 xf[4];
#pragma unroll
                    for (int t = 0; t < 4; ++t) xf[t] = lds_frag(px[t] + (c % 3) * 4096);
                    if (c + 3 < 8) { asm volatile("s_waitcnt lgkmcnt(0)" : "+v"(xf[0]), "+v"(xf[1]), "+v"(xf[2]), "+v"(xf[3]) :: "memory"); XCHUNK(c + 3); }
                    if (c == 7) {
                        const unsigned f0a = opaque(WS_F0 + r * 128 + (lane & 7) * 8), f5a = opaque(WS_F5 + (g * 8 + w) * 128 + (lane & 15) * 8);
                        wait64(ws, hh ? f0a : f5a, 0, tag_lo, tag_hi);
#pragma unroll
                        for (int i = 0; i < 4; ++i) sp[i] = __builtin_amdgcn_raw_buffer_load_b128(ws, lane * 16 + i * 1024, sp_soff, AUX_SC1);
                        SBAR();
                    }
#pragma unroll
                    for (int t = 0; t < 4; ++t) {
                        const int k = 4 * c + t;
                        qkv_step(a, xf[t], wf[k & 1], wf[(k + 1) & 1], k < 31, pqk, pv, (k + 1) * 1536, (k + 1) * 1536 + 384);
                    }
                    if (c == 1) { BAR(); if (w == 0) colsum_final(lds, lane); }
                    if (c == 3) BAR();
                }
                LN_PREP();
#undef XCHUNK
            } else {
                if (w >= 4) {
#pragma unroll
                    for (int i = 0; i < 12; ++i) *(LAS u32x4*)(lds + L_WIMG + (w - 4) * 12288 + i * 1024 + lane * 16) = wc[i];
                }
                wait_cnt(ws, WS_C5 + ((g * 8 + w) * 2 + (r & 1)) * 128, 16, lane);
                STAMP(1);
                ISTAMP(32);
                bf16x8 xf[32];
                const unsigned x_soff = WS_X2 + (g * 8 + w) * 32768;
#pragma unroll
                for (int k = 0; k < XPD; ++k) xf[k] = ld_frag(ws, lane * 16 + (k & 3) * 1024, x_soff + (k >> 2) * 4096);
                ISTAMP(33);
                BAR();
                ISTAMP(34);
                const lptr pv = lds + L_WIMG + hh * 384 + q * 32 + p * 8 + 256, pqk = pv + gq * 128 - 256;
                WFrag wf[2];
                wf[0].qk = tr_frag(pqk, pqk + 768); wf[0].v = tr_frag(pv, pv + 768);
#pragma unroll
                for (int k = 0; k < 32; ++k) {
                    qkv_step(a, xf[k], wf[k & 1], wf[(k + 1) & 1], k < 31, pqk, pv, (k + 1) * 1536, (k + 1) * 1536 + 768);
                    if (k + XPD < 32) xf[k + XPD] = ld_frag(ws, lane * 16 + ((k + XPD) & 3) * 1024, x_soff + ((k + XPD) >> 2) * 4096);
                    if (k + XPD == 32) {
#pragma unroll
                        for (int i = 0; i < 4; ++i) sp[i] = __builtin_amdgcn_raw_buffer_load_b128(ws, lane * 16 + i * 1024, sp_soff, AUX_SC1);
                    }
                    if (k == 0) ISTAMP(35);
                    if ((k & 3) == 3) ISTAMP(36 + (k >> 2));
                    if (k == 27) { LN_PREP(); ISTAMP(44); }
                }
                UNDEF_WC();
            }
            STAMP(2);
            float qk[16], vv[16];
#pragma unroll
            for (int i = 0; i < 16; ++i) qk[i] = a.qk[i] * rstd - mr * cs[i >> 2][i & 3];
            const u32x4 kpk = {pk2(qk[8], qk[9]), pk2(qk[10], qk[11]), pk2(qk[12], qk[13]), pk2(qk[14], qk[15])};
            const unsigned koff = (hh * 32 + ((l31 & 19) | ((l31 & 4) << 1) | ((l31 & 8) >> 1))) * 16;
            st_frag(kpk, ws, koff, kv_soff + (g * 8 + w) * 2048);
#pragma unroll
            for (int i = 0; i < 16; ++i) vv[i] = a.v[i] * rs[i >> 2][i & 3] - ms[i >> 2][i & 3] * csv;
            u32x4 vpk;
#pragma unroll
            for (int i = 0; i < 4; ++i) { const unsigned lo = pk2(vv[2 * i], vv[2 * i + 1]), hi = pk2(vv[8 + 2 * i], vv[9 + 2 * i]); vpk[i] = gq ? hi : lo; }
            const unsigned voff = gq * 512 + hh * 256 + (lane & 15) * 16;
            st_frag(vpk, ws, voff, kv_soff + (g * 8 + w) * 2048 + 1024);
            constexpr float QS = 0.25f * 1.4426950408889634f;
            const u32x4 qpk = {pk2(qk[0] * QS, qk[1] * QS), pk2(qk[2] * QS, qk[3] * QS), pk2(qk[4] * QS, qk[5] * QS), pk2(qk[6] * QS, qk[7] * QS)};
            qf = __builtin_bit_cast(bf16x8, qpk);
            if (L == 0) {
                BAR();
                const unsigned one = (lane & 15) ? 0u : 0x3f803f80u;
                *(LAS u32x4*)(lds + L_ONES + w * 512 + hh * 256 + (lane & 15) * 16) = u32x4{one, one, one, one};
            }
            *(LAS u32x4*)(lds + w * 1024 + koff) = kpk;
            *(LAS u32x4*)(lds + 8192 + w * 1024 + voff) = vpk;
            *(LAS u32x4*)(lds + L_REG + w * 3072 + lane * 16) = qpk;
            BAR();
            STAMP(3);
        }
        {
            PHASE_IDS
            const int gq = (lane >> 4) & 1;
            const unsigned kv_soff = WS_KV + (L * 32 + r) * 131072;
            const int idx = opaque(0xDDDD);
            lptr kp = lds + (w & 1) * 4096 + lane * 16;
            lptr vp = lds + (gq ? L_ONES : 8192 + (w & 1) * 4096) + hh * 256 + (lane & 15) * 16;
            const int vinc = gq ? 0 : 16384;
            const bf16x8 qf2 = lds_frag(lds + L_REG + (w ^ 1) * 3072 + lane * 16), k0 = lds_frag(lds + lane * 16);
            bf16x8 kf = lds_frag(kp), v0 = lds_frag(vp), v1 = lds_frag(vp + 512);
            f32x16 sA = MFMA(k0, qf, f32x16{}), sB = MFMA(k0, qf2, f32x16{}), o = {}, o2 = {};
            const f32x16 cneg = neg_row_max(sA), cneg2 = neg_row_max(sB);
#define KVCHUNK(j) dma_sc1<2, 8192, 1024>(lds0 + (j) * 16384 + w * 1024, lane * 16, ws, kv_soff + (((g + (j)) & 7) * 8 + w) * 2048)
            sA = MFMA(kf, qf, cneg);
            publish<1>(ws, WS_C1 + ((L * 32 + r) * 8 + w) * 128, lane);
            sA[0] = EX(sA[0]);
#define PRIO(n) do { if (APRIO) __builtin_amdgcn_s_setprio(n); } while (0)
#pragma unroll
            for (int c = 0; c < 8; ++c) {
                if (c == 0 || c == 1 || c == 3 || c == 4 || c == 7) PRIO(2);
                ATT_TILE(1024, 1024);
                if (c == 0) {
                    STAMP(4);
                    wait_cnt(ws, WS_C1 + ((L * 32 + r) * 8 + w) * 128, 8, lane);
                    STAMP(5);
                    KVCHUNK(1);
                }
                if (c == 0 || c == 1 || c == 3 || c == 4) PRIO(1);
                ATT_TILE(2048, 2048);
                if (c == 0) { KVCHUNK(2); KVCHUNK(3); }
                if (c == 0 || c == 1 || c == 3 || c == 4) PRIO(0); else if (c == 7) PRIO(1);
                ATT_TILE(3072, 3072);
                if (c == 0) { vm_wait_bar<4>(); KVCHUNK(4); KVCHUNK(5); STAMP(6); }
                if (c == 1) { vm_wait_bar<4>(); KVCHUNK(6); KVCHUNK(7); }
                if (c == 3) vm_wait_bar<0>();
                if (c == 4) {
                    BAR();
                    dma<8, 1024, 8192>(lds0 + w * 8192, wvoff(lane, 1024), mk_rsrc(wo, 512 * 1024), ((w & 1) * 256 + (((w >> 1) + RHO0) & 3) * 64) * 1024 + dt64 * 128);
                }
                if (c == 5 && WARM1) dma<8, 0, 8 * 3584>(lds0 + L_REG + w * 3072, wvoff(lane, 3584), mk_rsrc(w1, 2048 * 3584), ((r & 3) * 512 + w * 64) * 3584 + (w1_on ? ft1 : 0) * 128);
                if (c == 6 && WARM2) dma<4, 0, 8 * 4096>(lds0 + L_REG + w * 3072, wvoff(lane, 4096), mk_rsrc(w2, 1792 * 4096), (dt64 * 224 + (w < 7 ? w : 0) * 32) * 4096 + st64 * 128);
                if (c < 7) { if (c != 2 && c < 5) PRIO(3); vp += vinc; ATT_TILE(16384, 0); kp += 16384; }
                else { PRIO(0); ATT_TILE(0, 0); }
            }
            STAMP(7);
            VMWAIT(0);
            STAMP(20);
            {
                const lptr mine = lds + L_REG + w * 3072 + lane * 16, other = lds + L_REG + (w ^ 1) * 3072 + lane * 16;
                *(LAS f32x4*)mine = f32x4{o2[0], o2[1], o2[2], o2[3]};
                *(LAS f32x4*)(mine + 1024) = f32x4{o2[4], o2[5], o2[6], o2[7]};
                *(LAS float*)(mine + 2048) = o2[8];
                BAR();
                STAMP(21);
                const f32x4 a0 = *(LAS const f32x4*)other, a1 = *(LAS const f32x4*)(other + 1024);
                o[8] += *(LAS const float*)(other + 2048);
#pragma unroll
                for (int i = 0; i < 4; ++i) { o[i] += a0[i]; o[4 + i] += a1[i]; }
            }
            float chk = __builtin_fabsf(o[8]);
#pragma unroll
            for (int i = 0; i < 8; ++i) chk += __builtin_fabsf(o[i]);
            if (__builtin_expect(__any(!(chk < 1e36f)), 0)) {
                float m = -__builtin_inff();
                o = f32x16{};
                const u32x4 one4 = {0x3f803f80u, 0x3f803f80u, 0x3f803f80u, 0x3f803f80u};
#pragma unroll 1
                for (int t = 0; t < 64; ++t) {
                    const bf16x8 kk = ld_frag(ws, lane * 16, kv_soff + t * 2048);
                    u32x4 a0 = __builtin_amdgcn_raw_buffer_load_b128(ws, hh * 256 + (lane & 15) * 16, kv_soff + t * 2048 + 1024, AUX_SC1);
                    u32x4 a1 = __builtin_amdgcn_raw_buffer_load_b128(ws, hh * 256 + (lane & 15) * 16, kv_soff + t * 2048 + 1536, AUX_SC1);
                    a0 = gq ? one4 : a0; a1 = gq ? one4 : a1;
                    f32x16 s = MFMA(kk, qf, f32x16{});
                    float tm = m;
#pragma unroll
                    for (int i = 0; i < 16; ++i) tm = __builtin_fmaxf(tm, s[i]);
                    const auto sw = __builtin_amdgcn_permlane32_swap(__float_as_uint(tm), __float_as_uint(tm), false, false);
                    tm = __builtin_fmaxf(__uint_as_float(sw[0]), __uint_as_float(sw[1]));
                    const float al = EX(m - tm);
                    m = tm;
#pragma unroll
                    for (int i = 0; i < 16; ++i) { o[i] *= al; s[i] = EX(s[i] - m); }
                    o = smf(__builtin_bit_cast(bf16x8, a0), lo8(s), o, idx);
                    o = smf(__builtin_bit_cast(bf16x8, a1), hi8(s), o, idx);
                }
            }
            const float il = __builtin_amdgcn_rcpf(__uint_as_float(__builtin_amdgcn_permlane32_swap(__float_as_uint(o[8]), __float_as_uint(o[8]), false, false)[0]));
            st_frag(u32x4{pk2(o[0] * il, o[1] * il), pk2(o[2] * il, o[3] * il), pk2(o[4] * il, o[5] * il), pk2(o[6] * il, o[7] * il)}, ws, lane * 16,
                    WS_O + L * 0x200000 + ((g * 8 + w) * 32 + r) * 1024);
            STAMP(22);
            publish<1>(ws, WS_C2 + ((L * 64 + g * 8 + w) * 2 + (r >> 4)) * 128, lane);
            STAMP(8);
        }
        if (w_ < 4) {
            f32x16 acc0, acc1;
            __builtin_amdgcn_s_setprio(CPRIO);
            {
                PHASE_IDS
                const int kh = w >> 1;
                NOTE(26, wait_cnt(ws, WS_C2 + ((L * 64 + own_s32) * 2 + kh) * 128, 16, lane));
                STAMP(9);
                gemm_compute<4, 0, false, false, 4>(lds, lane, kh, ws, WS_O + L * 0x200000 + (own_s32 * 32 + kh * 16) * 1024, RHO0, acc0, acc1);
                STAMP(10);
                exchange(lds, lane, w, w ^ 2, acc0, acc1);
#pragma unroll
                for (int i = 0; i < 8; ++i) xres[i] = pk2(bf_lo(xres[i]) + acc0[2 * i], bf_hi(xres[i]) + acc0[2 * i + 1]);
                const unsigned st_soff = WS_ST + (L * 64 + own_s32) * 4096, tg = L + 1;
                {
                    const u32x2 rp = row_partial(xres);
                    __builtin_amdgcn_raw_buffer_store_b64(u32x2{rp[0], (rp[1] & ~3u) | tg}, ws, hh ? OOB : l31 * 8u, st_soff + own_d32 * 256, AUX_SC1);
                    arrive<1>(ws, WS_C6 + (L * 64 + own_s32) * 128, lane);
                }
                STAMP(11);
                NOTE(30, wait_cnt(ws, WS_C6 + (L * 64 + own_s32) * 128, 16, lane));
                u32x4 gv[4];
                unsigned it;
#pragma unroll 1
                for (it = 0; it < SPIN_CAP; ++it) {
                    unsigned bad = 0;
#pragma unroll
                    for (int i = 0; i < 4; ++i) gv[i] = __builtin_amdgcn_raw_buffer_load_b128(ws, lane * 16 + i * 1024, st_soff, AUX_SC1);
#pragma unroll
                    for (int i = 0; i < 4; ++i) bad |= (gv[i][1] ^ tg) | (gv[i][3] ^ tg);
                    if (__all((bad & 3) == 0)) break;
                    POLL_PAUSE();
                }
                NOTE(27, it);
                STAMP(12);
                const lptr tp = lds + L_SCR + (w ^ 2) * 4096;
                {
                    const f32x4 tot = record_total(gv);
                    const float m0 = tot[0] * (1.f / 512.f), m1 = tot[2] * (1.f / 512.f);
                    const float r0 = __builtin_amdgcn_rsqf(__builtin_fmaxf(tot[1] * (1.f / 512.f) - m0 * m0, 0.f) * (1.f + LN_EPS) + LN_EPS * LN_EPS);
                    const float r1 = __builtin_amdgcn_rsqf(__builtin_fmaxf(tot[3] * (1.f / 512.f) - m1 * m1, 0.f) * (1.f + LN_EPS) + LN_EPS * LN_EPS);
                    *(LAS f32x4*)(tp + 2048 + (lane & 15) * 16) = f32x4{r0, m0 * r0, r1, m1 * r1};
                }
                const f32x2 sm = *(LAS const f32x2*)(tp + 2048 + l31 * 8);
                const float sc = sm[0], ms = sm[1];
                float xv[16];
#pragma unroll
                for (int i = 0; i < 8; ++i) { xv[2 * i] = bf_lo(xres[i]); xv[2 * i + 1] = bf_hi(xres[i]); }
                u32x4 h0, h1;
#pragma unroll
                for (int i = 0; i < 4; ++i) {
                    h0[i] = pk2(xv[2 * i] * sc - ms, xv[2 * i + 1] * sc - ms);
                    h1[i] = pk2(xv[8 + 2 * i] * sc - ms, xv[9 + 2 * i] * sc - ms);
                }
                *(LAS u32x4*)(tp + l31 * 64 + hh * 32) = h0;
                *(LAS u32x4*)(tp + l31 * 64 + hh * 32 + 16) = h1;
                const lptr rp = tp + (4 * hh + ((lane >> 2) & 3)) * 64 + ((lane >> 4) & 1) * 32 + (lane & 3) * 8;
                const unsigned hv = (hh * 32 + ((l31 & 3) | ((l31 & 12) << 1) | ((l31 & 16) >> 2))) * 16;
#pragma unroll
                for (int t = 0; t < 2; ++t)
                    st_frag(__builtin_bit_cast(u32x4, tr_frag(rp + t * 1024, rp + t * 1024 + 512)), ws, hv, WS_H2 + L * 0x200000 + (own_d32 * 128 + own_s32 * 2 + t) * 1024);
                STAMP(23);
                publish<2>(ws, WS_C3 + ((L * 16 + own_d32) * 2 + (own_s32 >> 5)) * 256, lane);
                if (L == 0 && bid == 0 && w == 0)
                    __builtin_amdgcn_raw_buffer_store_b64(u32x2{nonce[0] + 1u, nonce[1] + (nonce[0] == 0xffffffffu)}, ws, lane == 0 ? 0u : OOB, WS_NONCE, AUX_SC1);
                STAMP(13);
            }
            if (w1_on) {
                PHASE_IDS
                const int kh = w >> 1, d32 = dt1 * 2 + (w & 1), f32t = ft1 * 2 + kh;
                u32x2 bb[4];
#pragma unroll
                for (int i = 0; i < 4; ++i) bb[i] = *(const u32x2*)(b1 + f32t * 32 + 8 * i + 4 * hh);
                NOTE(28, wait_cnt(ws, WS_C3 + ((L * 16 + d32) * 2 + kh) * 256 + (g & 2) * 64, 32, lane));
                STAMP(14);
                gemm_compute<16, 4, false, true, 4>(lds, lane, kh, ws, WS_H2 + L * 0x200000 + (d32 * 128 + kh * 64) * 1024, RHO1, acc0, acc1 ISTARG(0));
                STAMP(15);
                exchange(lds, lane, w, w ^ 2, acc0, acc1);
                float u[16];
#pragma unroll
                for (int i = 0; i < 16; ++i) {
                    const unsigned bw = bb[i >> 2][(i >> 1) & 1];
                    const float z = acc0[i] + ((i & 1) ? bf_hi(bw) : bf_lo(bw));
                    const float e = EX(z * __builtin_fmaf(z * z, -0.10294324f, -2.30220818f));
                    u[i] = z * __builtin_amdgcn_rcpf(1.f + e);
                }
#pragma unroll
                for (int t = 0; t < 2; ++t)
                    st_frag(u32x4{pk2(u[8 * t], u[8 * t + 1]), pk2(u[8 * t + 2], u[8 * t + 3]), pk2(u[8 * t + 4], u[8 * t + 5]), pk2(u[8 * t + 6], u[8 * t + 7])}, ws, lane * 16,
                            WS_U + L * 0x200000 + (d32 * 112 + f32t * 2 + t) * 1024);
                STAMP(24);
                publish<2>(ws, WS_C4 + ((L * 16 + d32) * 2 + ((f32t + 4) >> 5)) * 256, lane);
                STAMP(16);
            }
            {
                PHASE_IDS
                const int kh = w & 1;
                const float bias = __uint_as_float((unsigned)b2[own_s32 * 32 + l31] << 16);
                NOTE(29, wait_cnt(ws, WS_C4 + ((L * 16 + own_d32) * 2 + kh) * 256 + (g & 1) * 128, 28, lane));
                STAMP(17);
                gemm_compute<14, 4, true, true, 4>(lds, lane, kh, ws, WS_U + L * 0x200000 + (own_d32 * 112 + kh * 56) * 1024, RHO2, acc0, acc1 ISTARG(16));
                STAMP(18);
                exchange(lds, lane, w, w ^ 1, acc0, acc1);
#pragma unroll
                for (int i = 0; i < 8; ++i) xres[i] = pk2(bf_lo(xres[i]) + (acc0[2 * i] + bias), bf_hi(xres[i]) + (acc0[2 * i + 1] + bias));
                if (L == 0) {
#pragma unroll
                    for (int t = 0; t < 2; ++t)
                        st_frag(u32x4{xres[4 * t], xres[4 * t + 1], xres[4 * t + 2], xres[4 * t + 3]}, ws, lane * 16, WS_X2 + (own_s32 * 32 + own_d32 * 2 + t) * 1024);
                    __builtin_amdgcn_raw_buffer_store_b64(row_partial(xres), ws, hh ? OOB : l31 * 8u, WS_SP + ((64 + own_s32) * 16 + own_d32) * 256, AUX_SC1);
                    STAMP(25);
                    publish<2>(ws, WS_C5 + own_s32 * 256, lane);
                } else {
                    char* op = (char*)out + (own_s32 * 32 + l31) * 1024 + own_d32 * 64 + hh * 16;
#pragma unroll
                    for (int t = 0; t < 2; ++t) {
                        const auto s0 = __builtin_amdgcn_permlane32_swap(xres[4 * t], xres[4 * t + 2], false, false);
                        const auto s1 = __builtin_amdgcn_permlane32_swap(xres[4 * t + 1], xres[4 * t + 3], false, false);
                        *(u32x4*)(op + t * 32) = u32x4{s0[0], s1[0], s0[1], s1[1]};
                    }
                }
                STAMP(19);
            }
            __builtin_amdgcn_s_setprio(0);
            UNDEF_WC();
        } else {
            PHASE_IDS
            const int lw = w - 4;
            const rsrc_t r1 = mk_rsrc(w1, 2048 * 3584), r2 = mk_rsrc(w2, 1792 * 4096);
            const unsigned v1o = wvoff(lane, 3584), v2o = wvoff(lane, 4096), lb = lds0 + lw * 4096;
            const unsigned g1 = ((lw >> 1) * 1024 + (lw & 1) * 32) * 3584 + ft1 * 128, g2 = ((lw >> 1) * 896 + (lw & 1) * 32) * 4096 + st64 * 128;
            const lptr lp = lds + lw * 4096 + lane * 16;
            u32x4 rg[8][4];
            if (w1_on) {
                ring_prefetch<0, 4, 16, 4, 3584, THR>(lb, r1, v1o, g1, RHO1);
                reg_prefetch<8, 8 + EARLY, 16, 3584, THR>(rg, r1, v1o, g1, RHO1);
                BAR();
                ring_prefetch<4, 8, 16, 4, 3584, THR1>(lb, r1, v1o, g1, RHO1);
                reg_prefetch<8 + EARLY, 16, 16, 3584, THR1>(rg, r1, v1o, g1, RHO1);
                ring_loader<16, 4, false>(lp, rg);
            }
            BAR();
            ring_prefetch<0, 8, 14, 4, 4096, THR2>(lb, r2, v2o, g2, RHO2);
            reg_prefetch<8, 14, 14, 4096, -1>(rg, r2, v2o, g2, RHO2);
            ring_loader<14, 4, true>(lp, rg);
            BAR();
        }
    }
}

extern "C" void kernel_launch(void* const* d_in, const int* in_sizes, int n_in, void* d_out, int out_size, void* d_ws, size_t ws_size,
                              hipStream_t stream) {
    fused_fwd<<<256, 512, 0, stream>>>(d_in[0], d_in[1], d_in[2], d_in[4], (const unsigned short*)d_in[5], d_in[6], (const unsigned short*)d_in[7],
                                       (unsigned short*)d_out, (char*)d_ws);
}
```

```cpp
#include <hip/hip_runtime.h>
#include <hip/hip_bf16.h>
#include <stdint.h>

#define WARM1 5
#define WARM2 1
#define AN0 6
#define APD 12
#define XN0 6
#define XPD 12
#define CPRIO 2
#define APRIO 1
#define NTLD 0
#define TS1 1
#define TS2 2
#define RDBK 1
#define ASSIST 0
#define QPRIO 1
#define STCNT 1
#define OUTAUX -1
#define CODEPF 0
#define LNK2 4
#define LNK 12
#define INNER 15
#define STAMPS 0

typedef __attribute__((ext_vector_type(16))) float f32x16;
typedef __attribute__((ext_vector_type(8))) float f32x8;
typedef __attribute__((ext_vector_type(4))) float f32x4;
typedef __attribute__((ext_vector_type(2))) float f32x2;
typedef __attribute__((ext_vector_type(16))) __bf16 bf16x16;
typedef __attribute__((ext_vector_type(8))) __bf16 bf16x8;
typedef __attribute__((ext_vector_type(2))) __bf16 bf16x2;
typedef __attribute__((ext_vector_type(4))) unsigned u32x4;
typedef __attribute__((ext_vector_type(2))) unsigned u32x2;
typedef __attribute__((ext_vector_type(4))) short s16x4;
typedef __amdgpu_buffer_rsrc_t rsrc_t;
#define LAS __attribute__((address_space(3)))
typedef LAS char* lptr;
#define DI __device__ __forceinline__

constexpr unsigned WS_NONCE = 0x0;
constexpr unsigned WS_F0 = 0x1000;
constexpr unsigned WS_F5 = 0x2000;
constexpr unsigned WS_C1 = 0x10000;
constexpr unsigned WS_C2 = 0x20000;
constexpr unsigned WS_C3 = 0x28000;
constexpr unsigned WS_C4 = 0x2C000;
constexpr unsigned WS_C5 = 0x30000;
constexpr unsigned WS_C6 = 0x34000;
constexpr unsigned WS_ST = 0x40000;
constexpr unsigned WS_SP = 0x140000;
constexpr unsigned WS_KV = 0x200000;
constexpr unsigned WS_O = 0xA00000;
constexpr unsigned WS_H2 = 0xE00000;
constexpr unsigned WS_U = 0x1200000;
constexpr unsigned WS_X2 = 0x1600000;
constexpr unsigned WS_DBG = 0x1800000;
constexpr unsigned WS_END = 0x1A00000;
constexpr unsigned L_WIMG = 16384;
constexpr unsigned L_ONES = 131072;
constexpr unsigned L_CSP = 135168;
constexpr unsigned L_CSF = L_CSP + 1536;
constexpr unsigned L_PROG = L_CSP + 1792;
constexpr unsigned L_FILL = L_PROG + 16;
constexpr unsigned L_SST = L_CSP + 2048;
constexpr unsigned L_REG = 139264;
constexpr unsigned L_SCR = 147456;
constexpr unsigned L_TOTAL = 163840;
constexpr int AUX_SC1 = 16;
constexpr unsigned OOB = 0x80000000u;
constexpr unsigned SPIN_CAP = 1u << 16;
constexpr float LN_EPS = 1e-5f;

#define SBAR() __builtin_amdgcn_sched_barrier(0)
#define PIN(x) asm volatile("" : "+v"(x))
#define MFMA(a, b, c) __builtin_amdgcn_mfma_f32_32x32x16_bf16(a, b, c, 0, 0, 0)
#define EX(x) __builtin_amdgcn_exp2f(x)
#define VMWAIT(n) asm volatile("s_waitcnt vmcnt(" #n ")" ::: "memory")
#define BAR() asm volatile("s_waitcnt lgkmcnt(0)\n\ts_barrier" ::: "memory")

template <int N> DI void vm_wait() { asm volatile("s_waitcnt vmcnt(%0)" ::"i"(N) : "memory"); }
template <int N> DI void vm_wait_bar() { asm volatile("s_waitcnt vmcnt(%0) lgkmcnt(0)\n\ts_barrier" ::"i"(N) : "memory"); }
DI rsrc_t mk_rsrc(const void* p, unsigned bytes) { asm volatile("" : "+s"(p)); return __builtin_amdgcn_make_buffer_rsrc((void*)p, 0, bytes, 0x00020000); }
template <int N, int LSTEP, int GSTEP> DI void dma(unsigned lds, unsigned voff, rsrc_t rs, unsigned soff) {
    unsigned t;
    asm volatile("s_mov_b32 m0, %1\n\ts_mov_b32 %0, %4\n\ts_nop 2\n\tbuffer_load_dwordx4 %2, %3, %0 offen lds\n\t"
                 ".rept %5\n\ts_add_u32 m0, m0, %6\n\ts_add_u32 %0, %0, %7\n\tbuffer_load_dwordx4 %2, %3, %0 offen lds\n\t.endr"
                 : "=&s"(t) : "s"(lds), "v"(voff), "s"(rs), "s"(soff), "i"(N - 1), "i"(LSTEP), "i"(GSTEP) : "memory", "scc");
}
#define NTS ""
template <int N, int LSTEP, int GSTEP> DI void dma_sc1(unsigned lds, unsigned voff, rsrc_t rs, unsigned soff) {
    unsigned t;
    asm volatile("s_mov_b32 m0, %1\n\ts_mov_b32 %0, %4\n\ts_nop 2\n\tbuffer_load_dwordx4 %2, %3, %0 offen sc1 " NTS " lds\n\t"
                 ".rept %5\n\ts_add_u32 m0, m0, %6\n\ts_add_u32 %0, %0, %7\n\tbuffer_load_dwordx4 %2, %3, %0 offen sc1 " NTS " lds\n\t.endr"
                 : "=&s"(t) : "s"(lds), "v"(voff), "s"(rs), "s"(soff), "i"(N - 1), "i"(LSTEP), "i"(GSTEP) : "memory", "scc");
}
template <int N, int GSTEP> DI void touch(unsigned pad, unsigned voff, rsrc_t rs, unsigned soff) {
    unsigned t;
    asm volatile("s_mov_b32 m0, %1\n\ts_mov_b32 %0, %4\n\ts_nop 2\n\tbuffer_load_dword %2, %3, %0 offen sc1 lds\n\t"
                 ".rept %5\n\ts_add_u32 %0, %0, %6\n\tbuffer_load_dword %2, %3, %0 offen sc1 lds\n\t.endr"
                 : "=&s"(t) : "s"(pad), "v"(voff), "s"(rs), "s"(soff), "i"(N - 1), "i"(GSTEP) : "memory", "scc");
}
DI bf16x8 ld_frag(rsrc_t rs, unsigned voff, unsigned soff) { return __builtin_bit_cast(bf16x8, __builtin_amdgcn_raw_buffer_load_b128(rs, voff, soff, AUX_SC1 | (NTLD ? 2 : 0))); }
DI void st_frag(u32x4 v, rsrc_t rs, unsigned voff, unsigned soff) {
    __builtin_amdgcn_raw_buffer_store_b128(v, rs, voff, soff, AUX_SC1);
    asm volatile("s_nop 1" ::"v"(v) : "memory");
}
DI bf16x8 lds_frag(lptr p) { return *(LAS const bf16x8*)p; }
DI s16x4 lds_tr(lptr p) { return __builtin_amdgcn_ds_read_tr16_b64_v4i16((LAS s16x4*)p); }
DI bf16x8 tr_frag(lptr p0, lptr p1) {
    const s16x4 a = lds_tr(p0), b = lds_tr(p1);
    return __builtin_bit_cast(bf16x8, __builtin_shufflevector(a, b, 0, 1, 2, 3, 4, 5, 6, 7));
}
DI unsigned pk2(float a, float b) { const f32x2 v = {a, b}; return __builtin_bit_cast(unsigned, __builtin_convertvector(v, bf16x2)); }
DI float bf_lo(unsigned u) { return __uint_as_float(u << 16); }
DI float bf_hi(unsigned u) { return __uint_as_float(u & 0xffff0000u); }
DI float dot2(unsigned a, unsigned b, float c) { return __builtin_amdgcn_fdot2_f32_bf16(__builtin_bit_cast(bf16x2, a), __builtin_bit_cast(bf16x2, b), c, false); }
DI float half_sum2(float a, float b) { const auto r = __builtin_amdgcn_permlane32_swap(__float_as_uint(a), __float_as_uint(b), false, false); return __uint_as_float(r[0]) + __uint_as_float(r[1]); }
DI f32x8 lo8(f32x16 v) { return __builtin_shufflevector(v, v, 0, 1, 2, 3, 4, 5, 6, 7); }
DI f32x8 hi8(f32x16 v) { return __builtin_shufflevector(v, v, 8, 9, 10, 11, 12, 13, 14, 15); }
DI f32x16 smf(bf16x8 a, f32x8 p, f32x16 c, int idx) { return __builtin_amdgcn_smfmac_f32_32x32x32_bf16(a, __builtin_bit_cast(bf16x16, p), c, idx, 0, 0); }
DI int opaque(int v) { asm volatile("" : "+v"(v)); return v; }
DI int sopaque(int v) { asm volatile("" : "+s"(v)); return v; }

#define PSLEEP 0
#define POLL_PAUSE() do { if (PSLEEP) __builtin_amdgcn_s_sleep(PSLEEP); asm volatile("" ::: "memory"); } while (0)
DI unsigned wait_cnt(rsrc_t ws, unsigned soff, unsigned target, int lane) {
    unsigned i;
#pragma unroll 1
    for (i = 0; i < SPIN_CAP; ++i) {
        const unsigned v = __builtin_amdgcn_raw_ptr_buffer_atomic_add_i32(0, ws, lane == 0 ? 0u : OOB, soff, AUX_SC1);
        if ((unsigned)__builtin_amdgcn_readfirstlane(v) >= target) break;
        POLL_PAUSE();
    }
    asm volatile("" ::: "memory");
    return i;
}
template <int NREP> DI void arrive(rsrc_t ws, unsigned soff, int lane) {
    __builtin_amdgcn_raw_ptr_buffer_atomic_add_i32(1, ws, lane < NREP ? lane * 128u : OOB, soff, AUX_SC1);
    asm volatile("" ::: "memory");
}
template <int NREP> DI void publish(rsrc_t ws, unsigned soff, int lane) { VMWAIT(0); arrive<NREP>(ws, soff, lane); }
DI unsigned wait64(rsrc_t ws, unsigned voff, unsigned soff, unsigned tlo, unsigned thi) {
    unsigned i;
#pragma unroll 1
    for (i = 0; i < SPIN_CAP; ++i) {
        const u32x2 v = __builtin_amdgcn_raw_buffer_load_b64(ws, voff, soff, AUX_SC1);
        if (__all(((v[0] ^ tlo) | (v[1] ^ thi)) == 0)) break;
        POLL_PAUSE();
    }
    asm volatile("" ::: "memory");
    return i;
}

#define ISTAMP(i) do { } while (0)
#define ISTPAR
#define NOTE(i, v) (void)(v)
#define STAMP(i) do { } while (0)

DI unsigned wvoff(int lane, unsigned ldw) { return (lane >> 3) * ldw + (((lane & 7) ^ (((lane >> 4) & 1) << 2)) << 4); }
DI void lds_post(lptr p, unsigned v) { *(volatile LAS unsigned*)p = v; }
DI unsigned lds_min2(lptr p) {
    const u32x2 v = *(volatile LAS u32x2*)p;
    const unsigned a = __builtin_amdgcn_readfirstlane(v[0]), b = __builtin_amdgcn_readfirstlane(v[1]);
    return a < b ? a : b;
}
template <int SLEEP> DI void lds_need(lptr p, unsigned& have, unsigned need) {
#pragma unroll 1
    for (unsigned i = 0; have < need && i < (SPIN_CAP << 4); ++i) { have = lds_min2(p); if (SLEEP && have < need) __builtin_amdgcn_s_sleep(SLEEP); }
    asm volatile("" ::: "memory");
}
template <int S0, int S1, int SLOT0, int LDW, int TS, int ib> DI void ring_prefetch(unsigned lb, u32x4 (&rg)[8][4], rsrc_t rw, unsigned voff, unsigned gb, lptr fill, unsigned base ISTPAR) {
#pragma unroll
    for (int s = S0; s < S1; ++s) {
        if (!(s & 1)) ISTAMP(ib + (s >> 1));
        const unsigned so = gb + s * (64 * LDW);
#pragma unroll
        for (int h = 0; h < 2; ++h) {
            if (s < 8) dma<2, 1024, 8 * LDW>(lb + ((s + SLOT0) & 7) * 16384 + h * 2048, voff, rw, so + h * 16 * LDW);
            else {
#pragma unroll
                for (int p = 2 * h; p < 2 * h + 2; ++p)
                    asm volatile("s_nop 4\n\tbuffer_load_dwordx4 %0, %1, %2, %3 offen" : "=v"(rg[s & 7][p]) : "v"(voff), "s"(rw), "s"(so + p * 8 * LDW) : "memory");
            }
            vm_wait<4 * TS>();
        }
        if (s >= TS && s < 8 + TS) lds_post(fill, base + s + 1 - TS);
    }
}
template <int NST, int SLOT0, int ib> DI void ring_refill(lptr lp, u32x4 (&rg)[8][4], lptr prog, lptr fill, unsigned base ISTPAR) {
    unsigned have = 0;
#pragma unroll
    for (int s = 8; s < NST; ++s) {
        lds_need<1>(prog, have, base + s - 7);
        if (s == NST - 2) asm volatile("s_waitcnt vmcnt(0)" : "+v"(rg[s & 7][0]), "+v"(rg[s & 7][1]), "+v"(rg[s & 7][2]), "+v"(rg[s & 7][3]) :: "memory");
        else asm volatile("" : "+v"(rg[s & 7][0]), "+v"(rg[s & 7][1]), "+v"(rg[s & 7][2]), "+v"(rg[s & 7][3]) :: "memory");
#pragma unroll
        for (int p = 0; p < 4; ++p) *(LAS u32x4*)(lp + ((s + SLOT0) & 7) * 16384 + p * 1024) = rg[s & 7][p];
        asm volatile("" ::: "memory");
        lds_post(fill, base + s + 1);
        ISTAMP(ib + s);
    }
}
template <int NST, int SLOT0, bool ACT_A, bool RING, int ib>
DI void gemm_compute(lptr lds, int lane, int kh, int ah, rsrc_t ws, unsigned act_soff, unsigned base, f32x16& acc0, f32x16& acc1 ISTPAR) {
    constexpr int NK = NST * 4, LA = 2;
    const unsigned av = lane * 16;
    ISTAMP(ib);
    bf16x8 f[NK], w0[NK], w1[NK];
#define ALOAD(i) f[i] = ld_frag(ws, av + ((i) & 3) * 1024, act_soff + ((i) >> 2) * 4096)
#pragma unroll
    for (int i = 0; i < AN0; ++i) ALOAD(i);
    SBAR();
    const int hh = lane >> 5, gq = (lane >> 4) & 1, q = (lane >> 2) & 3, p = lane & 3;
    const unsigned a0 = kh * 8192 + (4 * hh + q) * 128 + (((4 * kh + 2 * gq + (p >> 1)) ^ (((q >> 1) & 1) << 2)) << 4) + (p & 1) * 8;
    const lptr b0 = lds + a0, b1 = lds + (a0 ^ 64), b0h = b0 + 65536, b1h = b1 + 65536;
    const lptr fill = lds + L_FILL + kh * 8, prog = lds + L_PROG + kh * 8 + ah * 4;
    unsigned have = 0;
#define WREAD(dst, pl, ph, k) do { const int sl_ = (((k) >> 2) + SLOT0) & 7, o_ = (sl_ & 3) * 16384 + ((k) & 3) * 2048; \
        const lptr p_ = (sl_ & 4) ? ph : pl; dst[k] = tr_frag(p_ + o_, p_ + o_ + 1024); } while (0)
    acc1 = f32x16{};
    ISTAMP(ib + 1);
    if (RING) lds_need<0>(fill, have, base + 1);
    ISTAMP(ib + 2);
#pragma unroll
    for (int k = 0; k < LA; ++k) { WREAD(w1, b1, b1h, k); WREAD(w0, b0, b0h, k); }
    int nl = AN0;
#pragma unroll
    for (int k = 0; k < NK; ++k) {
        if (RING && ((k + LA) & 3) == 0 && k + LA < NK) lds_need<0>(fill, have, base + ((k + LA) >> 2) + 1);
        SBAR();
        acc1 = ACT_A ? MFMA(f[k], w1[k], acc1) : MFMA(w1[k], f[k], acc1);
        SBAR();
        if (k + LA < NK) WREAD(w1, b1, b1h, k + LA);
        if (nl < NK && nl < k + APD) { ALOAD(nl); ++nl; }
        SBAR();
        acc0 = ACT_A ? MFMA(f[k], w0[k], acc0) : MFMA(w0[k], f[k], acc0);
        SBAR();
        if (RING && (k & 3) == 3 && (k >> 2) + 8 < NST) lds_post(prog, base + (k >> 2) + 1);
        if (k + LA < NK) WREAD(w0, b0, b0h, k + LA);
        if (nl < NK && nl < k + 1 + APD) { ALOAD(nl); ++nl; }
        if (k == 0) ISTAMP(ib + 3);
        if ((k & 7) == 7) ISTAMP(ib + 4 + (k >> 3));
    }
    SBAR();
#undef WREAD
#undef ALOAD
}
DI void exchange(lptr lds, int lane, int w, int partner, f32x16& acc0, const f32x16& acc1) {
    LAS f32x4* dst = (LAS f32x4*)(lds + L_SCR + w * 4096 + lane * 16);
#pragma unroll
    for (int i = 0; i < 4; ++i) dst[i * 64] = f32x4{acc1[4 * i], acc1[4 * i + 1], acc1[4 * i + 2], acc1[4 * i + 3]};
    BAR();
    LAS const f32x4* src = (LAS const f32x4*)(lds + L_SCR + partner * 4096 + lane * 16);
#pragma unroll
    for (int i = 0; i < 4; ++i) { const f32x4 v = src[i * 64]; acc0[4 * i] += v[0]; acc0[4 * i + 1] += v[1]; acc0[4 * i + 2] += v[2]; acc0[4 * i + 3] += v[3]; }
}

struct QkvAcc { f32x16 qk, v; };
struct WFrag { bf16x8 qk, v; };
DI void qkv_step(QkvAcc& a, bf16x8 x, const WFrag& f, WFrag& n, bool more, lptr pqk, lptr pv, int o0, int o1) {
    SBAR(); a.qk = MFMA(f.qk, x, a.qk); SBAR();
    if (more) n.qk = tr_frag(pqk + o0, pqk + o1);
    SBAR(); a.v = MFMA(x, f.v, a.v); SBAR();
    if (more) n.v = tr_frag(pv + o0, pv + o1);
}
DI u32x2 row_partial(const unsigned (&x)[8]) {
    float sum = 0.f, sq = 0.f;
#pragma unroll
    for (int i = 0; i < 8; ++i) { const float a = bf_lo(x[i]), b = bf_hi(x[i]); sum += a + b; sq = __builtin_fmaf(a, a, __builtin_fmaf(b, b, sq)); }
    const unsigned t = __float_as_uint(half_sum2(sum, sq));
    const auto sw = __builtin_amdgcn_permlane32_swap(t, t, false, false);
    return u32x2{sw[0], sw[1]};
}
DI f32x4 record_total(const u32x4 (&p)[4]) {
    f32x4 t;
#pragma unroll
    for (int j = 0; j < 4; ++j) {
        const unsigned v = __float_as_uint((__uint_as_float(p[0][j]) + __uint_as_float(p[1][j])) + (__uint_as_float(p[2][j]) + __uint_as_float(p[3][j])));
        const auto a = __builtin_amdgcn_permlane32_swap(v, v, false, false);
        const unsigned u = __float_as_uint(__uint_as_float(a[0]) + __uint_as_float(a[1]));
        const auto b = __builtin_amdgcn_permlane16_swap(u, u, false, false);
        t[j] = __uint_as_float(b[0]) + __uint_as_float(b[1]);
    }
    return t;
}
DI void colsum_step(float& cq, float& cv, const WFrag& f) {
    const u32x4 a = __builtin_bit_cast(u32x4, f.qk), b = __builtin_bit_cast(u32x4, f.v);
#pragma unroll
    for (int i = 0; i < 4; ++i) { cq = dot2(a[i], 0x3f803f80u, cq); cv = dot2(b[i], 0x3f803f80u, cv); }
}
DI void colsum_final(lptr lds, int lane) {
    const int n = lane < 48 ? lane : lane - 16;
    const f32x4 a = *(LAS const f32x4*)(lds + L_CSP + n * 32), b = *(LAS const f32x4*)(lds + L_CSP + n * 32 + 16);
    *(LAS float*)(lds + L_CSF + n * 4) = ((a[0] + a[1]) + (a[2] + a[3])) + ((b[0] + b[1]) + (b[2] + b[3]));
}

#define XE(r) "v_exp_f32 v" #r ", v" #r "\n\t"
#define SMF(d, a, b) "v_smfmac_f32_32x32x32_bf16 v[" d "], v[" a "], v[" b "], v173\n\t"
#define ATT_HEAD XE(66) XE(67) XE(68) XE(69) "v_mfma_f32_32x32x16_bf16 v[80:95], v[160:163], v[156:159], v[0:15]\n\t"
#define ATT_FULL(C0, C1, N0, N1) ATT_HEAD \
    "ds_read_b128 v[160:163], %[kp] offset:%[ko]\n\t" XE(70) XE(71) "ds_read_b128 v[" N0 "], %[vp] offset:%[vo]\n\t" XE(72) XE(73) SMF("32:47", C0, "64:71") \
    "ds_read_b128 v[" N1 "], %[vp] offset:%[vo2]\n\t" XE(74) XE(75) XE(76) XE(77) XE(78) XE(79) XE(80) XE(81) SMF("32:47", C1, "72:79") \
    XE(82) XE(83) XE(84) "s_waitcnt lgkmcnt(0)\n\tv_mfma_f32_32x32x16_bf16 v[64:79], v[160:163], v[152:155], v[48:63]\n\t" \
    XE(85) XE(86) XE(87) XE(88) XE(89) SMF("16:31", C0, "80:87") \
    XE(90) XE(91) XE(92) XE(93) XE(94) XE(95) XE(64) XE(65) SMF("16:31", C1, "88:95")
#define ATT_LAST(C0, C1) ATT_HEAD \
    XE(70) XE(71) XE(72) XE(73) SMF("32:47", C0, "64:71") XE(74) XE(75) XE(76) XE(77) XE(78) XE(79) "s_nop 0\n\t" XE(80) XE(81) SMF("32:47", C1, "72:79") \
    XE(82) XE(83) XE(84) XE(85) XE(86) XE(87) XE(88) XE(89) SMF("16:31", C0, "80:87") \
    XE(90) XE(91) XE(92) XE(93) XE(94) XE(95) "s_nop 1\n\t" SMF("16:31", C1, "88:95") "s_nop 11"
#define ATT_OPS(KO, VO) \
    : "+{v[64:79]}"(sA), "+{v[80:95]}"(sB), "+{v[32:47]}"(o), "+{v[16:31]}"(o2), "+{v[160:163]}"(kf), \
      "+{v[164:167]}"(v0), "+{v[168:171]}"(v1), "+{v[176:179]}"(v2), "+{v[180:183]}"(v3) \
    : "{v[48:63]}"(cneg), "{v[0:15]}"(cneg2), "{v[152:155]}"(qf), "{v[156:159]}"(qf2), "{v173}"(idx), \
      [kp] "v"(kp), [vp] "v"(vp), [ko] "i"(KO), [vo] "i"(VO), [vo2] "i"((VO) + 512) : "memory"
#define ATT_EVEN(KO, VO) asm volatile(ATT_FULL("164:167", "168:171", "176:179", "180:183") ATT_OPS(KO, VO))
#define ATT_ODD(KO, VO) asm volatile(ATT_FULL("176:179", "180:183", "164:167", "168:171") ATT_OPS(KO, VO))
#define ATT_ODD_LAST() asm volatile(ATT_LAST("176:179", "180:183") ATT_OPS(0, 0))
DI f32x16 neg_row_max(f32x16 s) {
    float m = __builtin_fmaxf(s[0], s[1]);
#pragma unroll
    for (int i = 2; i < 16; i += 2) m = __builtin_fmaxf(__builtin_fmaxf(m, s[i]), s[i + 1]);
    const auto sw = __builtin_amdgcn_permlane32_swap(__float_as_uint(m), __float_as_uint(m), false, false);
    m = -__builtin_fmaxf(__uint_as_float(sw[0]), __uint_as_float(sw[1]));
    f32x16 c;
#pragma unroll
    for (int i = 0; i < 16; ++i) c[i] = m;
    PIN(c);
    return c;
}

__global__ __launch_bounds__(512, 2) void fused_fwd(const void* __restrict__ xin, const void* __restrict__ wqkv, const void* __restrict__ wo,
                                                    const void* __restrict__ w1, const unsigned short* __restrict__ b1, const void* __restrict__ w2,
                                                    const unsigned short* __restrict__ b2, unsigned short* __restrict__ out, char* wsp) {
    __shared__ __attribute__((aligned(16))) char lds_raw[L_TOTAL];
    const lptr lds = (lptr)lds_raw;
    const unsigned lds0 = (unsigned)(uintptr_t)lds;
    const int w_ = __builtin_amdgcn_readfirstlane(threadIdx.x >> 6);
#define PHASE_IDS \
    const int lane = opaque(threadIdx.x & 63), l31 = lane & 31, hh = lane >> 5; \
    const int w = sopaque(w_), bid = sopaque(blockIdx.x), g = bid & 7, r = bid >> 3; \
    const int st64 = g * 4 + (r >> 3), dt64 = r & 7; \
    const int dt1 = (g & 1) * 4 + (r & 3), ft1 = (g >> 1) * 7 + (r >> 2); \
    const int own_s32 = st64 * 2 + (w & 1), own_d32 = dt64 * 2 + ((w >> 1) & 1); \
    (void)l31; (void)hh; (void)dt1; (void)ft1; (void)own_s32; (void)own_d32;
    const bool w1_on = (blockIdx.x >> 3) < 28;
    const rsrc_t ws = mk_rsrc(wsp, WS_END);

#define ISTARG(b)
    unsigned xres[8] = {};
    unsigned tag_lo = 0, tag_hi = 0;
    u32x2 nonce = {};
    u32x4 wc[12];
#define UNDEF_WC() do { _Pragma("unroll") for (int i_ = 0; i_ < 12; ++i_) asm volatile("" : "=v"(wc[i_])); } while (0)
    UNDEF_WC();

#pragma unroll 1
    for (int L = 0; L < 2; ++L) {
        bf16x8 qf;
        {
            PHASE_IDS
            const int gq = (lane >> 4) & 1, q = (lane >> 2) & 3, p = lane & 3;
            const unsigned kv_soff = WS_KV + (L * 32 + r) * 131072;
            STAMP(0);
            QkvAcc a; a.qk = f32x16{}; a.v = f32x16{};
            u32x4 sp[4];
            float rstd, mr, csv;
            f32x4 cs[4], rs[4], ms[4];
            asm volatile("" : "=v"(rstd), "=v"(mr), "=v"(rs[0]), "=v"(rs[1]), "=v"(rs[2]), "=v"(rs[3]), "=v"(ms[0]), "=v"(ms[1]), "=v"(ms[2]), "=v"(ms[3]));
#define LN_PREP() do { \
                const f32x4 tot = record_total(sp); \
                const float m0 = tot[0] * (1.f / 512.f), m1 = tot[2] * (1.f / 512.f); \
                const float r0 = __builtin_amdgcn_rsqf(__builtin_fmaxf(tot[1] * (1.f / 512.f) - m0 * m0, 0.f) + LN_EPS); \
                const float r1 = __builtin_amdgcn_rsqf(__builtin_fmaxf(tot[3] * (1.f / 512.f) - m1 * m1, 0.f) + LN_EPS); \
                const lptr sst = lds + L_SST + w * 256; \
                *(LAS f32x2*)(sst + hh * 128 + (lane & 15) * 8) = hh ? f32x2{m0 * r0, m1 * r1} : f32x2{r0, r1}; \
                rstd = *(LAS const float*)(sst + l31 * 4); mr = *(LAS const float*)(sst + 128 + l31 * 4); \
                _Pragma("unroll") for (int i = 0; i < 4; ++i) { \
                    rs[i] = *(LAS const f32x4*)(sst + (8 * i + 4 * hh) * 4); ms[i] = *(LAS const f32x4*)(sst + 128 + (8 * i + 4 * hh) * 4); } \
                SBAR(); } while (0)
#define CS_READ() do { \
                _Pragma("unroll") for (int i = 0; i < 4; ++i) cs[i] = *(LAS const f32x4*)(lds + L_CSF + (8 * i + 4 * hh) * 4); \
                csv = *(LAS const float*)(lds + L_CSF + 128 + (lane & 15) * 4); \
                SBAR(); } while (0)
            const unsigned sp_soff = WS_SP + (L * 64 + g * 8 + w) * 4096;
            if (L == 0) {
                const rsrc_t rx = mk_rsrc(xin, 2048 * 1024), rw = mk_rsrc(wqkv, 512 * 3072);
                unsigned wv[3];
#pragma unroll
                for (int ty = 0; ty < 3; ++ty) {
                    const int o = ty * 1024 + lane * 16, rg = o / 384, rem = o % 384;
                    wv[ty] = (4 * rg + ((rem >> 5) & 3)) * 3072 + (rem >> 7) * 1024 + (rem & 16);
                }
#define WQUART(j) do { if (w >= 4) { _Pragma("unroll") for (int ty = 0; ty < 3; ++ty) \
                       dma<1, 0, 0>(lds0 + L_WIMG + (4 * (j) + w - 4) * 3072 + ty * 1024, wv[ty], rw, (4 * (j) + w - 4) * 98304 + r * 32); } } while (0)
                const unsigned tokb = (g * 256 + w * 32) * 1024, stg = w < 5 ? 65536 + w * 12288 : (w == 5 ? 0 : L_REG + (w - 6) * 12288);
                const unsigned vA = (lane >> 3) * 1024 + (((lane & 7) ^ (lane >> 4)) << 4), vB = vA ^ 64;
#define XCHUNK(c) do { dma<2, 2048, 16384>(lds0 + stg + ((c) % 3) * 4096, vA, rx, tokb + (c) * 128); \
                       dma<2, 2048, 16384>(lds0 + stg + ((c) % 3) * 4096 + 1024, vB, rx, tokb + 8192 + (c) * 128); } while (0)
#define ROLE_WAIT(a, b) do { if (w >= 4) vm_wait<a>(); else vm_wait<b>(); } while (0)
                u32x2 xr[4];
                WQUART(0); XCHUNK(0);
                asm volatile("s_nop 4\n\tbuffer_load_dwordx2 %0, off, %1, 0 sc1" : "=v"(nonce) : "s"(ws) : "memory");
                if (w < 4) {
                    asm volatile("s_nop 4\n\tbuffer_load_dwordx2 %0, %4, %5, %6 offen\n\tbuffer_load_dwordx2 %1, %4, %5, %6 offen offset:16\n\t"
                                 "buffer_load_dwordx2 %2, %4, %5, %6 offen offset:32\n\tbuffer_load_dwordx2 %3, %4, %5, %6 offen offset:48"
                                 : "=&v"(xr[0]), "=&v"(xr[1]), "=&v"(xr[2]), "=&v"(xr[3]) : "v"(l31 * 1024 + hh * 8), "s"(rx), "s"(own_s32 * 32768 + own_d32 * 64) : "memory");
                } else {
#pragma unroll
                    for (int i = 0; i < 4; ++i) asm volatile("" : "=v"(xr[i]));
                }
                XCHUNK(1); WQUART(1); XCHUNK(2);
                if (CODEPF && w == 3) {
                    const char* pc;
                    asm volatile("s_getpc_b64 %0" : "=s"(pc));
                    touch<1, 0>(lds0 + L_SST + w * 256, (lane & 15) * 128, mk_rsrc((const char*)((uintptr_t)pc & ~(uintptr_t)127), CODEPF * 1024), r * 2048);
                }
                const lptr pqk = lds + L_WIMG + hh * 768 + gq * 128 + q * 32 + p * 8, pv = lds + L_WIMG + hh * 768 + 256 + q * 32 + p * 8;
                lptr px[4];
#pragma unroll
                for (int t = 0; t < 4; ++t) px[t] = lds + stg + l31 * 128 + (((2 * t + hh) ^ ((l31 >> 1) & 7)) << 4);
                WFrag wf[2];
                u32x2 fl;
                const unsigned f0r = opaque(WS_F0 + r * 128 + (lane & 7) * 8), f5r = opaque(WS_F5 + (g * 8 + w) * 128 + (lane & 15) * 8), flr = hh ? f0r : f5r;
#pragma unroll
                for (int c = 0; c < 8; ++c) {
                    if (c == 0) ISTAMP(54);
                    if (c <= 5) ROLE_WAIT(11, 8); else if (c == 6) vm_wait<0>();
                    ISTAMP(55 + c);
                    if (c == 0) { BAR(); STAMP(1); wf[0].qk = tr_frag(pqk, pqk + 384); wf[0].v = tr_frag(pv, pv + 384); }
                    bf16x8 xf[4];
#pragma unroll
                    for (int t = 0; t < 4; ++t) xf[t] = lds_frag(px[t] + (c % 3) * 4096);
                    if (c == 5) asm volatile("s_nop 4\n\tbuffer_load_dwordx2 %0, %1, %2, 0 offen sc1" : "=v"(fl) : "v"(flr), "s"(ws) : "memory");
                    if (c + 3 < 8) { asm volatile("s_waitcnt lgkmcnt(0)" : "+v"(xf[0]), "+v"(xf[1]), "+v"(xf[2]), "+v"(xf[3]) :: "memory"); XCHUNK(c + 3); }
                    if (c == 1) WQUART(2);
                    if (c == 3) WQUART(3);
                    if (c == 0) {
                        asm volatile("" : "+v"(nonce), "+v"(xr[0]), "+v"(xr[1]), "+v"(xr[2]), "+v"(xr[3]));
                        unsigned long long z = ((unsigned long long)__builtin_amdgcn_readfirstlane(nonce[1]) << 32) | (unsigned)__builtin_amdgcn_readfirstlane(nonce[0]);
                        z = z * 0x9E3779B97F4A7C15ull + 0x632BE59BD9B4E019ull;
                        z ^= z >> 29; z *= 0xBF58476D1CE4E5B9ull; z ^= z >> 32;
                        tag_lo = (unsigned)z; tag_hi = (unsigned)(z >> 32);
#pragma unroll
                        for (int i = 0; i < 4; ++i) { xres[2 * i] = xr[i][0]; xres[2 * i + 1] = xr[i][1]; }
                        if (w < 4) {
                            __builtin_amdgcn_raw_buffer_store_b64(row_partial(xres), ws, hh ? OOB : l31 * 8u, WS_SP + (own_s32 * 16 + own_d32) * 256, AUX_SC1);
                            __builtin_amdgcn_raw_buffer_store_b64(u32x2{}, ws, l31 * 8 + hh * 0x40000, WS_ST + (own_s32 * 16 + own_d32) * 256, AUX_SC1);
                            const unsigned c1a = opaque(WS_C1 + (r * 8 + g) * 128 + lane * 32768), c3a = opaque(WS_C3 + bid * 256 + (lane - 3) * 128);
                            const unsigned c2a = sopaque(WS_C2 + (((r >> 4) * 64 + g * 8) * 2 + (r & 15)) * 128);
                            const unsigned ca = lane < 2 ? c1a : (lane == 2 ? c2a : c3a);
                            __builtin_amdgcn_raw_buffer_store_b32(0u, ws, (w == 0 && lane < 5) ? ca : OOB, 0, AUX_SC1);
                        }
                    }
                    if (c == 6) {
                        asm volatile("" : "+v"(fl));
                        if (!__all(((fl[0] ^ tag_lo) | (fl[1] ^ tag_hi)) == 0)) wait64(ws, flr, 0, tag_lo, tag_hi);
                        asm volatile("" ::: "memory");
#pragma unroll
                        for (int i = 0; i < 4; ++i) sp[i] = __builtin_amdgcn_raw_buffer_load_b128(ws, lane * 16 + i * 1024, sp_soff, AUX_SC1);
                        SBAR();
                    }
                    if (c == 6) {
                        float cq = 0.f, cv = 0.f;
#pragma unroll
                        for (int j = 0; j < 4; ++j) {
                            const int o = (4 * w + j) * 1536;
                            WFrag e; e.qk = tr_frag(pqk + o, pqk + o + 384); e.v = tr_frag(pv + o, pv + o + 384);
                            colsum_step(cq, cv, e);
                        }
                        *(LAS float*)(lds + L_CSP + (lane < 48 ? lane : lane - 16) * 32 + w * 4) = half_sum2(cq, cv);
                        if (w >= 4) {
#pragma unroll
                            for (int i = 0; i < 12; ++i) wc[i] = *(LAS const u32x4*)(lds + L_WIMG + (w - 4) * 12288 + i * 1024 + lane * 16);
                        }
                    }
                    if (c == 7) { BAR(); if (w == 0) colsum_final(lds, lane); LN_PREP(); }
#pragma unroll
                    for (int t = 0; t < 4; ++t) {
                        const int k = 4 * c + t;
                        if (k == 7) { ROLE_WAIT(15, 63); BAR(); }
                        if (k == 15) { ROLE_WAIT(11, 63); BAR(); }
                        if (k == 23) { ROLE_WAIT(4, 63); BAR(); }
                        qkv_step(a, xf[t], wf[k & 1], wf[(k + 1) & 1], k < 31, pqk, pv, (k + 1) * 1536, (k + 1) * 1536 + 384);
                    }
                    if (c == 1 && w < 4) {
                        VMWAIT(0);
                        const unsigned f5a = sopaque(WS_F5 + own_s32 * 128 + own_d32 * 8), f0a = sopaque(w == 0 ? WS_F0 + r * 128 + g * 8 : OOB);
                        __builtin_amdgcn_raw_buffer_store_b64(u32x2{tag_lo, tag_hi}, ws, lane == 0 ? f5a : (lane == 1 ? f0a : OOB), 0, AUX_SC1);
                    }
                }
                ISTAMP(63);
                BAR();
                CS_READ();
#undef WQUART
#undef ROLE_WAIT
#undef XCHUNK
            } else {
                if (w >= 4) {
#pragma unroll
                    for (int i = 0; i < 12; ++i) *(LAS u32x4*)(lds + L_WIMG + (w - 4) * 12288 + i * 1024 + lane * 16) = wc[i];
                }
                BAR();
                wait_cnt(ws, WS_C5 + ((g * 8 + w) * 2 + (r & 1)) * 128, 16, lane);
                STAMP(1);
                ISTAMP(32);
                bf16x8 xf[32];
                const unsigned x_soff = WS_X2 + (g * 8 + w) * 32768;
#define XLOAD(k) xf[k] = ld_frag(ws, lane * 16 + ((k) & 3) * 1024, x_soff + ((k) >> 2) * 4096)
#pragma unroll
                for (int i = 0; i < 4; ++i) sp[i] = __builtin_amdgcn_raw_buffer_load_b128(ws, lane * 16 + i * 1024, sp_soff, AUX_SC1);
#pragma unroll
                for (int k = 0; k < XN0; ++k) XLOAD(k);
                SBAR();
                ISTAMP(33);
                ISTAMP(34);
                const lptr pv = lds + L_WIMG + hh * 384 + q * 32 + p * 8 + 256, pqk = pv + gq * 128 - 256;
                WFrag wf[2];
                wf[0].qk = tr_frag(pqk, pqk + 768); wf[0].v = tr_frag(pv, pv + 768);
                int nl = XN0;
                if (QPRIO) __builtin_amdgcn_s_setprio(3);
#pragma unroll
                for (int k = 0; k < 32; ++k) {
                    qkv_step(a, xf[k], wf[k & 1], wf[(k + 1) & 1], k < 31, pqk, pv, (k + 1) * 1536, (k + 1) * 1536 + 768);
                    if (nl < 32 && nl < k + XPD) { XLOAD(nl); ++nl; }
                    if (nl < 32 && nl < k + 1 + XPD) { XLOAD(nl); ++nl; }
                    if (QPRIO && k == 11) __builtin_amdgcn_s_setprio(2);
                    if (QPRIO && k == 21) __builtin_amdgcn_s_setprio(1);
                    if (QPRIO && k == 27) __builtin_amdgcn_s_setprio(0);
                    if (k == LNK && w < 4) LN_PREP();
                    if (k == LNK2 && w >= 4) LN_PREP();
                    if (k == 24) CS_READ();
                    if (k == 0) ISTAMP(35);
                    if ((k & 3) == 3) ISTAMP(36 + (k >> 2));
                }
#undef XLOAD
                UNDEF_WC();
            }
            STAMP(2);
            if (APRIO) __builtin_amdgcn_s_setprio(3);
            float qk[16], vv[16];
#pragma unroll
            for (int i = 0; i < 16; ++i) qk[i] = a.qk[i] * rstd - mr * cs[i >> 2][i & 3];
            const u32x4 kpk = {pk2(qk[8], qk[9]), pk2(qk[10], qk[11]), pk2(qk[12], qk[13]), pk2(qk[14], qk[15])};
            const unsigned koff = (hh * 32 + ((l31 & 19) | ((l31 & 4) << 1) | ((l31 & 8) >> 1))) * 16;
            st_frag(kpk, ws, koff, kv_soff + (g * 8 + w) * 2048);
#pragma unroll
            for (int i = 0; i < 16; ++i) vv[i] = a.v[i] * rs[i >> 2][i & 3] - ms[i >> 2][i & 3] * csv;
            u32x4 vpk;
#pragma unroll
            for (int i = 0; i < 4; ++i) { const unsigned lo = pk2(vv[2 * i], vv[2 * i + 1]), hi = pk2(vv[8 + 2 * i], vv[9 + 2 * i]); vpk[i] = gq ? hi : lo; }
            const unsigned voff = gq * 512 + hh * 256 + (lane & 15) * 16;
            st_frag(vpk, ws, voff, kv_soff + (g * 8 + w) * 2048 + 1024);
            constexpr float QS = 0.25f * 1.4426950408889634f;
            const u32x4 qpk = {pk2(qk[0] * QS, qk[1] * QS), pk2(qk[2] * QS, qk[3] * QS), pk2(qk[4] * QS, qk[5] * QS), pk2(qk[6] * QS, qk[7] * QS)};
            qf = __builtin_bit_cast(bf16x8, qpk);
            if (L == 0) {
                if (w == 0) *(LAS unsigned*)(lds + L_PROG + (lane & 7) * 4) = 0u;
                const unsigned one = (lane & 15) ? 0u : 0x3f803f80u;
                *(LAS u32x4*)(lds + L_ONES + w * 512 + hh * 256 + (lane & 15) * 16) = u32x4{one, one, one, one};
            }
            *(LAS u32x4*)(lds + w * 1024 + koff) = kpk;
            *(LAS u32x4*)(lds + 8192 + w * 1024 + voff) = vpk;
            *(LAS u32x4*)(lds + L_REG + w * 3072 + lane * 16) = qpk;
            BAR();
            STAMP(3);
        }
        {
            PHASE_IDS
            const int gq = (lane >> 4) & 1;
            const unsigned kv_soff = WS_KV + (L * 32 + r) * 131072;
            const int idx = opaque(0xDDDD);
            lptr kp = lds + (w & 1) * 4096 + lane * 16;
            lptr vp = lds + (gq ? L_ONES : 8192 + (w & 1) * 4096) + hh * 256 + (lane & 15) * 16;
            const int vinc = gq ? 0 : 16384;
            const bf16x8 qf2 = lds_frag(lds + L_REG + (w ^ 1) * 3072 + lane * 16), k0 = lds_frag(lds + lane * 16);
            bf16x8 kf = lds_frag(kp), v0 = lds_frag(vp), v1 = lds_frag(vp + 512), v2, v3;
            asm volatile("" : "=v"(v2), "=v"(v3));
            f32x16 sA = MFMA(k0, qf, f32x16{}), sB = MFMA(k0, qf2, f32x16{}), o = {}, o2 = {};
            const f32x16 cneg = neg_row_max(sA), cneg2 = neg_row_max(sB);
#define KVCHUNK(j) dma_sc1<2, 8192, 1024>(lds0 + (j) * 16384 + w * 1024, lane * 16, ws, kv_soff + (((g + (j)) & 7) * 8 + w) * 2048)
            sA = MFMA(kf, qf, cneg);
            publish<1>(ws, WS_C1 + ((L * 32 + r) * 8 + w) * 128, lane);
            sA[0] = EX(sA[0]); sA[1] = EX(sA[1]);
#define PRIO(n) do { if (APRIO) __builtin_amdgcn_s_setprio(n); } while (0)
            {
                const unsigned c1o = WS_C1 + ((L * 32 + r) * 8 + w) * 128, pv0 = lane == 0 ? 0u : OOB;
                int pc = 0;
#define POLL_ISSUE() asm volatile("s_nop 4\n\tbuffer_atomic_add %0, %1, %2, %3 offen sc0 sc1" : "+v"(pc) : "v"(pv0), "s"(ws), "s"(c1o) : "memory")
#define POLL_READ() asm volatile("s_waitcnt vmcnt(0)" : "+v"(pc) :: "memory")
                PRIO(2);
                ATT_EVEN(1024, 1024);
                POLL_ISSUE();
                ATT_ODD(2048, 2048);
                STAMP(4);
                POLL_READ();
                if (__builtin_amdgcn_readfirstlane(pc) < 8) wait_cnt(ws, c1o, 8, lane);
                STAMP(5);
                KVCHUNK(1);
                PRIO(1);
                ATT_EVEN(3072, 3072);
                KVCHUNK(2); KVCHUNK(3);
                PRIO(0);
                ATT_ODD_LAST();
                vm_wait_bar<4>();
                STAMP(6);
                kp += 16384; vp += vinc;
                kf = lds_frag(kp); v0 = lds_frag(vp); v1 = lds_frag(vp + 512);
                KVCHUNK(4); KVCHUNK(5);
                sA = MFMA(kf, qf, cneg);
                sA[0] = EX(sA[0]); sA[1] = EX(sA[1]);
            }
#pragma unroll
            for (int c = 1; c < 8; ++c) {
                if (c == 1 || c == 3 || c == 4 || c == 7) PRIO(2);
                ATT_EVEN(1024, 1024);
                if (c == 1 || c == 3 || c == 4) PRIO(1);
                ATT_ODD(2048, 2048);
                if (c == 1 || c == 3 || c == 4) PRIO(0); else if (c == 7) PRIO(1);
                ATT_EVEN(3072, 3072);
                if (c == 1) { vm_wait_bar<4>(); KVCHUNK(6); KVCHUNK(7); }
                if (c == 3) vm_wait_bar<0>();
                if (c == 4) {
                    BAR();
                    dma<8, 1024, 8192>(lds0 + w * 8192, wvoff(lane, 1024), mk_rsrc(wo, 512 * 1024), ((w & 1) * 256 + (w >> 1) * 64) * 1024 + dt64 * 128);
                }
                if (c == WARM1) touch<1, 0>(lds0 + L_SST + w * 256, lane * 3584, mk_rsrc(w1, 2048 * 3584), ((r & 3) * 512 + w * 64) * 3584 + (w1_on ? ft1 : 0) * 128);
                if (c == 2 && L == 0) touch<1, 0>(lds0 + L_SST + w * 256, (lane < 28 ? lane : 0) * 4096, mk_rsrc(w2, 1792 * 4096), (dt64 * 224 + w * 28) * 4096 + st64 * 128);
                if (c < 7) { if (c != 2 && c < 5) PRIO(3); vp += vinc; ATT_ODD(16384, 0); kp += 16384; }
                else { PRIO(0); ATT_ODD_LAST(); }
            }
            STAMP(7);
            VMWAIT(0);
            STAMP(20);
            {
                const lptr mine = lds + L_REG + w * 3072 + lane * 16, other = lds + L_REG + (w ^ 1) * 3072 + lane * 16;
                *(LAS f32x4*)mine = f32x4{o2[0], o2[1], o2[2], o2[3]};
                *(LAS f32x4*)(mine + 1024) = f32x4{o2[4], o2[5], o2[6], o2[7]};
                *(LAS float*)(mine + 2048) = o2[8];
                BAR();
                STAMP(21);
                const f32x4 a0 = *(LAS const f32x4*)other, a1 = *(LAS const f32x4*)(other + 1024);
                o[8] += *(LAS const float*)(other + 2048);
#pragma unroll
                for (int i = 0; i < 4; ++i) { o[i] += a0[i]; o[4 + i] += a1[i]; }
            }
#define O_STORE() do { const float il = __builtin_amdgcn_rcpf(__uint_as_float(__builtin_amdgcn_permlane32_swap(__float_as_uint(o[8]), __float_as_uint(o[8]), false, false)[0])); \
                st_frag(u32x4{pk2(o[0] * il, o[1] * il), pk2(o[2] * il, o[3] * il), pk2(o[4] * il, o[5] * il), pk2(o[6] * il, o[7] * il)}, ws, lane * 16, \
                        WS_O + L * 0x200000 + ((g * 8 + w) * 32 + r) * 1024); SBAR(); } while (0)
            O_STORE();
            STAMP(22);
            float chk = __builtin_fabsf(o[8]);
#pragma unroll
            for (int i = 0; i < 8; ++i) chk += __builtin_fabsf(o[i]);
            if (__builtin_expect(__any(!(chk < 1e36f)), 0)) {
                float m = -__builtin_inff();
                o = f32x16{};
                const u32x4 one4 = {0x3f803f80u, 0x3f803f80u, 0x3f803f80u, 0x3f803f80u};
#pragma unroll 1
                for (int t = 0; t < 64; ++t) {
                    const bf16x8 kk = ld_frag(ws, lane * 16, kv_soff + t * 2048);
                    u32x4 a0 = __builtin_amdgcn_raw_buffer_load_b128(ws, hh * 256 + (lane & 15) * 16, kv_soff + t * 2048 + 1024, AUX_SC1);
                    u32x4 a1 = __builtin_amdgcn_raw_buffer_load_b128(ws, hh * 256 + (lane & 15) * 16, kv_soff + t * 2048 + 1536, AUX_SC1);
                    a0 = gq ? one4 : a0; a1 = gq ? one4 : a1;
                    f32x16 s = MFMA(kk, qf, f32x16{});
                    float tm = m;
#pragma unroll
                    for (int i = 0; i < 16; ++i) tm = __builtin_fmaxf(tm, s[i]);
                    const auto sw = __builtin_amdgcn_permlane32_swap(__float_as_uint(tm), __float_as_uint(tm), false, false);
                    tm = __builtin_fmaxf(__uint_as_float(sw[0]), __uint_as_float(sw[1]));
                    const float al = EX(m - tm);
                    m = tm;
#pragma unroll
                    for (int i = 0; i < 16; ++i) { o[i] *= al; s[i] = EX(s[i] - m); }
                    o = smf(__builtin_bit_cast(bf16x8, a0), lo8(s), o, idx);
                    o = smf(__builtin_bit_cast(bf16x8, a1), hi8(s), o, idx);
                }
                VMWAIT(0);
                O_STORE();
            }
            publish<1>(ws, WS_C2 + ((L * 64 + g * 8 + w) * 2 + (r >> 4)) * 128, lane);
            if (RDBK) touch<1, 0>(lds0 + L_SST + w * 256, (lane & 7) * 128, ws, WS_O + L * 0x200000 + ((g * 8 + w) * 32 + r) * 1024);
            STAMP(8);
        }
        if (w_ < 4) {
            f32x16 acc0, acc1;
            u32x2 bb[4];
            unsigned b2u;
            __builtin_amdgcn_s_setprio(CPRIO);
            {
                PHASE_IDS
                const int kh = w >> 1;
#pragma unroll
                for (int i = 0; i < 4; ++i) bb[i] = *(const u32x2*)(b1 + ((w1_on ? ft1 : 0) * 2 + kh) * 32 + 8 * i + 4 * hh);
                b2u = b2[own_s32 * 32 + l31];
#pragma unroll
                for (int i = 0; i < 8; ++i) { acc0[2 * i] = bf_lo(xres[i]); acc0[2 * i + 1] = bf_hi(xres[i]); }
                PIN(acc0);
                (wait_cnt(ws, WS_C2 + ((L * 64 + own_s32) * 2 + kh) * 128, 16, lane));
                STAMP(9);
                gemm_compute<4, 0, false, false, 48>(lds, lane, kh, 0, ws, WS_O + L * 0x200000 + (own_s32 * 32 + kh * 16) * 1024, 0, acc0, acc1 ISTARG(48));
                STAMP(10);
                exchange(lds, lane, w, w ^ 2, acc0, acc1);
#pragma unroll
                for (int i = 0; i < 8; ++i) xres[i] = pk2(acc0[2 * i], acc0[2 * i + 1]);
                const unsigned st_soff = WS_ST + (L * 64 + own_s32) * 4096, tg = L + 1;
                {
                    const u32x2 rp = row_partial(xres);
                    __builtin_amdgcn_raw_buffer_store_b64(u32x2{rp[0], (rp[1] & ~3u) | tg}, ws, hh ? OOB : l31 * 8u, st_soff + own_d32 * 256, AUX_SC1);
                    if (STCNT) arrive<1>(ws, WS_C6 + (L * 64 + own_s32) * 128, lane);
                }
                STAMP(11);
                const lptr tp = lds + L_SCR + (w ^ 2) * 4096;
                *(LAS u32x4*)(tp + l31 * 64 + hh * 32) = u32x4{xres[0], xres[1], xres[2], xres[3]};
                *(LAS u32x4*)(tp + l31 * 64 + hh * 32 + 16) = u32x4{xres[4], xres[5], xres[6], xres[7]};
                const lptr rp = tp + (4 * hh + ((lane >> 2) & 3)) * 64 + ((lane >> 4) & 1) * 32 + (lane & 3) * 8;
                float xt[16];
#pragma unroll
                for (int t = 0; t < 2; ++t) {
                    const u32x4 v = __builtin_bit_cast(u32x4, tr_frag(rp + t * 1024, rp + t * 1024 + 512));
#pragma unroll
                    for (int i = 0; i < 4; ++i) { xt[8 * t + 2 * i] = bf_lo(v[i]); xt[8 * t + 2 * i + 1] = bf_hi(v[i]); }
                }
#pragma unroll
                for (int i = 0; i < 16; ++i) PIN(xt[i]);
                if (STCNT) NOTE(30, wait_cnt(ws, WS_C6 + (L * 64 + own_s32) * 128, 16, lane)); else VMWAIT(0);
                u32x4 gv[4];
                unsigned it;
#pragma unroll 1
                for (it = 0; it < SPIN_CAP; ++it) {
                    unsigned bad = 0;
#pragma unroll
                    for (int i = 0; i < 4; ++i) gv[i] = __builtin_amdgcn_raw_buffer_load_b128(ws, lane * 16 + i * 1024, st_soff, AUX_SC1);
#pragma unroll
                    for (int i = 0; i < 4; ++i) bad |= (gv[i][1] ^ tg) | (gv[i][3] ^ tg);
                    if (__all((bad & 3) == 0)) break;
                    POLL_PAUSE();
                }
                (void)it;
                STAMP(12);
                {
                    const f32x4 tot = record_total(gv);
                    const float m0 = tot[0] * (1.f / 512.f), m1 = tot[2] * (1.f / 512.f);
                    const float r0 = __builtin_amdgcn_rsqf(__builtin_fmaxf(tot[1] * (1.f / 512.f) - m0 * m0, 0.f) * (1.f + LN_EPS) + LN_EPS * LN_EPS);
                    const float r1 = __builtin_amdgcn_rsqf(__builtin_fmaxf(tot[3] * (1.f / 512.f) - m1 * m1, 0.f) * (1.f + LN_EPS) + LN_EPS * LN_EPS);
                    *(LAS f32x2*)(tp + 2048 + hh * 128 + (lane & 15) * 8) = hh ? f32x2{m0 * r0, m1 * r1} : f32x2{r0, r1};
                }
                const unsigned hv = (hh * 32 + ((l31 & 3) | ((l31 & 12) << 1) | ((l31 & 16) >> 2))) * 16;
#pragma unroll
                for (int t = 0; t < 2; ++t) {
                    u32x4 h;
#pragma unroll
                    for (int j = 0; j < 2; ++j) {
                        const f32x4 sc = *(LAS const f32x4*)(tp + 2048 + (16 * t + 8 * j + 4 * hh) * 4), ms = *(LAS const f32x4*)(tp + 2176 + (16 * t + 8 * j + 4 * hh) * 4);
                        const float* x = xt + 8 * t + 4 * j;
                        h[2 * j] = pk2(x[0] * sc[0] - ms[0], x[1] * sc[1] - ms[1]); h[2 * j + 1] = pk2(x[2] * sc[2] - ms[2], x[3] * sc[3] - ms[3]);
                    }
                    st_frag(h, ws, hv, WS_H2 + L * 0x200000 + (own_d32 * 128 + own_s32 * 2 + t) * 1024);
                }
                STAMP(23);
                publish<2>(ws, WS_C3 + ((L * 16 + own_d32) * 2 + (own_s32 >> 5)) * 256, lane);
                if (L == 0 && bid == 0 && w == 0)
                    __builtin_amdgcn_raw_buffer_store_b64(u32x2{nonce[0] + 1u, nonce[1] + (nonce[0] == 0xffffffffu)}, ws, lane == 0 ? 0u : OOB, WS_NONCE, AUX_SC1);
                STAMP(13);
            }
            if (w1_on) {
                PHASE_IDS
                const int kh = w >> 1, d32 = dt1 * 2 + (w & 1), f32t = ft1 * 2 + kh;
#pragma unroll
                for (int i = 0; i < 16; ++i) { const unsigned bw = bb[i >> 2][(i >> 1) & 1]; acc0[i] = (i & 1) ? bf_hi(bw) : bf_lo(bw); }
                PIN(acc0);
                NOTE(28, wait_cnt(ws, WS_C3 + ((L * 16 + d32) * 2 + kh) * 256 + (g & 2) * 64, 32, lane));
                STAMP(14);
                gemm_compute<16, 4, false, true, 0>(lds, lane, kh, w & 1, ws, WS_H2 + L * 0x200000 + (d32 * 128 + kh * 64) * 1024, L * 32, acc0, acc1 ISTARG(0));
                STAMP(15);
                exchange(lds, lane, w, w ^ 2, acc0, acc1);
                float u[16];
#pragma unroll
                for (int i = 0; i < 16; ++i) {
                    const float z = acc0[i];
                    const float e = EX(z * __builtin_fmaf(z * z, -0.10294324f, -2.30220818f));
                    u[i] = z * __builtin_amdgcn_rcpf(1.f + e);
                }
#pragma unroll
                for (int t = 0; t < 2; ++t)
                    st_frag(u32x4{pk2(u[8 * t], u[8 * t + 1]), pk2(u[8 * t + 2], u[8 * t + 3]), pk2(u[8 * t + 4], u[8 * t + 5]), pk2(u[8 * t + 6], u[8 * t + 7])}, ws, lane * 16,
                            WS_U + L * 0x200000 + (d32 * 112 + f32t * 2 + t) * 1024);
                STAMP(24);
                publish<2>(ws, WS_C4 + ((L * 16 + d32) * 2 + ((f32t + 4) >> 5)) * 256, lane);
                STAMP(16);
            }
            else if (ASSIST) {
                PHASE_IDS
                const int kh = w >> 1, d32 = dt1 * 2 + (w & 1);
                wait_cnt(ws, WS_C3 + ((L * 16 + d32) * 2 + kh) * 256 + (g & 2) * 64, 32, lane);
                touch<8 - ASSIST, 8192>(lds0 + L_SST + w * 256, lane * 128, ws, WS_H2 + L * 0x200000 + (d32 * 128 + kh * 64 + 8 * ASSIST) * 1024);
            }
            {
                PHASE_IDS
                const int kh = w & 1;
                const float bias = __uint_as_float(b2u << 16);
#pragma unroll
                for (int i = 0; i < 8; ++i) { acc0[2 * i] = bf_lo(xres[i]) + bias; acc0[2 * i + 1] = bf_hi(xres[i]) + bias; }
                PIN(acc0);
                NOTE(29, wait_cnt(ws, WS_C4 + ((L * 16 + own_d32) * 2 + kh) * 256 + (g & 1) * 128, 28, lane));
                STAMP(17);
                gemm_compute<14, 4, true, true, 16>(lds, lane, kh, w >> 1, ws, WS_U + L * 0x200000 + (own_d32 * 112 + kh * 56) * 1024, L * 32 + 16, acc0, acc1 ISTARG(16));
                STAMP(18);
                exchange(lds, lane, w, w ^ 1, acc0, acc1);
#pragma unroll
                for (int i = 0; i < 8; ++i) xres[i] = pk2(acc0[2 * i], acc0[2 * i + 1]);
                if (L == 0) {
#pragma unroll
                    for (int t = 0; t < 2; ++t)
                        st_frag(u32x4{xres[4 * t], xres[4 * t + 1], xres[4 * t + 2], xres[4 * t + 3]}, ws, lane * 16, WS_X2 + (own_s32 * 32 + own_d32 * 2 + t) * 1024);
                    __builtin_amdgcn_raw_buffer_store_b64(row_partial(xres), ws, hh ? OOB : l31 * 8u, WS_SP + ((64 + own_s32) * 16 + own_d32) * 256, AUX_SC1);
                    STAMP(25);
                    publish<2>(ws, WS_C5 + own_s32 * 256, lane);
                    if (RDBK) {
                        const unsigned xa = opaque(WS_X2 + (own_s32 * 32 + own_d32 * 2) * 1024 + (lane & 15) * 128), sa = opaque(WS_SP + ((64 + own_s32) * 16 + own_d32) * 256 + (lane & 1) * 128);
                        touch<1, 0>(lds0 + L_SST + w * 256, hh ? sa : xa, ws, 0);
                    }
                } else {
                    char* op = (char*)out + (own_s32 * 32 + l31) * 1024 + own_d32 * 64 + hh * 16;
#pragma unroll
                    for (int t = 0; t < 2; ++t) {
                        const auto s0 = __builtin_amdgcn_permlane32_swap(xres[4 * t], xres[4 * t + 2], false, false);
                        const auto s1 = __builtin_amdgcn_permlane32_swap(xres[4 * t + 1], xres[4 * t + 3], false, false);
                        if (OUTAUX < 0) *(u32x4*)(op + t * 32) = u32x4{s0[0], s1[0], s0[1], s1[1]};
                        else {
                            const u32x4 v = {s0[0], s1[0], s0[1], s1[1]};
                            __builtin_amdgcn_raw_buffer_store_b128(v, mk_rsrc(out, 2048 * 1024), l31 * 1024 + hh * 16 + t * 32, own_s32 * 32768 + own_d32 * 64, OUTAUX < 0 ? 0 : OUTAUX);
                            asm volatile("s_nop 1" ::"v"(v) : "memory");
                        }
                    }
                }
                STAMP(19);
            }
            __builtin_amdgcn_s_setprio(0);
            UNDEF_WC();
        } else {
            PHASE_IDS
            const int lw = w - 4;
            const rsrc_t r1 = mk_rsrc(w1, 2048 * 3584), r2 = mk_rsrc(w2, 1792 * 4096);
            const unsigned v1o = wvoff(lane, 3584), v2o = wvoff(lane, 4096), lb = lds0 + lw * 4096;
            const unsigned g1 = ((lw >> 1) * 1024 + (lw & 1) * 32) * 3584 + ft1 * 128, g2 = ((lw >> 1) * 896 + (lw & 1) * 32) * 4096 + st64 * 128;
            const lptr lp = lds + lw * 4096 + lane * 16, fill = lds + L_FILL + lw * 4, prog = lds + L_PROG + (lw >> 1) * 8;
            u32x4 rg[8][4];
#define W2WARM() do { if (WARM2) touch<1, 0>(lds0 + L_SST + w * 256, (lane < 56 ? lane : 0) * 4096, r2, (dt64 * 224 + lw * 56) * 4096 + st64 * 128); } while (0)
            if (w1_on) {
                ring_prefetch<0, 4, 4, 3584, TS1, 0>(lb, rg, r1, v1o, g1, fill, L * 32 ISTARG(0));
                BAR();
                ring_prefetch<4, 16, 4, 3584, TS1, 0>(lb, rg, r1, v1o, g1, fill, L * 32 ISTARG(0));
                W2WARM();
                ring_refill<16, 4, 0>(lp, rg, prog, fill, L * 32 ISTARG(0));
            }
            else W2WARM();
            BAR();
            ring_prefetch<0, 14, 4, 4096, TS2, 16>(lb, rg, r2, v2o, g2, fill, L * 32 + 16 ISTARG(16));
            ring_refill<14, 4, 16>(lp, rg, prog, fill, L * 32 + 16 ISTARG(16));
            BAR();
        }
    }
}

extern "C" void kernel_launch(void* const* d_in, const int* in_sizes, int n_in, void* d_out, int out_size, void* d_ws, size_t ws_size,
                              hipStream_t stream) {
    fused_fwd<<<256, 512, 0, stream>>>(d_in[0], d_in[1], d_in[2], d_in[4], (const unsigned short*)d_in[5], d_in[6], (const unsigned short*)d_in[7],
                                       (unsigned short*)d_out, (char*)d_ws);
}
```

```cpp
#include <hip/hip_runtime.h>
#include <hip/hip_bf16.h>
#include <stdint.h>

#define WARM1 5
#define WARM2 1
#define AN0 6
#define APD 12
#define XN0 6
#define XPD 12
#define CPRIO 2
#define APRIO 1
#define NTLD 0
#define TS1 1
#define TS2 2
#define RDBK 1
#define ASSIST 0
#define QPRIO 1
#define STCNT 1
#define OUTAUX -1
#define CODEPF 0
#define TLBW 0
#define LNK2 4
#define LNK 12
#define INNER 15
#define STAMPS 0

typedef __attribute__((ext_vector_type(16))) float f32x16;
typedef __attribute__((ext_vector_type(8))) float f32x8;
typedef __attribute__((ext_vector_type(4))) float f32x4;
typedef __attribute__((ext_vector_type(2))) float f32x2;
typedef __attribute__((ext_vector_type(16))) __bf16 bf16x16;
typedef __attribute__((ext_vector_type(8))) __bf16 bf16x8;
typedef __attribute__((ext_vector_type(2))) __bf16 bf16x2;
typedef __attribute__((ext_vector_type(4))) unsigned u32x4;
typedef __attribute__((ext_vector_type(2))) unsigned u32x2;
typedef __attribute__((ext_vector_type(4))) short s16x4;
typedef __amdgpu_buffer_rsrc_t rsrc_t;
#define LAS __attribute__((address_space(3)))
typedef LAS char* lptr;
#define DI __device__ __forceinline__

constexpr unsigned WS_NONCE = 0x0;
constexpr unsigned WS_F0 = 0x1000;
constexpr unsigned WS_F5 = 0x2000;
constexpr unsigned WS_C1 = 0x10000;
constexpr unsigned WS_C2 = 0x20000;
constexpr unsigned WS_C3 = 0x28000;
constexpr unsigned WS_C4 = 0x2C000;
constexpr unsigned WS_C5 = 0x30000;
constexpr unsigned WS_C6 = 0x34000;
constexpr unsigned WS_ST = 0x40000;
constexpr unsigned WS_SP = 0x140000;
constexpr unsigned WS_KV = 0x200000;
constexpr unsigned WS_O = 0xA00000;
constexpr unsigned WS_H2 = 0xE00000;
constexpr unsigned WS_U = 0x1200000;
constexpr unsigned WS_X2 = 0x1600000;
constexpr unsigned WS_DBG = 0x1800000;
constexpr unsigned WS_END = 0x1A00000;
constexpr unsigned L_WIMG = 16384;
constexpr unsigned L_ONES = 131072;
constexpr unsigned L_CSP = 135168;
constexpr unsigned L_CSF = L_CSP + 1536;
constexpr unsigned L_PROG = L_CSP + 1792;
constexpr unsigned L_FILL = L_PROG + 16;
constexpr unsigned L_SST = L_CSP + 2048;
constexpr unsigned L_REG = 139264;
constexpr unsigned L_SCR = 147456;
constexpr unsigned L_TOTAL = 163840;
constexpr int AUX_SC1 = 16;
constexpr unsigned OOB = 0x80000000u;
constexpr unsigned SPIN_CAP = 1u << 16;
constexpr float LN_EPS = 1e-5f;

#define SBAR() __builtin_amdgcn_sched_barrier(0)
#define PIN(x) asm volatile("" : "+v"(x))
#define MFMA(a, b, c) __builtin_amdgcn_mfma_f32_32x32x16_bf16(a, b, c, 0, 0, 0)
#define EX(x) __builtin_amdgcn_exp2f(x)
#define VMWAIT(n) asm volatile("s_waitcnt vmcnt(" #n ")" ::: "memory")
#define BAR() asm volatile("s_waitcnt lgkmcnt(0)\n\ts_barrier" ::: "memory")

template <int N> DI void vm_wait() { asm volatile("s_waitcnt vmcnt(%0)" ::"i"(N) : "memory"); }
template <int N> DI void vm_wait_bar() { asm volatile("s_waitcnt vmcnt(%0) lgkmcnt(0)\n\ts_barrier" ::"i"(N) : "memory"); }
DI rsrc_t mk_rsrc(const void* p, unsigned bytes) { asm volatile("" : "+s"(p)); return __builtin_amdgcn_make_buffer_rsrc((void*)p, 0, bytes, 0x00020000); }
template <int N, int LSTEP, int GSTEP> DI void dma(unsigned lds, unsigned voff, rsrc_t rs, unsigned soff) {
    unsigned t;
    asm volatile("s_mov_b32 m0, %1\n\ts_mov_b32 %0, %4\n\ts_nop 2\n\tbuffer_load_dwordx4 %2, %3, %0 offen lds\n\t"
                 ".rept %5\n\ts_add_u32 m0, m0, %6\n\ts_add_u32 %0, %0, %7\n\tbuffer_load_dwordx4 %2, %3, %0 offen lds\n\t.endr"
                 : "=&s"(t) : "s"(lds), "v"(voff), "s"(rs), "s"(soff), "i"(N - 1), "i"(LSTEP), "i"(GSTEP) : "memory", "scc");
}
#define NTS ""
template <int N, int LSTEP, int GSTEP> DI void dma_sc1(unsigned lds, unsigned voff, rsrc_t rs, unsigned soff) {
    unsigned t;
    asm volatile("s_mov_b32 m0, %1\n\ts_mov_b32 %0, %4\n\ts_nop 2\n\tbuffer_load_dwordx4 %2, %3, %0 offen sc1 " NTS " lds\n\t"
                 ".rept %5\n\ts_add_u32 m0, m0, %6\n\ts_add_u32 %0, %0, %7\n\tbuffer_load_dwordx4 %2, %3, %0 offen sc1 " NTS " lds\n\t.endr"
                 : "=&s"(t) : "s"(lds), "v"(voff), "s"(rs), "s"(soff), "i"(N - 1), "i"(LSTEP), "i"(GSTEP) : "memory", "scc");
}
template <int N, int GSTEP> DI void touch(unsigned pad, unsigned voff, rsrc_t rs, unsigned soff) {
    unsigned t;
    asm volatile("s_mov_b32 m0, %1\n\ts_mov_b32 %0, %4\n\ts_nop 2\n\tbuffer_load_dword %2, %3, %0 offen sc1 lds\n\t"
                 ".rept %5\n\ts_add_u32 %0, %0, %6\n\tbuffer_load_dword %2, %3, %0 offen sc1 lds\n\t.endr"
                 : "=&s"(t) : "s"(pad), "v"(voff), "s"(rs), "s"(soff), "i"(N - 1), "i"(GSTEP) : "memory", "scc");
}
DI bf16x8 ld_frag(rsrc_t rs, unsigned voff, unsigned soff) { return __builtin_bit_cast(bf16x8, __builtin_amdgcn_raw_buffer_load_b128(rs, voff, soff, AUX_SC1 | (NTLD ? 2 : 0))); }
DI void st_frag(u32x4 v, rsrc_t rs, unsigned voff, unsigned soff) {
    __builtin_amdgcn_raw_buffer_store_b128(v, rs, voff, soff, AUX_SC1);
    asm volatile("s_nop 1" ::"v"(v) : "memory");
}
DI bf16x8 lds_frag(lptr p) { return *(LAS const bf16x8*)p; }
DI s16x4 lds_tr(lptr p) { return __builtin_amdgcn_ds_read_tr16_b64_v4i16((LAS s16x4*)p); }
DI bf16x8 tr_frag(lptr p0, lptr p1) {
    const s16x4 a = lds_tr(p0), b = lds_tr(p1);
    return __builtin_bit_cast(bf16x8, __builtin_shufflevector(a, b, 0, 1, 2, 3, 4, 5, 6, 7));
}
DI unsigned pk2(float a, float b) { const f32x2 v = {a, b}; return __builtin_bit_cast(unsigned, __builtin_convertvector(v, bf16x2)); }
DI float bf_lo(unsigned u) { return __uint_as_float(u << 16); }
DI float bf_hi(unsigned u) { return __uint_as_float(u & 0xffff0000u); }
DI float dot2(unsigned a, unsigned b, float c) { return __builtin_amdgcn_fdot2_f32_bf16(__builtin_bit_cast(bf16x2, a), __builtin_bit_cast(bf16x2, b), c, false); }
DI float half_sum2(float a, float b) { const auto r = __builtin_amdgcn_permlane32_swap(__float_as_uint(a), __float_as_uint(b), false, false); return __uint_as_float(r[0]) + __uint_as_float(r[1]); }
DI f32x8 lo8(f32x16 v) { return __builtin_shufflevector(v, v, 0, 1, 2, 3, 4, 5, 6, 7); }
DI f32x8 hi8(f32x16 v) { return __builtin_shufflevector(v, v, 8, 9, 10, 11, 12, 13, 14, 15); }
DI f32x16 smf(bf16x8 a, f32x8 p, f32x16 c, int idx) { return __builtin_amdgcn_smfmac_f32_32x32x32_bf16(a, __builtin_bit_cast(bf16x16, p), c, idx, 0, 0); }
DI int opaque(int v) { asm volatile("" : "+v"(v)); return v; }
DI int sopaque(int v) { asm volatile("" : "+s"(v)); return v; }

#define PSLEEP 0
#define POLL_PAUSE() do { if (PSLEEP) __builtin_amdgcn_s_sleep(PSLEEP); asm volatile("" ::: "memory"); } while (0)
DI unsigned wait_cnt(rsrc_t ws, unsigned soff, unsigned target, int lane) {
    unsigned i;
#pragma unroll 1
    for (i = 0; i < SPIN_CAP; ++i) {
        const unsigned v = __builtin_amdgcn_raw_ptr_buffer_atomic_add_i32(0, ws, lane == 0 ? 0u : OOB, soff, AUX_SC1);
        if ((unsigned)__builtin_amdgcn_readfirstlane(v) >= target) break;
        POLL_PAUSE();
    }
    asm volatile("" ::: "memory");
    return i;
}
template <int NREP> DI void arrive(rsrc_t ws, unsigned soff, int lane) {
    __builtin_amdgcn_raw_ptr_buffer_atomic_add_i32(1, ws, lane < NREP ? lane * 128u : OOB, soff, AUX_SC1);
    asm volatile("" ::: "memory");
}
template <int NREP> DI void publish(rsrc_t ws, unsigned soff, int lane) { VMWAIT(0); arrive<NREP>(ws, soff, lane); }
DI unsigned wait64(rsrc_t ws, unsigned voff, unsigned soff, unsigned tlo, unsigned thi) {
    unsigned i;
#pragma unroll 1
    for (i = 0; i < SPIN_CAP; ++i) {
        const u32x2 v = __builtin_amdgcn_raw_buffer_load_b64(ws, voff, soff, AUX_SC1);
        if (__all(((v[0] ^ tlo) | (v[1] ^ thi)) == 0)) break;
        POLL_PAUSE();
    }
    asm volatile("" ::: "memory");
    return i;
}

#define ISTAMP(i) do { } while (0)
#define ISTPAR
#define NOTE(i, v) (void)(v)
#define STAMP(i) do { } while (0)

DI unsigned wvoff(int lane, unsigned ldw) { return (lane >> 3) * ldw + (((lane & 7) ^ (((lane >> 4) & 1) << 2)) << 4); }
DI void lds_post(lptr p, unsigned v) { *(volatile LAS unsigned*)p = v; }
DI unsigned lds_min2(lptr p) {
    const u32x2 v = *(volatile LAS u32x2*)p;
    const unsigned a = __builtin_amdgcn_readfirstlane(v[0]), b = __builtin_amdgcn_readfirstlane(v[1]);
    return a < b ? a : b;
}
template <int SLEEP> DI void lds_need(lptr p, unsigned& have, unsigned need) {
#pragma unroll 1
    for (unsigned i = 0; have < need && i < (SPIN_CAP << 4); ++i) { have = lds_min2(p); if (SLEEP && have < need) __builtin_amdgcn_s_sleep(SLEEP); }
    asm volatile("" ::: "memory");
}
template <int S0, int S1, int SLOT0, int LDW, int TS, int ib> DI void ring_prefetch(unsigned lb, u32x4 (&rg)[8][4], rsrc_t rw, unsigned voff, unsigned gb, lptr fill, unsigned base ISTPAR) {
#pragma unroll
    for (int s = S0; s < S1; ++s) {
        if (!(s & 1)) ISTAMP(ib + (s >> 1));
        const unsigned so = gb + s * (64 * LDW);
#pragma unroll
        for (int h = 0; h < 2; ++h) {
            if (s < 8) dma<2, 1024, 8 * LDW>(lb + ((s + SLOT0) & 7) * 16384 + h * 2048, voff, rw, so + h * 16 * LDW);
            else {
#pragma unroll
                for (int p = 2 * h; p < 2 * h + 2; ++p)
                    asm volatile("s_nop 4\n\tbuffer_load_dwordx4 %0, %1, %2, %3 offen" : "=v"(rg[s & 7][p]) : "v"(voff), "s"(rw), "s"(so + p * 8 * LDW) : "memory");
            }
            vm_wait<4 * TS>();
        }
        if (s >= TS && s < 8 + TS) lds_post(fill, base + s + 1 - TS);
    }
}
template <int NST, int SLOT0, int ib> DI void ring_refill(lptr lp, u32x4 (&rg)[8][4], lptr prog, lptr fill, unsigned base ISTPAR) {
    unsigned have = 0;
#pragma unroll
    for (int s = 8; s < NST; ++s) {
        lds_need<1>(prog, have, base + s - 7);
        if (s == NST - 2) asm volatile("s_waitcnt vmcnt(0)" : "+v"(rg[s & 7][0]), "+v"(rg[s & 7][1]), "+v"(rg[s & 7][2]), "+v"(rg[s & 7][3]) :: "memory");
        else asm volatile("" : "+v"(rg[s & 7][0]), "+v"(rg[s & 7][1]), "+v"(rg[s & 7][2]), "+v"(rg[s & 7][3]) :: "memory");
#pragma unroll
        for (int p = 0; p < 4; ++p) *(LAS u32x4*)(lp + ((s + SLOT0) & 7) * 16384 + p * 1024) = rg[s & 7][p];
        asm volatile("" ::: "memory");
        lds_post(fill, base + s + 1);
        ISTAMP(ib + s);
    }
}
template <int NST, int SLOT0, bool ACT_A, bool RING, int ib>
DI void gemm_compute(lptr lds, int lane, int kh, int ah, rsrc_t ws, unsigned act_soff, unsigned base, f32x16& acc0, f32x16& acc1 ISTPAR) {
    constexpr int NK = NST * 4, LA = 2;
    const unsigned av = lane * 16;
    ISTAMP(ib);
    bf16x8 f[NK], w0[NK], w1[NK];
#define ALOAD(i) f[i] = ld_frag(ws, av + ((i) & 3) * 1024, act_soff + ((i) >> 2) * 4096)
#pragma unroll
    for (int i = 0; i < AN0; ++i) ALOAD(i);
    SBAR();
    const int hh = lane >> 5, gq = (lane >> 4) & 1, q = (lane >> 2) & 3, p = lane & 3;
    const unsigned a0 = kh * 8192 + (4 * hh + q) * 128 + (((4 * kh + 2 * gq + (p >> 1)) ^ (((q >> 1) & 1) << 2)) << 4) + (p & 1) * 8;
    const lptr b0 = lds + a0, b1 = lds + (a0 ^ 64), b0h = b0 + 65536, b1h = b1 + 65536;
    const lptr fill = lds + L_FILL + kh * 8, prog = lds + L_PROG + kh * 8 + ah * 4;
    unsigned have = 0;
#define WREAD(dst, pl, ph, k) do { const int sl_ = (((k) >> 2) + SLOT0) & 7, o_ = (sl_ & 3) * 16384 + ((k) & 3) * 2048; \
        const lptr p_ = (sl_ & 4) ? ph : pl; dst[k] = tr_frag(p_ + o_, p_ + o_ + 1024); } while (0)
    acc1 = f32x16{};
    ISTAMP(ib + 1);
    if (RING) lds_need<0>(fill, have, base + 1);
    ISTAMP(ib + 2);
#pragma unroll
    for (int k = 0; k < LA; ++k) { WREAD(w1, b1, b1h, k); WREAD(w0, b0, b0h, k); }
    int nl = AN0;
#pragma unroll
    for (int k = 0; k < NK; ++k) {
        if (RING && ((k + LA) & 3) == 0 && k + LA < NK) lds_need<0>(fill, have, base + ((k + LA) >> 2) + 1);
        SBAR();
        acc1 = ACT_A ? MFMA(f[k], w1[k], acc1) : MFMA(w1[k], f[k], acc1);
        SBAR();
        if (k + LA < NK) WREAD(w1, b1, b1h, k + LA);
        if (nl < NK && nl < k + APD) { ALOAD(nl); ++nl; }
        SBAR();
        acc0 = ACT_A ? MFMA(f[k], w0[k], acc0) : MFMA(w0[k], f[k], acc0);
        SBAR();
        if (RING && (k & 3) == 3 && (k >> 2) + 8 < NST) lds_post(prog, base + (k >> 2) + 1);
        if (k + LA < NK) WREAD(w0, b0, b0h, k + LA);
        if (nl < NK && nl < k + 1 + APD) { ALOAD(nl); ++nl; }
        if (k == 0) ISTAMP(ib + 3);
        if ((k & 7) == 7) ISTAMP(ib + 4 + (k >> 3));
    }
    SBAR();
#undef WREAD
#undef ALOAD
}
DI void exchange(lptr lds, int lane, int w, int partner, f32x16& acc0, const f32x16& acc1) {
    LAS f32x4* dst = (LAS f32x4*)(lds + L_SCR + w * 4096 + lane * 16);
#pragma unroll
    for (int i = 0; i < 4; ++i) dst[i * 64] = f32x4{acc1[4 * i], acc1[4 * i + 1], acc1[4 * i + 2], acc1[4 * i + 3]};
    BAR();
    LAS const f32x4* src = (LAS const f32x4*)(lds + L_SCR + partner * 4096 + lane * 16);
#pragma unroll
    for (int i = 0; i < 4; ++i) { const f32x4 v = src[i * 64]; acc0[4 * i] += v[0]; acc0[4 * i + 1] += v[1]; acc0[4 * i + 2] += v[2]; acc0[4 * i + 3] += v[3]; }
}

struct QkvAcc { f32x16 qk, v; };
struct WFrag { bf16x8 qk, v; };
DI void qkv_step(QkvAcc& a, bf16x8 x, const WFrag& f, WFrag& n, bool more, lptr pqk, lptr pv, int o0, int o1) {
    SBAR(); a.qk = MFMA(f.qk, x, a.qk); SBAR();
    if (more) n.qk = tr_frag(pqk + o0, pqk + o1);
    SBAR(); a.v = MFMA(x, f.v, a.v); SBAR();
    if (more) n.v = tr_frag(pv + o0, pv + o1);
}
DI u32x2 row_partial(const unsigned (&x)[8]) {
    float sum = 0.f, sq = 0.f;
#pragma unroll
    for (int i = 0; i < 8; ++i) { const float a = bf_lo(x[i]), b = bf_hi(x[i]); sum += a + b; sq = __builtin_fmaf(a, a, __builtin_fmaf(b, b, sq)); }
    const unsigned t = __float_as_uint(half_sum2(sum, sq));
    const auto sw = __builtin_amdgcn_permlane32_swap(t, t, false, false);
    return u32x2{sw[0], sw[1]};
}
DI f32x4 record_total(const u32x4 (&p)[4]) {
    f32x4 t;
#pragma unroll
    for (int j = 0; j < 4; ++j) {
        const unsigned v = __float_as_uint((__uint_as_float(p[0][j]) + __uint_as_float(p[1][j])) + (__uint_as_float(p[2][j]) + __uint_as_float(p[3][j])));
        const auto a = __builtin_amdgcn_permlane32_swap(v, v, false, false);
        const unsigned u = __float_as_uint(__uint_as_float(a[0]) + __uint_as_float(a[1]));
        const auto b = __builtin_amdgcn_permlane16_swap(u, u, false, false);
        t[j] = __uint_as_float(b[0]) + __uint_as_float(b[1]);
    }
    return t;
}
DI void colsum_step(float& cq, float& cv, const WFrag& f) {
    const u32x4 a = __builtin_bit_cast(u32x4, f.qk), b = __builtin_bit_cast(u32x4, f.v);
#pragma unroll
    for (int i = 0; i < 4; ++i) { cq = dot2(a[i], 0x3f803f80u, cq); cv = dot2(b[i], 0x3f803f80u, cv); }
}
DI void colsum_final(lptr lds, int lane) {
    const int n = lane < 48 ? lane : lane - 16;
    const f32x4 a = *(LAS const f32x4*)(lds + L_CSP + n * 32), b = *(LAS const f32x4*)(lds + L_CSP + n * 32 + 16);
    *(LAS float*)(lds + L_CSF + n * 4) = ((a[0] + a[1]) + (a[2] + a[3])) + ((b[0] + b[1]) + (b[2] + b[3]));
}

#define XE(r) "v_exp_f32 v" #r ", v" #r "\n\t"
#define SMF(d, a, b) "v_smfmac_f32_32x32x32_bf16 v[" d "], v[" a "], v[" b "], v173\n\t"
#define ATT_HEAD XE(66) XE(67) XE(68) XE(69) "v_mfma_f32_32x32x16_bf16 v[80:95], v[160:163], v[156:159], v[0:15]\n\t"
#define ATT_FULL(C0, C1, N0, N1) ATT_HEAD \
    "ds_read_b128 v[160:163], %[kp] offset:%[ko]\n\t" XE(70) XE(71) "ds_read_b128 v[" N0 "], %[vp] offset:%[vo]\n\t" XE(72) XE(73) SMF("32:47", C0, "64:71") \
    "ds_read_b128 v[" N1 "], %[vp] offset:%[vo2]\n\t" XE(74) XE(75) XE(76) XE(77) XE(78) XE(79) XE(80) XE(81) SMF("32:47", C1, "72:79") \
    XE(82) XE(83) XE(84) "s_waitcnt lgkmcnt(0)\n\tv_mfma_f32_32x32x16_bf16 v[64:79], v[160:163], v[152:155], v[48:63]\n\t" \
    XE(85) XE(86) XE(87) XE(88) XE(89) SMF("16:31", C0, "80:87") \
    XE(90) XE(91) XE(92) XE(93) XE(94) XE(95) XE(64) XE(65) SMF("16:31", C1, "88:95")
#define ATT_LAST(C0, C1) ATT_HEAD \
    XE(70) XE(71) XE(72) XE(73) SMF("32:47", C0, "64:71") XE(74) XE(75) XE(76) XE(77) XE(78) XE(79) "s_nop 0\n\t" XE(80) XE(81) SMF("32:47", C1, "72:79") \
    XE(82) XE(83) XE(84) XE(85) XE(86) XE(87) XE(88) XE(89) SMF("16:31", C0, "80:87") \
    XE(90) XE(91) XE(92) XE(93) XE(94) XE(95) "s_nop 1\n\t" SMF("16:31", C1, "88:95") "s_nop 11"
#define ATT_OPS(KO, VO) \
    : "+{v[64:79]}"(sA), "+{v[80:95]}"(sB), "+{v[32:47]}"(o), "+{v[16:31]}"(o2), "+{v[160:163]}"(kf), \
      "+{v[164:167]}"(v0), "+{v[168:171]}"(v1), "+{v[176:179]}"(v2), "+{v[180:183]}"(v3) \
    : "{v[48:63]}"(cneg), "{v[0:15]}"(cneg2), "{v[152:155]}"(qf), "{v[156:159]}"(qf2), "{v173}"(idx), \
      [kp] "v"(kp), [vp] "v"(vp), [ko] "i"(KO), [vo] "i"(VO), [vo2] "i"((VO) + 512) : "memory"
#define ATT_EVEN(KO, VO) asm volatile(ATT_FULL("164:167", "168:171", "176:179", "180:183") ATT_OPS(KO, VO))
#define ATT_ODD(KO, VO) asm volatile(ATT_FULL("176:179", "180:183", "164:167", "168:171") ATT_OPS(KO, VO))
#define ATT_ODD_LAST() asm volatile(ATT_LAST("176:179", "180:183") ATT_OPS(0, 0))
DI f32x16 neg_row_max(f32x16 s) {
    float m = __builtin_fmaxf(s[0], s[1]);
#pragma unroll
    for (int i = 2; i < 16; i += 2) m = __builtin_fmaxf(__builtin_fmaxf(m, s[i]), s[i + 1]);
    const auto sw = __builtin_amdgcn_permlane32_swap(__float_as_uint(m), __float_as_uint(m), false, false);
    m = -__builtin_fmaxf(__uint_as_float(sw[0]), __uint_as_float(sw[1]));
    f32x16 c;
#pragma unroll
    for (int i = 0; i < 16; ++i) c[i] = m;
    PIN(c);
    return c;
}

__global__ __launch_bounds__(512, 2) void fused_fwd(const void* __restrict__ xin, const void* __restrict__ wqkv, const void* __restrict__ wo,
                                                    const void* __restrict__ w1, const unsigned short* __restrict__ b1, const void* __restrict__ w2,
                                                    const unsigned short* __restrict__ b2, unsigned short* __restrict__ out, char* wsp) {
    __shared__ __attribute__((aligned(16))) char lds_raw[L_TOTAL];
    const lptr lds = (lptr)lds_raw;
    const unsigned lds0 = (unsigned)(uintptr_t)lds;
    const unsigned long long t_start = __builtin_amdgcn_s_memrealtime();
    const int w_ = __builtin_amdgcn_readfirstlane(threadIdx.x >> 6);
#define PHASE_IDS \
    const int lane = opaque(threadIdx.x & 63), l31 = lane & 31, hh = lane >> 5; \
    const int w = sopaque(w_), bid = sopaque(blockIdx.x), g = bid & 7, r = bid >> 3; \
    const int st64 = g * 4 + (r >> 3), dt64 = r & 7; \
    const int dt1 = (g & 1) * 4 + (r & 3), ft1 = (g >> 1) * 7 + (r >> 2); \
    const int own_s32 = st64 * 2 + (w & 1), own_d32 = dt64 * 2 + ((w >> 1) & 1); \
    (void)l31; (void)hh; (void)dt1; (void)ft1; (void)own_s32; (void)own_d32;
    const bool w1_on = (blockIdx.x >> 3) < 28;
    const rsrc_t ws = mk_rsrc(wsp, WS_END);

#define ISTARG(b)
    unsigned xres[8] = {};
    unsigned tag_lo = 0, tag_hi = 0;
    u32x2 nonce = {};
    u32x4 wc[12];
#define UNDEF_WC() do { _Pragma("unroll") for (int i_ = 0; i_ < 12; ++i_) asm volatile("" : "=v"(wc[i_])); } while (0)
    UNDEF_WC();

#pragma unroll 1
    for (int L = 0; L < 2; ++L) {
        bf16x8 qf;
        {
            PHASE_IDS
            const int gq = (lane >> 4) & 1, q = (lane >> 2) & 3, p = lane & 3;
            const unsigned kv_soff = WS_KV + (L * 32 + r) * 131072;
            STAMP(0);
            QkvAcc a; a.qk = f32x16{}; a.v = f32x16{};
            u32x4 sp[4];
            float rstd, mr, csv;
            f32x4 cs[4], rs[4], ms[4];
            asm volatile("" : "=v"(rstd), "=v"(mr), "=v"(rs[0]), "=v"(rs[1]), "=v"(rs[2]), "=v"(rs[3]), "=v"(ms[0]), "=v"(ms[1]), "=v"(ms[2]), "=v"(ms[3]));
#define LN_PREP() do { \
                const f32x4 tot = record_total(sp); \
                const float m0 = tot[0] * (1.f / 512.f), m1 = tot[2] * (1.f / 512.f); \
                const float r0 = __builtin_amdgcn_rsqf(__builtin_fmaxf(tot[1] * (1.f / 512.f) - m0 * m0, 0.f) + LN_EPS); \
                const float r1 = __builtin_amdgcn_rsqf(__builtin_fmaxf(tot[3] * (1.f / 512.f) - m1 * m1, 0.f) + LN_EPS); \
                const lptr sst = lds + L_SST + w * 256; \
                *(LAS f32x2*)(sst + hh * 128 + (lane & 15) * 8) = hh ? f32x2{m0 * r0, m1 * r1} : f32x2{r0, r1}; \
                rstd = *(LAS const float*)(sst + l31 * 4); mr = *(LAS const float*)(sst + 128 + l31 * 4); \
                _Pragma("unroll") for (int i = 0; i < 4; ++i) { \
                    rs[i] = *(LAS const f32x4*)(sst + (8 * i + 4 * hh) * 4); ms[i] = *(LAS const f32x4*)(sst + 128 + (8 * i + 4 * hh) * 4); } \
                SBAR(); } while (0)
#define CS_READ() do { \
                _Pragma("unroll") for (int i = 0; i < 4; ++i) cs[i] = *(LAS const f32x4*)(lds + L_CSF + (8 * i + 4 * hh) * 4); \
                csv = *(LAS const float*)(lds + L_CSF + 128 + (lane & 15) * 4); \
                SBAR(); } while (0)
            const unsigned sp_soff = WS_SP + (L * 64 + g * 8 + w) * 4096;
            if (L == 0) {
                const rsrc_t rx = mk_rsrc(xin, 2048 * 1024), rw = mk_rsrc(wqkv, 512 * 3072);
                unsigned wv[3];
#pragma unroll
                for (int ty = 0; ty < 3; ++ty) {
                    const int o = ty * 1024 + lane * 16, rg = o / 384, rem = o % 384;
                    wv[ty] = (4 * rg + ((rem >> 5) & 3)) * 3072 + (rem >> 7) * 1024 + (rem & 16);
                }
#define WQUART(j) do { if (w >= 4) { _Pragma("unroll") for (int ty = 0; ty < 3; ++ty) \
                       dma<1, 0, 0>(lds0 + L_WIMG + (4 * (j) + w - 4) * 3072 + ty * 1024, wv[ty], rw, (4 * (j) + w - 4) * 98304 + r * 32); } } while (0)
                const unsigned tokb = (g * 256 + w * 32) * 1024, stg = w < 5 ? 65536 + w * 12288 : (w == 5 ? 0 : L_REG + (w - 6) * 12288);
                const unsigned vA = (lane >> 3) * 1024 + (((lane & 7) ^ (lane >> 4)) << 4), vB = vA ^ 64;
#define XCHUNK(c) do { dma<2, 2048, 16384>(lds0 + stg + ((c) % 3) * 4096, vA, rx, tokb + (c) * 128); \
                       dma<2, 2048, 16384>(lds0 + stg + ((c) % 3) * 4096 + 1024, vB, rx, tokb + 8192 + (c) * 128); } while (0)
#define ROLE_WAIT(a, b) do { if (w >= 4) vm_wait<a>(); else vm_wait<b>(); } while (0)
                u32x2 xr[4];
                WQUART(0); XCHUNK(0);
                asm volatile("s_nop 4\n\tbuffer_load_dwordx2 %0, off, %1, 0 sc1" : "=v"(nonce) : "s"(ws) : "memory");
                if (w < 4) {
                    asm volatile("s_nop 4\n\tbuffer_load_dwordx2 %0, %4, %5, %6 offen\n\tbuffer_load_dwordx2 %1, %4, %5, %6 offen offset:16\n\t"
                                 "buffer_load_dwordx2 %2, %4, %5, %6 offen offset:32\n\tbuffer_load_dwordx2 %3, %4, %5, %6 offen offset:48"
                                 : "=&v"(xr[0]), "=&v"(xr[1]), "=&v"(xr[2]), "=&v"(xr[3]) : "v"(l31 * 1024 + hh * 8), "s"(rx), "s"(own_s32 * 32768 + own_d32 * 64) : "memory");
                } else {
#pragma unroll
                    for (int i = 0; i < 4; ++i) asm volatile("" : "=v"(xr[i]));
                }
                XCHUNK(1); WQUART(1); XCHUNK(2);
                if (CODEPF && w == 3) {
                    const char* pc;
                    asm volatile("s_getpc_b64 %0" : "=s"(pc));
                    touch<1, 0>(lds0 + L_SST + w * 256, (lane & 15) * 128, mk_rsrc((const char*)((uintptr_t)pc & ~(uintptr_t)127), CODEPF * 1024), r * 2048);
                }
                const lptr pqk = lds + L_WIMG + hh * 768 + gq * 128 + q * 32 + p * 8, pv = lds + L_WIMG + hh * 768 + 256 + q * 32 + p * 8;
                lptr px[4];
#pragma unroll
                for (int t = 0; t < 4; ++t) px[t] = lds + stg + l31 * 128 + (((2 * t + hh) ^ ((l31 >> 1) & 7)) << 4);
                WFrag wf[2];
                u32x2 fl;
                const unsigned f0r = opaque(WS_F0 + r * 128 + (lane & 7) * 8), f5r = opaque(WS_F5 + (g * 8 + w) * 128 + (lane & 15) * 8), flr = hh ? f0r : f5r;
#pragma unroll
                for (int c = 0; c < 8; ++c) {
                    if (c == 0) ISTAMP(54);
                    if (c <= 5) ROLE_WAIT(11, 8); else if (c == 6) vm_wait<0>();
                    ISTAMP(55 + c);
                    if (c == 0) { BAR(); STAMP(1); wf[0].qk = tr_frag(pqk, pqk + 384); wf[0].v = tr_frag(pv, pv + 384); }
                    bf16x8 xf[4];
#pragma unroll
                    for (int t = 0; t < 4; ++t) xf[t] = lds_frag(px[t] + (c % 3) * 4096);
                    if (c == 5) asm volatile("s_nop 4\n\tbuffer_load_dwordx2 %0, %1, %2, 0 offen sc1" : "=v"(fl) : "v"(flr), "s"(ws) : "memory");
                    if (c + 3 < 8) { asm volatile("s_waitcnt lgkmcnt(0)" : "+v"(xf[0]), "+v"(xf[1]), "+v"(xf[2]), "+v"(xf[3]) :: "memory"); XCHUNK(c + 3); }
                    if (c == 1) WQUART(2);
                    if (c == 3) WQUART(3);
                    if (c == 0) {
                        asm volatile("" : "+v"(nonce), "+v"(xr[0]), "+v"(xr[1]), "+v"(xr[2]), "+v"(xr[3]));
                        unsigned long long z = ((unsigned long long)__builtin_amdgcn_readfirstlane(nonce[1]) << 32) | (unsigned)__builtin_amdgcn_readfirstlane(nonce[0]);
                        z = z * 0x9E3779B97F4A7C15ull + 0x632BE59BD9B4E019ull;
                        z ^= z >> 29; z *= 0xBF58476D1CE4E5B9ull; z ^= z >> 32;
                        tag_lo = (unsigned)z; tag_hi = (unsigned)(z >> 32);
#pragma unroll
                        for (int i = 0; i < 4; ++i) { xres[2 * i] = xr[i][0]; xres[2 * i + 1] = xr[i][1]; }
                        if (w < 4) {
                            __builtin_amdgcn_raw_buffer_store_b64(row_partial(xres), ws, hh ? OOB : l31 * 8u, WS_SP + (own_s32 * 16 + own_d32) * 256, AUX_SC1);
                            __builtin_amdgcn_raw_buffer_store_b64(u32x2{}, ws, l31 * 8 + hh * 0x40000, WS_ST + (own_s32 * 16 + own_d32) * 256, AUX_SC1);
                            const unsigned c1a = opaque(WS_C1 + (r * 8 + g) * 128 + lane * 32768), c3a = opaque(WS_C3 + bid * 256 + (lane - 3) * 128);
                            const unsigned c2a = sopaque(WS_C2 + (((r >> 4) * 64 + g * 8) * 2 + (r & 15)) * 128);
                            const unsigned ca = lane < 2 ? c1a : (lane == 2 ? c2a : c3a);
                            __builtin_amdgcn_raw_buffer_store_b32(0u, ws, (w == 0 && lane < 5) ? ca : OOB, 0, AUX_SC1);
                        }
                    }
                    if (c == 6) {
                        asm volatile("" : "+v"(fl));
                        if (!__all(((fl[0] ^ tag_lo) | (fl[1] ^ tag_hi)) == 0)) wait64(ws, flr, 0, tag_lo, tag_hi);
                        asm volatile("" ::: "memory");
#pragma unroll
                        for (int i = 0; i < 4; ++i) sp[i] = __builtin_amdgcn_raw_buffer_load_b128(ws, lane * 16 + i * 1024, sp_soff, AUX_SC1);
                        SBAR();
                    }
                    if (c == 6) {
                        float cq = 0.f, cv = 0.f;
#pragma unroll
                        for (int j = 0; j < 4; ++j) {
                            const int o = (4 * w + j) * 1536;
                            WFrag e; e.qk = tr_frag(pqk + o, pqk + o + 384); e.v = tr_frag(pv + o, pv + o + 384);
                            colsum_step(cq, cv, e);
                        }
                        *(LAS float*)(lds + L_CSP + (lane < 48 ? lane : lane - 16) * 32 + w * 4) = half_sum2(cq, cv);
                        if (w >= 4) {
#pragma unroll
                            for (int i = 0; i < 12; ++i) wc[i] = *(LAS const u32x4*)(lds + L_WIMG + (w - 4) * 12288 + i * 1024 + lane * 16);
                        }
                    }
                    if (c == 7) { BAR(); if (w == 0) colsum_final(lds, lane); LN_PREP(); }
#pragma unroll
                    for (int t = 0; t < 4; ++t) {
                        const int k = 4 * c + t;
                        if (k == 7) { ROLE_WAIT(15, 63); BAR(); }
                        if (k == 15) { ROLE_WAIT(11, 63); BAR(); }
                        if (k == 23) { ROLE_WAIT(4, 63); BAR(); }
                        qkv_step(a, xf[t], wf[k & 1], wf[(k + 1) & 1], k < 31, pqk, pv, (k + 1) * 1536, (k + 1) * 1536 + 384);
                    }
                    if (c == 1 && w < 4) {
                        VMWAIT(0);
                        const unsigned f5a = sopaque(WS_F5 + own_s32 * 128 + own_d32 * 8), f0a = sopaque(w == 0 ? WS_F0 + r * 128 + g * 8 : OOB);
                        __builtin_amdgcn_raw_buffer_store_b64(u32x2{tag_lo, tag_hi}, ws, lane == 0 ? f5a : (lane == 1 ? f0a : OOB), 0, AUX_SC1);
                    }
                }
                ISTAMP(63);
                BAR();
                CS_READ();
#undef WQUART
#undef ROLE_WAIT
#undef XCHUNK
            } else {
                if (w >= 4) {
#pragma unroll
                    for (int i = 0; i < 12; ++i) *(LAS u32x4*)(lds + L_WIMG + (w - 4) * 12288 + i * 1024 + lane * 16) = wc[i];
                }
                BAR();
                wait_cnt(ws, WS_C5 + ((g * 8 + w) * 2 + (r & 1)) * 128, 16, lane);
                STAMP(1);
                ISTAMP(32);
                bf16x8 xf[32];
                const unsigned x_soff = WS_X2 + (g * 8 + w) * 32768;
#define XLOAD(k) xf[k] = ld_frag(ws, lane * 16 + ((k) & 3) * 1024, x_soff + ((k) >> 2) * 4096)
#pragma unroll
                for (int i = 0; i < 4; ++i) sp[i] = __builtin_amdgcn_raw_buffer_load_b128(ws, lane * 16 + i * 1024, sp_soff, AUX_SC1);
#pragma unroll
                for (int k = 0; k < XN0; ++k) XLOAD(k);
                SBAR();
                ISTAMP(33);
                ISTAMP(34);
                const lptr pv = lds + L_WIMG + hh * 384 + q * 32 + p * 8 + 256, pqk = pv + gq * 128 - 256;
                WFrag wf[2];
                wf[0].qk = tr_frag(pqk, pqk + 768); wf[0].v = tr_frag(pv, pv + 768);
                int nl = XN0;
                if (QPRIO) __builtin_amdgcn_s_setprio(3);
#pragma unroll
                for (int k = 0; k < 32; ++k) {
                    qkv_step(a, xf[k], wf[k & 1], wf[(k + 1) & 1], k < 31, pqk, pv, (k + 1) * 1536, (k + 1) * 1536 + 768);
                    if (nl < 32 && nl < k + XPD) { XLOAD(nl); ++nl; }
                    if (nl < 32 && nl < k + 1 + XPD) { XLOAD(nl); ++nl; }
                    if (QPRIO && k == 11) __builtin_amdgcn_s_setprio(2);
                    if (QPRIO && k == 21) __builtin_amdgcn_s_setprio(1);
                    if (QPRIO && k == 27) __builtin_amdgcn_s_setprio(0);
                    if (k == LNK && w < 4) LN_PREP();
                    if (k == LNK2 && w >= 4) LN_PREP();
                    if (k == 24) CS_READ();
                    if (k == 0) ISTAMP(35);
                    if ((k & 3) == 3) ISTAMP(36 + (k >> 2));
                }
#undef XLOAD
                UNDEF_WC();
            }
            STAMP(2);
            if (APRIO) __builtin_amdgcn_s_setprio(3);
            float qk[16], vv[16];
#pragma unroll
            for (int i = 0; i < 16; ++i) qk[i] = a.qk[i] * rstd - mr * cs[i >> 2][i & 3];
            const u32x4 kpk = {pk2(qk[8], qk[9]), pk2(qk[10], qk[11]), pk2(qk[12], qk[13]), pk2(qk[14], qk[15])};
            const unsigned koff = (hh * 32 + ((l31 & 19) | ((l31 & 4) << 1) | ((l31 & 8) >> 1))) * 16;
            st_frag(kpk, ws, koff, kv_soff + (g * 8 + w) * 2048);
#pragma unroll
            for (int i = 0; i < 16; ++i) vv[i] = a.v[i] * rs[i >> 2][i & 3] - ms[i >> 2][i & 3] * csv;
            u32x4 vpk;
#pragma unroll
            for (int i = 0; i < 4; ++i) { const unsigned lo = pk2(vv[2 * i], vv[2 * i + 1]), hi = pk2(vv[8 + 2 * i], vv[9 + 2 * i]); vpk[i] = gq ? hi : lo; }
            const unsigned voff = gq * 512 + hh * 256 + (lane & 15) * 16;
            st_frag(vpk, ws, voff, kv_soff + (g * 8 + w) * 2048 + 1024);
            constexpr float QS = 0.25f * 1.4426950408889634f;
            const u32x4 qpk = {pk2(qk[0] * QS, qk[1] * QS), pk2(qk[2] * QS, qk[3] * QS), pk2(qk[4] * QS, qk[5] * QS), pk2(qk[6] * QS, qk[7] * QS)};
            qf = __builtin_bit_cast(bf16x8, qpk);
            if (L == 0) {
                if (w == 0) *(LAS unsigned*)(lds + L_PROG + (lane & 7) * 4) = 0u;
                const unsigned one = (lane & 15) ? 0u : 0x3f803f80u;
                *(LAS u32x4*)(lds + L_ONES + w * 512 + hh * 256 + (lane & 15) * 16) = u32x4{one, one, one, one};
            }
            *(LAS u32x4*)(lds + w * 1024 + koff) = kpk;
            *(LAS u32x4*)(lds + 8192 + w * 1024 + voff) = vpk;
            *(LAS u32x4*)(lds + L_REG + w * 3072 + lane * 16) = qpk;
            BAR();
            STAMP(3);
        }
        {
            PHASE_IDS
            const int gq = (lane >> 4) & 1;
            const unsigned kv_soff = WS_KV + (L * 32 + r) * 131072;
            const int idx = opaque(0xDDDD);
            lptr kp = lds + (w & 1) * 4096 + lane * 16;
            lptr vp = lds + (gq ? L_ONES : 8192 + (w & 1) * 4096) + hh * 256 + (lane & 15) * 16;
            const int vinc = gq ? 0 : 16384;
            const bf16x8 qf2 = lds_frag(lds + L_REG + (w ^ 1) * 3072 + lane * 16), k0 = lds_frag(lds + lane * 16);
            bf16x8 kf = lds_frag(kp), v0 = lds_frag(vp), v1 = lds_frag(vp + 512), v2, v3;
            asm volatile("" : "=v"(v2), "=v"(v3));
            f32x16 sA = MFMA(k0, qf, f32x16{}), sB = MFMA(k0, qf2, f32x16{}), o = {}, o2 = {};
            const f32x16 cneg = neg_row_max(sA), cneg2 = neg_row_max(sB);
#define KVCHUNK(j) dma_sc1<2, 8192, 1024>(lds0 + (j) * 16384 + w * 1024, lane * 16, ws, kv_soff + (((g + (j)) & 7) * 8 + w) * 2048)
            sA = MFMA(kf, qf, cneg);
            publish<1>(ws, WS_C1 + ((L * 32 + r) * 8 + w) * 128, lane);
            sA[0] = EX(sA[0]); sA[1] = EX(sA[1]);
#define PRIO(n) do { if (APRIO) __builtin_amdgcn_s_setprio(n); } while (0)
            {
                const unsigned c1o = WS_C1 + ((L * 32 + r) * 8 + w) * 128, pv0 = lane == 0 ? 0u : OOB;
                int pc = 0;
#define POLL_ISSUE() asm volatile("s_nop 4\n\tbuffer_atomic_add %0, %1, %2, %3 offen sc0 sc1" : "+v"(pc) : "v"(pv0), "s"(ws), "s"(c1o) : "memory")
#define POLL_READ() asm volatile("s_waitcnt vmcnt(0)" : "+v"(pc) :: "memory")
                PRIO(2);
                ATT_EVEN(1024, 1024);
                POLL_ISSUE();
                ATT_ODD(2048, 2048);
                STAMP(4);
                POLL_READ();
                if (__builtin_amdgcn_readfirstlane(pc) < 8) wait_cnt(ws, c1o, 8, lane);
                STAMP(5);
                KVCHUNK(1);
                PRIO(1);
                ATT_EVEN(3072, 3072);
                KVCHUNK(2); KVCHUNK(3);
                PRIO(0);
                ATT_ODD_LAST();
                vm_wait_bar<4>();
                STAMP(6);
                kp += 16384; vp += vinc;
                kf = lds_frag(kp); v0 = lds_frag(vp); v1 = lds_frag(vp + 512);
                KVCHUNK(4); KVCHUNK(5);
                sA = MFMA(kf, qf, cneg);
                sA[0] = EX(sA[0]); sA[1] = EX(sA[1]);
            }
#pragma unroll
            for (int c = 1; c < 8; ++c) {
                if (c == 1 || c == 3 || c == 4 || c == 7) PRIO(2);
                ATT_EVEN(1024, 1024);
                if (c == 1 || c == 3 || c == 4) PRIO(1);
                ATT_ODD(2048, 2048);
                if (c == 1 || c == 3 || c == 4) PRIO(0); else if (c == 7) PRIO(1);
                ATT_EVEN(3072, 3072);
                if (c == 1) { vm_wait_bar<4>(); KVCHUNK(6); KVCHUNK(7); }
                if (TLBW && c == 2 && L == 0) {
                    if (w == 0) touch<1, 0>(lds0 + L_SST, lane < 24 ? lane * 32768u + 0x7f80u : OOB, ws, 0x40000 + r * 24 * 32768);
                    if (w == 1) touch<1, 0>(lds0 + L_SST + 256, lane < 2 ? lane * 32768u : OOB, mk_rsrc(out, 2048 * 1024), r * 65536);
                    if (w == 2) touch<1, 0>(lds0 + L_SST + 512, lane < 28 ? lane * 128u : OOB, mk_rsrc(b1, 3584), 0);
                    if (w == 3) touch<1, 0>(lds0 + L_SST + 768, lane < 32 ? lane * 128u : OOB, mk_rsrc(b2, 4096), 0);
                }
                if (c == 3) vm_wait_bar<0>();
                if (c == 4) {
                    BAR();
                    dma<8, 1024, 8192>(lds0 + w * 8192, wvoff(lane, 1024), mk_rsrc(wo, 512 * 1024), ((w & 1) * 256 + (w >> 1) * 64) * 1024 + dt64 * 128);
                }
                if (c == WARM1) touch<1, 0>(lds0 + L_SST + w * 256, lane * 3584, mk_rsrc(w1, 2048 * 3584), ((r & 3) * 512 + w * 64) * 3584 + (w1_on ? ft1 : 0) * 128);
                if (c == 2 && L == 0) touch<1, 0>(lds0 + L_SST + w * 256, (lane < 28 ? lane : 0) * 4096, mk_rsrc(w2, 1792 * 4096), (dt64 * 224 + w * 28) * 4096 + st64 * 128);
                if (c < 7) { if (c != 2 && c < 5) PRIO(3); vp += vinc; ATT_ODD(16384, 0); kp += 16384; }
                else { PRIO(0); ATT_ODD_LAST(); }
            }
            STAMP(7);
            VMWAIT(0);
            STAMP(20);
            {
                const lptr mine = lds + L_REG + w * 3072 + lane * 16, other = lds + L_REG + (w ^ 1) * 3072 + lane * 16;
                *(LAS f32x4*)mine = f32x4{o2[0], o2[1], o2[2], o2[3]};
                *(LAS f32x4*)(mine + 1024) = f32x4{o2[4], o2[5], o2[6], o2[7]};
                *(LAS float*)(mine + 2048) = o2[8];
                BAR();
                STAMP(21);
                const f32x4 a0 = *(LAS const f32x4*)other, a1 = *(LAS const f32x4*)(other + 1024);
                o[8] += *(LAS const float*)(other + 2048);
#pragma unroll
                for (int i = 0; i < 4; ++i) { o[i] += a0[i]; o[4 + i] += a1[i]; }
            }
#define O_STORE() do { const float il = __builtin_amdgcn_rcpf(__uint_as_float(__builtin_amdgcn_permlane32_swap(__float_as_uint(o[8]), __float_as_uint(o[8]), false, false)[0])); \
                st_frag(u32x4{pk2(o[0] * il, o[1] * il), pk2(o[2] * il, o[3] * il), pk2(o[4] * il, o[5] * il), pk2(o[6] * il, o[7] * il)}, ws, lane * 16, \
                        WS_O + L * 0x200000 + ((g * 8 + w) * 32 + r) * 1024); SBAR(); } while (0)
            O_STORE();
            STAMP(22);
            float chk = __builtin_fabsf(o[8]);
#pragma unroll
            for (int i = 0; i < 8; ++i) chk += __builtin_fabsf(o[i]);
            if (__builtin_expect(__any(!(chk < 1e36f)), 0)) {
                float m = -__builtin_inff();
                o = f32x16{};
                const u32x4 one4 = {0x3f803f80u, 0x3f803f80u, 0x3f803f80u, 0x3f803f80u};
#pragma unroll 1
                for (int t = 0; t < 64; ++t) {
                    const bf16x8 kk = ld_frag(ws, lane * 16, kv_soff + t * 2048);
                    u32x4 a0 = __builtin_amdgcn_raw_buffer_load_b128(ws, hh * 256 + (lane & 15) * 16, kv_soff + t * 2048 + 1024, AUX_SC1);
                    u32x4 a1 = __builtin_amdgcn_raw_buffer_load_b128(ws, hh * 256 + (lane & 15) * 16, kv_soff + t * 2048 + 1536, AUX_SC1);
                    a0 = gq ? one4 : a0; a1 = gq ? one4 : a1;
                    f32x16 s = MFMA(kk, qf, f32x16{});
                    float tm = m;
#pragma unroll
                    for (int i = 0; i < 16; ++i) tm = __builtin_fmaxf(tm, s[i]);
                    const auto sw = __builtin_amdgcn_permlane32_swap(__float_as_uint(tm), __float_as_uint(tm), false, false);
                    tm = __builtin_fmaxf(__uint_as_float(sw[0]), __uint_as_float(sw[1]));
                    const float al = EX(m - tm);
                    m = tm;
#pragma unroll
                    for (int i = 0; i < 16; ++i) { o[i] *= al; s[i] = EX(s[i] - m); }
                    o = smf(__builtin_bit_cast(bf16x8, a0), lo8(s), o, idx);
                    o = smf(__builtin_bit_cast(bf16x8, a1), hi8(s), o, idx);
                }
                VMWAIT(0);
                O_STORE();
            }
            publish<1>(ws, WS_C2 + ((L * 64 + g * 8 + w) * 2 + (r >> 4)) * 128, lane);
            if (RDBK) touch<1, 0>(lds0 + L_SST + w * 256, (lane & 7) * 128, ws, WS_O + L * 0x200000 + ((g * 8 + w) * 32 + r) * 1024);
            STAMP(8);
        }
        if (w_ < 4) {
            f32x16 acc0, acc1;
            u32x2 bb[4];
            unsigned b2u;
            __builtin_amdgcn_s_setprio(CPRIO);
            {
                PHASE_IDS
                const int kh = w >> 1;
#pragma unroll
                for (int i = 0; i < 4; ++i) bb[i] = *(const u32x2*)(b1 + ((w1_on ? ft1 : 0) * 2 + kh) * 32 + 8 * i + 4 * hh);
                b2u = b2[own_s32 * 32 + l31];
#pragma unroll
                for (int i = 0; i < 8; ++i) { acc0[2 * i] = bf_lo(xres[i]); acc0[2 * i + 1] = bf_hi(xres[i]); }
                PIN(acc0);
                (wait_cnt(ws, WS_C2 + ((L * 64 + own_s32) * 2 + kh) * 128, 16, lane));
                STAMP(9);
                gemm_compute<4, 0, false, false, 48>(lds, lane, kh, 0, ws, WS_O + L * 0x200000 + (own_s32 * 32 + kh * 16) * 1024, 0, acc0, acc1 ISTARG(48));
                STAMP(10);
                exchange(lds, lane, w, w ^ 2, acc0, acc1);
#pragma unroll
                for (int i = 0; i < 8; ++i) xres[i] = pk2(acc0[2 * i], acc0[2 * i + 1]);
                const unsigned st_soff = WS_ST + (L * 64 + own_s32) * 4096, tg = L + 1;
                {
                    const u32x2 rp = row_partial(xres);
                    __builtin_amdgcn_raw_buffer_store_b64(u32x2{rp[0], (rp[1] & ~3u) | tg}, ws, hh ? OOB : l31 * 8u, st_soff + own_d32 * 256, AUX_SC1);
                    if (STCNT) arrive<1>(ws, WS_C6 + (L * 64 + own_s32) * 128, lane);
                }
                STAMP(11);
                const lptr tp = lds + L_SCR + (w ^ 2) * 4096;
                *(LAS u32x4*)(tp + l31 * 64 + hh * 32) = u32x4{xres[0], xres[1], xres[2], xres[3]};
                *(LAS u32x4*)(tp + l31 * 64 + hh * 32 + 16) = u32x4{xres[4], xres[5], xres[6], xres[7]};
                const lptr rp = tp + (4 * hh + ((lane >> 2) & 3)) * 64 + ((lane >> 4) & 1) * 32 + (lane & 3) * 8;
                float xt[16];
#pragma unroll
                for (int t = 0; t < 2; ++t) {
                    const u32x4 v = __builtin_bit_cast(u32x4, tr_frag(rp + t * 1024, rp + t * 1024 + 512));
#pragma unroll
                    for (int i = 0; i < 4; ++i) { xt[8 * t + 2 * i] = bf_lo(v[i]); xt[8 * t + 2 * i + 1] = bf_hi(v[i]); }
                }
#pragma unroll
                for (int i = 0; i < 16; ++i) PIN(xt[i]);
                if (STCNT) NOTE(30, wait_cnt(ws, WS_C6 + (L * 64 + own_s32) * 128, 16, lane)); else VMWAIT(0);
                u32x4 gv[4];
                unsigned it;
#pragma unroll 1
                for (it = 0; it < SPIN_CAP; ++it) {
                    unsigned bad = 0;
#pragma unroll
                    for (int i = 0; i < 4; ++i) gv[i] = __builtin_amdgcn_raw_buffer_load_b128(ws, lane * 16 + i * 1024, st_soff, AUX_SC1);
#pragma unroll
                    for (int i = 0; i < 4; ++i) bad |= (gv[i][1] ^ tg) | (gv[i][3] ^ tg);
                    if (__all((bad & 3) == 0)) break;
                    POLL_PAUSE();
                }
                (void)it;
                STAMP(12);
                {
                    const f32x4 tot = record_total(gv);
                    const float m0 = tot[0] * (1.f / 512.f), m1 = tot[2] * (1.f / 512.f);
                    const float r0 = __builtin_amdgcn_rsqf(__builtin_fmaxf(tot[1] * (1.f / 512.f) - m0 * m0, 0.f) * (1.f + LN_EPS) + LN_EPS * LN_EPS);
                    const float r1 = __builtin_amdgcn_rsqf(__builtin_fmaxf(tot[3] * (1.f / 512.f) - m1 * m1, 0.f) * (1.f + LN_EPS) + LN_EPS * LN_EPS);
                    *(LAS f32x2*)(tp + 2048 + hh * 128 + (lane & 15) * 8) = hh ? f32x2{m0 * r0, m1 * r1} : f32x2{r0, r1};
                }
                const unsigned hv = (hh * 32 + ((l31 & 3) | ((l31 & 12) << 1) | ((l31 & 16) >> 2))) * 16;
#pragma unroll
                for (int t = 0; t < 2; ++t) {
                    u32x4 h;
#pragma unroll
                    for (int j = 0; j < 2; ++j) {
                        const f32x4 sc = *(LAS const f32x4*)(tp + 2048 + (16 * t + 8 * j + 4 * hh) * 4), ms = *(LAS const f32x4*)(tp + 2176 + (16 * t + 8 * j + 4 * hh) * 4);
                        const float* x = xt + 8 * t + 4 * j;
                        h[2 * j] = pk2(x[0] * sc[0] - ms[0], x[1] * sc[1] - ms[1]); h[2 * j + 1] = pk2(x[2] * sc[2] - ms[2], x[3] * sc[3] - ms[3]);
                    }
                    st_frag(h, ws, hv, WS_H2 + L * 0x200000 + (own_d32 * 128 + own_s32 * 2 + t) * 1024);
                }
                STAMP(23);
                publish<2>(ws, WS_C3 + ((L * 16 + own_d32) * 2 + (own_s32 >> 5)) * 256, lane);
                if (L == 0 && bid == 0 && w == 0)
                    __builtin_amdgcn_raw_buffer_store_b64(u32x2{nonce[0] + 1u, nonce[1] + (nonce[0] == 0xffffffffu)}, ws, lane == 0 ? 0u : OOB, WS_NONCE, AUX_SC1);
                STAMP(13);
            }
            if (w1_on) {
                PHASE_IDS
                const int kh = w >> 1, d32 = dt1 * 2 + (w & 1), f32t = ft1 * 2 + kh;
#pragma unroll
                for (int i = 0; i < 16; ++i) { const unsigned bw = bb[i >> 2][(i >> 1) & 1]; acc0[i] = (i & 1) ? bf_hi(bw) : bf_lo(bw); }
                PIN(acc0);
                NOTE(28, wait_cnt(ws, WS_C3 + ((L * 16 + d32) * 2 + kh) * 256 + (g & 2) * 64, 32, lane));
                STAMP(14);
                gemm_compute<16, 4, false, true, 0>(lds, lane, kh, w & 1, ws, WS_H2 + L * 0x200000 + (d32 * 128 + kh * 64) * 1024, L * 32, acc0, acc1 ISTARG(0));
                STAMP(15);
                exchange(lds, lane, w, w ^ 2, acc0, acc1);
                float u[16];
#pragma unroll
                for (int i = 0; i < 16; ++i) {
                    const float z = acc0[i];
                    const float e = EX(z * __builtin_fmaf(z * z, -0.10294324f, -2.30220818f));
                    u[i] = z * __builtin_amdgcn_rcpf(1.f + e);
                }
#pragma unroll
                for (int t = 0; t < 2; ++t)
                    st_frag(u32x4{pk2(u[8 * t], u[8 * t + 1]), pk2(u[8 * t + 2], u[8 * t + 3]), pk2(u[8 * t + 4], u[8 * t + 5]), pk2(u[8 * t + 6], u[8 * t + 7])}, ws, lane * 16,
                            WS_U + L * 0x200000 + (d32 * 112 + f32t * 2 + t) * 1024);
                STAMP(24);
                publish<2>(ws, WS_C4 + ((L * 16 + d32) * 2 + ((f32t + 4) >> 5)) * 256, lane);
                STAMP(16);
            }
            else if (ASSIST) {
                PHASE_IDS
                const int kh = w >> 1, d32 = dt1 * 2 + (w & 1);
                wait_cnt(ws, WS_C3 + ((L * 16 + d32) * 2 + kh) * 256 + (g & 2) * 64, 32, lane);
                touch<8 - ASSIST, 8192>(lds0 + L_SST + w * 256, lane * 128, ws, WS_H2 + L * 0x200000 + (d32 * 128 + kh * 64 + 8 * ASSIST) * 1024);
            }
            {
                PHASE_IDS
                const int kh = w & 1;
                const float bias = __uint_as_float(b2u << 16);
#pragma unroll
                for (int i = 0; i < 8; ++i) { acc0[2 * i] = bf_lo(xres[i]) + bias; acc0[2 * i + 1] = bf_hi(xres[i]) + bias; }
                PIN(acc0);
                NOTE(29, wait_cnt(ws, WS_C4 + ((L * 16 + own_d32) * 2 + kh) * 256 + (g & 1) * 128, 28, lane));
                STAMP(17);
                gemm_compute<14, 4, true, true, 16>(lds, lane, kh, w >> 1, ws, WS_U + L * 0x200000 + (own_d32 * 112 + kh * 56) * 1024, L * 32 + 16, acc0, acc1 ISTARG(16));
                STAMP(18);
                exchange(lds, lane, w, w ^ 1, acc0, acc1);
#pragma unroll
                for (int i = 0; i < 8; ++i) xres[i] = pk2(acc0[2 * i], acc0[2 * i + 1]);
                if (L == 0) {
#pragma unroll
                    for (int t = 0; t < 2; ++t)
                        st_frag(u32x4{xres[4 * t], xres[4 * t + 1], xres[4 * t + 2], xres[4 * t + 3]}, ws, lane * 16, WS_X2 + (own_s32 * 32 + own_d32 * 2 + t) * 1024);
                    __builtin_amdgcn_raw_buffer_store_b64(row_partial(xres), ws, hh ? OOB : l31 * 8u, WS_SP + ((64 + own_s32) * 16 + own_d32) * 256, AUX_SC1);
                    STAMP(25);
                    publish<2>(ws, WS_C5 + own_s32 * 256, lane);
                    if (RDBK) {
                        const unsigned xa = opaque(WS_X2 + (own_s32 * 32 + own_d32 * 2) * 1024 + (lane & 15) * 128), sa = opaque(WS_SP + ((64 + own_s32) * 16 + own_d32) * 256 + (lane & 1) * 128);
                        touch<1, 0>(lds0 + L_SST + w * 256, hh ? sa : xa, ws, 0);
                    }
                } else {
                    { const unsigned long long tn = __builtin_amdgcn_s_memrealtime(); if (bid == 0 && w == 0 && tn - t_start > 7800ull) xres[0] ^= (lane == 0) ? (unsigned)(tn & 1) : 0u; }
                    char* op = (char*)out + (own_s32 * 32 + l31) * 1024 + own_d32 * 64 + hh * 16;
#pragma unroll
                    for (int t = 0; t < 2; ++t) {
                        const auto s0 = __builtin_amdgcn_permlane32_swap(xres[4 * t], xres[4 * t + 2], false, false);
                        const auto s1 = __builtin_amdgcn_permlane32_swap(xres[4 * t + 1], xres[4 * t + 3], false, false);
                        if (OUTAUX < 0) *(u32x4*)(op + t * 32) = u32x4{s0[0], s1[0], s0[1], s1[1]};
                        else {
                            const u32x4 v = {s0[0], s1[0], s0[1], s1[1]};
                            __builtin_amdgcn_raw_buffer_store_b128(v, mk_rsrc(out, 2048 * 1024), l31 * 1024 + hh * 16 + t * 32, own_s32 * 32768 + own_d32 * 64, OUTAUX < 0 ? 0 : OUTAUX);
                            asm volatile("s_nop 1" ::"v"(v) : "memory");
                        }
                    }
                }
                STAMP(19);
            }
            __builtin_amdgcn_s_setprio(0);
            UNDEF_WC();
        } else {
            PHASE_IDS
            const int lw = w - 4;
            const rsrc_t r1 = mk_rsrc(w1, 2048 * 3584), r2 = mk_rsrc(w2, 1792 * 4096);
            const unsigned v1o = wvoff(lane, 3584), v2o = wvoff(lane, 4096), lb = lds0 + lw * 4096;
            const unsigned g1 = ((lw >> 1) * 1024 + (lw & 1) * 32) * 3584 + ft1 * 128, g2 = ((lw >> 1) * 896 + (lw & 1) * 32) * 4096 + st64 * 128;
            const lptr lp = lds + lw * 4096 + lane * 16, fill = lds + L_FILL + lw * 4, prog = lds + L_PROG + (lw >> 1) * 8;
            u32x4 rg[8][4];
#define W2WARM() do { if (WARM2) touch<1, 0>(lds0 + L_SST + w * 256, (lane < 56 ? lane : 0) * 4096, r2, (dt64 * 224 + lw * 56) * 4096 + st64 * 128); } while (0)
            if (w1_on) {
                ring_prefetch<0, 4, 4, 3584, TS1, 0>(lb, rg, r1, v1o, g1, fill, L * 32 ISTARG(0));
                BAR();
                ring_prefetch<4, 16, 4, 3584, TS1, 0>(lb, rg, r1, v1o, g1, fill, L * 32 ISTARG(0));
                W2WARM();
                ring_refill<16, 4, 0>(lp, rg, prog, fill, L * 32 ISTARG(0));
            }
            else W2WARM();
            BAR();
            ring_prefetch<0, 14, 4, 4096, TS2, 16>(lb, rg, r2, v2o, g2, fill, L * 32 + 16 ISTARG(16));
            ring_refill<14, 4, 16>(lp, rg, prog, fill, L * 32 + 16 ISTARG(16));
            BAR();
        }
    }
}

extern "C" void kernel_launch(void* const* d_in, const int* in_sizes, int n_in, void* d_out, int out_size, void* d_ws, size_t ws_size,
                              hipStream_t stream) {
    fused_fwd<<<256, 512, 0, stream>>>(d_in[0], d_in[1], d_in[2], d_in[4], (const unsigned short*)d_in[5], d_in[6], (const unsigned short*)d_in[7],
                                       (unsigned short*)d_out, (char*)d_ws);
}
```

```cpp
#define QTOUCH 2
#include <hip/hip_runtime.h>
#include <hip/hip_bf16.h>
#include <stdint.h>

#define WARM1 5
#define WARM2 1
#define AN0 6
#define APD 12
#define XN0 6
#define XPD 12
#define CPRIO 2
#define APRIO 1
#define NTLD 0
#define TS1 1
#define TS2 2
#define RDBK 1
#define ASSIST 0
#define QPRIO 1
#define STCNT 1
#define OUTAUX -1
#define CODEPF 0
#define TLBW 0
#define WSOFF 0
#define LNK2 4
#define LNK 12
#define INNER 15
#define STAMPS 0

typedef __attribute__((ext_vector_type(16))) float f32x16;
typedef __attribute__((ext_vector_type(8))) float f32x8;
typedef __attribute__((ext_vector_type(4))) float f32x4;
typedef __attribute__((ext_vector_type(2))) float f32x2;
typedef __attribute__((ext_vector_type(16))) __bf16 bf16x16;
typedef __attribute__((ext_vector_type(8))) __bf16 bf16x8;
typedef __attribute__((ext_vector_type(2))) __bf16 bf16x2;
typedef __attribute__((ext_vector_type(4))) unsigned u32x4;
typedef __attribute__((ext_vector_type(2))) unsigned u32x2;
typedef __attribute__((ext_vector_type(4))) short s16x4;
typedef __amdgpu_buffer_rsrc_t rsrc_t;
#define LAS __attribute__((address_space(3)))
typedef LAS char* lptr;
#define DI __device__ __forceinline__

constexpr unsigned WS_NONCE = 0x0;
constexpr unsigned WS_F0 = 0x1000;
constexpr unsigned WS_F5 = 0x2000;
constexpr unsigned WS_C1 = 0x10000;
constexpr unsigned WS_C2 = 0x20000;
constexpr unsigned WS_C3 = 0x28000;
constexpr unsigned WS_C4 = 0x2C000;
constexpr unsigned WS_C5 = 0x30000;
constexpr unsigned WS_C6 = 0x34000;
constexpr unsigned WS_ST = 0x40000;
constexpr unsigned WS_SP = 0x140000;
constexpr unsigned WS_KV = 0x200000;
constexpr unsigned WS_O = 0xA00000;
constexpr unsigned WS_H2 = 0xE00000;
constexpr unsigned WS_U = 0x1200000;
constexpr unsigned WS_X2 = 0x1600000;
constexpr unsigned WS_DBG = 0x1800000;
constexpr unsigned WS_END = 0x1A00000;
constexpr unsigned L_WIMG = 16384;
constexpr unsigned L_ONES = 131072;
constexpr unsigned L_CSP = 135168;
constexpr unsigned L_CSF = L_CSP + 1536;
constexpr unsigned L_PROG = L_CSP + 1792;
constexpr unsigned L_FILL = L_PROG + 16;
constexpr unsigned L_SST = L_CSP + 2048;
constexpr unsigned L_REG = 139264;
constexpr unsigned L_SCR = 147456;
constexpr unsigned L_TOTAL = 163840;
constexpr int AUX_SC1 = 16;
constexpr unsigned OOB = 0x80000000u;
constexpr unsigned SPIN_CAP = 1u << 16;
constexpr float LN_EPS = 1e-5f;

#define SBAR() __builtin_amdgcn_sched_barrier(0)
#define PIN(x) asm volatile("" : "+v"(x))
#define MFMA(a, b, c) __builtin_amdgcn_mfma_f32_32x32x16_bf16(a, b, c, 0, 0, 0)
#define EX(x) __builtin_amdgcn_exp2f(x)
#define VMWAIT(n) asm volatile("s_waitcnt vmcnt(" #n ")" ::: "memory")
#define BAR() asm volatile("s_waitcnt lgkmcnt(0)\n\ts_barrier" ::: "memory")

template <int N> DI void vm_wait() { asm volatile("s_waitcnt vmcnt(%0)" ::"i"(N) : "memory"); }
template <int N> DI void vm_wait_bar() { asm volatile("s_waitcnt vmcnt(%0) lgkmcnt(0)\n\ts_barrier" ::"i"(N) : "memory"); }
DI rsrc_t mk_rsrc(const void* p, unsigned bytes) { asm volatile("" : "+s"(p)); return __builtin_amdgcn_make_buffer_rsrc((void*)p, 0, bytes, 0x00020000); }
template <int N, int LSTEP, int GSTEP> DI void dma(unsigned lds, unsigned voff, rsrc_t rs, unsigned soff) {
    unsigned t;
    asm volatile("s_mov_b32 m0, %1\n\ts_mov_b32 %0, %4\n\ts_nop 2\n\tbuffer_load_dwordx4 %2, %3, %0 offen lds\n\t"
                 ".rept %5\n\ts_add_u32 m0, m0, %6\n\ts_add_u32 %0, %0, %7\n\tbuffer_load_dwordx4 %2, %3, %0 offen lds\n\t.endr"
                 : "=&s"(t) : "s"(lds), "v"(voff), "s"(rs), "s"(soff), "i"(N - 1), "i"(LSTEP), "i"(GSTEP) : "memory", "scc");
}
#define NTS ""
template <int N, int LSTEP, int GSTEP> DI void dma_sc1(unsigned lds, unsigned voff, rsrc_t rs, unsigned soff) {
    unsigned t;
    asm volatile("s_mov_b32 m0, %1\n\ts_mov_b32 %0, %4\n\ts_nop 2\n\tbuffer_load_dwordx4 %2, %3, %0 offen sc1 " NTS " lds\n\t"
                 ".rept %5\n\ts_add_u32 m0, m0, %6\n\ts_add_u32 %0, %0, %7\n\tbuffer_load_dwordx4 %2, %3, %0 offen sc1 " NTS " lds\n\t.endr"
                 : "=&s"(t) : "s"(lds), "v"(voff), "s"(rs), "s"(soff), "i"(N - 1), "i"(LSTEP), "i"(GSTEP) : "memory", "scc");
}
template <int N, int GSTEP> DI void touch(unsigned pad, unsigned voff, rsrc_t rs, unsigned soff) {
    unsigned t;
    asm volatile("s_mov_b32 m0, %1\n\ts_mov_b32 %0, %4\n\ts_nop 2\n\tbuffer_load_dword %2, %3, %0 offen sc1 lds\n\t"
                 ".rept %5\n\ts_add_u32 %0, %0, %6\n\tbuffer_load_dword %2, %3, %0 offen sc1 lds\n\t.endr"
                 : "=&s"(t) : "s"(pad), "v"(voff), "s"(rs), "s"(soff), "i"(N - 1), "i"(GSTEP) : "memory", "scc");
}
DI bf16x8 ld_frag(rsrc_t rs, unsigned voff, unsigned soff) { return __builtin_bit_cast(bf16x8, __builtin_amdgcn_raw_buffer_load_b128(rs, voff, soff, AUX_SC1 | (NTLD ? 2 : 0))); }
DI void st_frag(u32x4 v, rsrc_t rs, unsigned voff, unsigned soff) {
    __builtin_amdgcn_raw_buffer_store_b128(v, rs, voff, soff, AUX_SC1);
    asm volatile("s_nop 1" ::"v"(v) : "memory");
}
DI bf16x8 lds_frag(lptr p) { return *(LAS const bf16x8*)p; }
DI s16x4 lds_tr(lptr p) { return __builtin_amdgcn_ds_read_tr16_b64_v4i16((LAS s16x4*)p); }
DI bf16x8 tr_frag(lptr p0, lptr p1) {
    const s16x4 a = lds_tr(p0), b = lds_tr(p1);
    return __builtin_bit_cast(bf16x8, __builtin_shufflevector(a, b, 0, 1, 2, 3, 4, 5, 6, 7));
}
DI unsigned pk2(float a, float b) { const f32x2 v = {a, b}; return __builtin_bit_cast(unsigned, __builtin_convertvector(v, bf16x2)); }
DI float bf_lo(unsigned u) { return __uint_as_float(u << 16); }
DI float bf_hi(unsigned u) { return __uint_as_float(u & 0xffff0000u); }
DI float dot2(unsigned a, unsigned b, float c) { return __builtin_amdgcn_fdot2_f32_bf16(__builtin_bit_cast(bf16x2, a), __builtin_bit_cast(bf16x2, b), c, false); }
DI float half_sum2(float a, float b) { const auto r = __builtin_amdgcn_permlane32_swap(__float_as_uint(a), __float_as_uint(b), false, false); return __uint_as_float(r[0]) + __uint_as_float(r[1]); }
DI f32x8 lo8(f32x16 v) { return __builtin_shufflevector(v, v, 0, 1, 2, 3, 4, 5, 6, 7); }
DI f32x8 hi8(f32x16 v) { return __builtin_shufflevector(v, v, 8, 9, 10, 11, 12, 13, 14, 15); }
DI f32x16 smf(bf16x8 a, f32x8 p, f32x16 c, int idx) { return __builtin_amdgcn_smfmac_f32_32x32x32_bf16(a, __builtin_bit_cast(bf16x16, p), c, idx, 0, 0); }
DI int opaque(int v) { asm volatile("" : "+v"(v)); return v; }
DI int sopaque(int v) { asm volatile("" : "+s"(v)); return v; }

#define PSLEEP 0
#define POLL_PAUSE() do { if (PSLEEP) __builtin_amdgcn_s_sleep(PSLEEP); asm volatile("" ::: "memory"); } while (0)
DI unsigned wait_cnt(rsrc_t ws, unsigned soff, unsigned target, int lane) {
    unsigned i;
#pragma unroll 1
    for (i = 0; i < SPIN_CAP; ++i) {
        const unsigned v = __builtin_amdgcn_raw_ptr_buffer_atomic_add_i32(0, ws, lane == 0 ? 0u : OOB, soff, AUX_SC1);
        if ((unsigned)__builtin_amdgcn_readfirstlane(v) >= target) break;
        POLL_PAUSE();
    }
    asm volatile("" ::: "memory");
    return i;
}
template <int NREP> DI void arrive(rsrc_t ws, unsigned soff, int lane) {
    __builtin_amdgcn_raw_ptr_buffer_atomic_add_i32(1, ws, lane < NREP ? lane * 128u : OOB, soff, AUX_SC1);
    asm volatile("" ::: "memory");
}
template <int NREP> DI void publish(rsrc_t ws, unsigned soff, int lane) { VMWAIT(0); arrive<NREP>(ws, soff, lane); }
DI unsigned wait64(rsrc_t ws, unsigned voff, unsigned soff, unsigned tlo, unsigned thi) {
    unsigned i;
#pragma unroll 1
    for (i = 0; i < SPIN_CAP; ++i) {
        const u32x2 v = __builtin_amdgcn_raw_buffer_load_b64(ws, voff, soff, AUX_SC1);
        if (__all(((v[0] ^ tlo) | (v[1] ^ thi)) == 0)) break;
        POLL_PAUSE();
    }
    asm volatile("" ::: "memory");
    return i;
}

#define ISTAMP(i) do { } while (0)
#define ISTPAR
#define NOTE(i, v) (void)(v)
#define STAMP(i) do { } while (0)

DI unsigned wvoff(int lane, unsigned ldw) { return (lane >> 3) * ldw + (((lane & 7) ^ (((lane >> 4) & 1) << 2)) << 4); }
DI void lds_post(lptr p, unsigned v) { *(volatile LAS unsigned*)p = v; }
DI unsigned lds_min2(lptr p) {
    const u32x2 v = *(volatile LAS u32x2*)p;
    const unsigned a = __builtin_amdgcn_readfirstlane(v[0]), b = __builtin_amdgcn_readfirstlane(v[1]);
    return a < b ? a : b;
}
template <int SLEEP> DI void lds_need(lptr p, unsigned& have, unsigned need) {
#pragma unroll 1
    for (unsigned i = 0; have < need && i < (SPIN_CAP << 4); ++i) { have = lds_min2(p); if (SLEEP && have < need) __builtin_amdgcn_s_sleep(SLEEP); }
    asm volatile("" ::: "memory");
}
template <int S0, int S1, int SLOT0, int LDW, int TS, int ib> DI void ring_prefetch(unsigned lb, u32x4 (&rg)[8][4], rsrc_t rw, unsigned voff, unsigned gb, lptr fill, unsigned base ISTPAR) {
#pragma unroll
    for (int s = S0; s < S1; ++s) {
        if (!(s & 1)) ISTAMP(ib + (s >> 1));
        const unsigned so = gb + s * (64 * LDW);
#pragma unroll
        for (int h = 0; h < 2; ++h) {
            if (s < 8) dma<2, 1024, 8 * LDW>(lb + ((s + SLOT0) & 7) * 16384 + h * 2048, voff, rw, so + h * 16 * LDW);
            else {
#pragma unroll
                for (int p = 2 * h; p < 2 * h + 2; ++p)
                    asm volatile("s_nop 4\n\tbuffer_load_dwordx4 %0, %1, %2, %3 offen" : "=v"(rg[s & 7][p]) : "v"(voff), "s"(rw), "s"(so + p * 8 * LDW) : "memory");
            }
            vm_wait<4 * TS>();
        }
        if (s >= TS && s < 8 + TS) lds_post(fill, base + s + 1 - TS);
    }
}
template <int NST, int SLOT0, int ib> DI void ring_refill(lptr lp, u32x4 (&rg)[8][4], lptr prog, lptr fill, unsigned base ISTPAR) {
    unsigned have = 0;
#pragma unroll
    for (int s = 8; s < NST; ++s) {
        lds_need<1>(prog, have, base + s - 7);
        if (s == NST - 2) asm volatile("s_waitcnt vmcnt(0)" : "+v"(rg[s & 7][0]), "+v"(rg[s & 7][1]), "+v"(rg[s & 7][2]), "+v"(rg[s & 7][3]) :: "memory");
        else asm volatile("" : "+v"(rg[s & 7][0]), "+v"(rg[s & 7][1]), "+v"(rg[s & 7][2]), "+v"(rg[s & 7][3]) :: "memory");
#pragma unroll
        for (int p = 0; p < 4; ++p) *(LAS u32x4*)(lp + ((s + SLOT0) & 7) * 16384 + p * 1024) = rg[s & 7][p];
        asm volatile("" ::: "memory");
        lds_post(fill, base + s + 1);
        ISTAMP(ib + s);
    }
}
template <int NST, int SLOT0, bool ACT_A, bool RING, int ib>
DI void gemm_compute(lptr lds, int lane, int kh, int ah, rsrc_t ws, unsigned act_soff, unsigned base, f32x16& acc0, f32x16& acc1 ISTPAR) {
    constexpr int NK = NST * 4, LA = 2;
    const unsigned av = lane * 16;
    ISTAMP(ib);
    bf16x8 f[NK], w0[NK], w1[NK];
#define ALOAD(i) f[i] = ld_frag(ws, av + ((i) & 3) * 1024, act_soff + ((i) >> 2) * 4096)
#pragma unroll
    for (int i = 0; i < AN0; ++i) ALOAD(i);
    SBAR();
    const int hh = lane >> 5, gq = (lane >> 4) & 1, q = (lane >> 2) & 3, p = lane & 3;
    const unsigned a0 = kh * 8192 + (4 * hh + q) * 128 + (((4 * kh + 2 * gq + (p >> 1)) ^ (((q >> 1) & 1) << 2)) << 4) + (p & 1) * 8;
    const lptr b0 = lds + a0, b1 = lds + (a0 ^ 64), b0h = b0 + 65536, b1h = b1 + 65536;
    const lptr fill = lds + L_FILL + kh * 8, prog = lds + L_PROG + kh * 8 + ah * 4;
    unsigned have = 0;
#define WREAD(dst, pl, ph, k) do { const int sl_ = (((k) >> 2) + SLOT0) & 7, o_ = (sl_ & 3) * 16384 + ((k) & 3) * 2048; \
        const lptr p_ = (sl_ & 4) ? ph : pl; dst[k] = tr_frag(p_ + o_, p_ + o_ + 1024); } while (0)
    acc1 = f32x16{};
    ISTAMP(ib + 1);
    if (RING) lds_need<0>(fill, have, base + 1);
    ISTAMP(ib + 2);
#pragma unroll
    for (int k = 0; k < LA; ++k) { WREAD(w1, b1, b1h, k); WREAD(w0, b0, b0h, k); }
    int nl = AN0;
#pragma unroll
    for (int k = 0; k < NK; ++k) {
        if (RING && ((k + LA) & 3) == 0 && k + LA < NK) lds_need<0>(fill, have, base + ((k + LA) >> 2) + 1);
        SBAR();
        acc1 = ACT_A ? MFMA(f[k], w1[k], acc1) : MFMA(w1[k], f[k], acc1);
        SBAR();
        if (k + LA < NK) WREAD(w1, b1, b1h, k + LA);
        if (nl < NK && nl < k + APD) { ALOAD(nl); ++nl; }
        SBAR();
        acc0 = ACT_A ? MFMA(f[k], w0[k], acc0) : MFMA(w0[k], f[k], acc0);
        SBAR();
        if (RING && (k & 3) == 3 && (k >> 2) + 8 < NST) lds_post(prog, base + (k >> 2) + 1);
        if (k + LA < NK) WREAD(w0, b0, b0h, k + LA);
        if (nl < NK && nl < k + 1 + APD) { ALOAD(nl); ++nl; }
        if (k == 0) ISTAMP(ib + 3);
        if ((k & 7) == 7) ISTAMP(ib + 4 + (k >> 3));
    }
    SBAR();
#undef WREAD
#undef ALOAD
}
DI void exchange(lptr lds, int lane, int w, int partner, f32x16& acc0, const f32x16& acc1) {
    LAS f32x4* dst = (LAS f32x4*)(lds + L_SCR + w * 4096 + lane * 16);
#pragma unroll
    for (int i = 0; i < 4; ++i) dst[i * 64] = f32x4{acc1[4 * i], acc1[4 * i + 1], acc1[4 * i + 2], acc1[4 * i + 3]};
    BAR();
    LAS const f32x4* src = (LAS const f32x4*)(lds + L_SCR + partner * 4096 + lane * 16);
#pragma unroll
    for (int i = 0; i < 4; ++i) { const f32x4 v = src[i * 64]; acc0[4 * i] += v[0]; acc0[4 * i + 1] += v[1]; acc0[4 * i + 2] += v[2]; acc0[4 * i + 3] += v[3]; }
}

struct QkvAcc { f32x16 qk, v; };
struct WFrag { bf16x8 qk, v; };
DI void qkv_step(QkvAcc& a, bf16x8 x, const WFrag& f, WFrag& n, bool more, lptr pqk, lptr pv, int o0, int o1) {
    SBAR(); a.qk = MFMA(f.qk, x, a.qk); SBAR();
    if (more) n.qk = tr_frag(pqk + o0, pqk + o1);
    SBAR(); a.v = MFMA(x, f.v, a.v); SBAR();
    if (more) n.v = tr_frag(pv + o0, pv + o1);
}
DI u32x2 row_partial(const unsigned (&x)[8]) {
    float sum = 0.f, sq = 0.f;
#pragma unroll
    for (int i = 0; i < 8; ++i) { const float a = bf_lo(x[i]), b = bf_hi(x[i]); sum += a + b; sq = __builtin_fmaf(a, a, __builtin_fmaf(b, b, sq)); }
    const unsigned t = __float_as_uint(half_sum2(sum, sq));
    const auto sw = __builtin_amdgcn_permlane32_swap(t, t, false, false);
    return u32x2{sw[0], sw[1]};
}
DI f32x4 record_total(const u32x4 (&p)[4]) {
    f32x4 t;
#pragma unroll
    for (int j = 0; j < 4; ++j) {
        const unsigned v = __float_as_uint((__uint_as_float(p[0][j]) + __uint_as_float(p[1][j])) + (__uint_as_float(p[2][j]) + __uint_as_float(p[3][j])));
        const auto a = __builtin_amdgcn_permlane32_swap(v, v, false, false);
        const unsigned u = __float_as_uint(__uint_as_float(a[0]) + __uint_as_float(a[1]));
        const auto b = __builtin_amdgcn_permlane16_swap(u, u, false, false);
        t[j] = __uint_as_float(b[0]) + __uint_as_float(b[1]);
    }
    return t;
}
DI void colsum_step(float& cq, float& cv, const WFrag& f) {
    const u32x4 a = __builtin_bit_cast(u32x4, f.qk), b = __builtin_bit_cast(u32x4, f.v);
#pragma unroll
    for (int i = 0; i < 4; ++i) { cq = dot2(a[i], 0x3f803f80u, cq); cv = dot2(b[i], 0x3f803f80u, cv); }
}
DI void colsum_final(lptr lds, int lane) {
    const int n = lane < 48 ? lane : lane - 16;
    const f32x4 a = *(LAS const f32x4*)(lds + L_CSP + n * 32), b = *(LAS const f32x4*)(lds + L_CSP + n * 32 + 16);
    *(LAS float*)(lds + L_CSF + n * 4) = ((a[0] + a[1]) + (a[2] + a[3])) + ((b[0] + b[1]) + (b[2] + b[3]));
}

#define XE(r) "v_exp_f32 v" #r ", v" #r "\n\t"
#define SMF(d, a, b) "v_smfmac_f32_32x32x32_bf16 v[" d "], v[" a "], v[" b "], v173\n\t"
#define ATT_HEAD XE(66) XE(67) XE(68) XE(69) "v_mfma_f32_32x32x16_bf16 v[80:95], v[160:163], v[156:159], v[0:15]\n\t"
#define ATT_FULL(C0, C1, N0, N1) ATT_HEAD \
    "ds_read_b128 v[160:163], %[kp] offset:%[ko]\n\t" XE(70) XE(71) "ds_read_b128 v[" N0 "], %[vp] offset:%[vo]\n\t" XE(72) XE(73) SMF("32:47", C0, "64:71") \
    "ds_read_b128 v[" N1 "], %[vp] offset:%[vo2]\n\t" XE(74) XE(75) XE(76) XE(77) XE(78) XE(79) XE(80) XE(81) SMF("32:47", C1, "72:79") \
    XE(82) XE(83) XE(84) "s_waitcnt lgkmcnt(0)\n\tv_mfma_f32_32x32x16_bf16 v[64:79], v[160:163], v[152:155], v[48:63]\n\t" \
    XE(85) XE(86) XE(87) XE(88) XE(89) SMF("16:31", C0, "80:87") \
    XE(90) XE(91) XE(92) XE(93) XE(94) XE(95) XE(64) XE(65) SMF("16:31", C1, "88:95")
#define ATT_LAST(C0, C1) ATT_HEAD \
    XE(70) XE(71) XE(72) XE(73) SMF("32:47", C0, "64:71") XE(74) XE(75) XE(76) XE(77) XE(78) XE(79) "s_nop 0\n\t" XE(80) XE(81) SMF("32:47", C1, "72:79") \
    XE(82) XE(83) XE(84) XE(85) XE(86) XE(87) XE(88) XE(89) SMF("16:31", C0, "80:87") \
    XE(90) XE(91) XE(92) XE(93) XE(94) XE(95) "s_nop 1\n\t" SMF("16:31", C1, "88:95") "s_nop 11"
#define ATT_OPS(KO, VO) \
    : "+{v[64:79]}"(sA), "+{v[80:95]}"(sB), "+{v[32:47]}"(o), "+{v[16:31]}"(o2), "+{v[160:163]}"(kf), \
      "+{v[164:167]}"(v0), "+{v[168:171]}"(v1), "+{v[176:179]}"(v2), "+{v[180:183]}"(v3) \
    : "{v[48:63]}"(cneg), "{v[0:15]}"(cneg2), "{v[152:155]}"(qf), "{v[156:159]}"(qf2), "{v173}"(idx), \
      [kp] "v"(kp), [vp] "v"(vp), [ko] "i"(KO), [vo] "i"(VO), [vo2] "i"((VO) + 512) : "memory"
#define ATT_EVEN(KO, VO) asm volatile(ATT_FULL("164:167", "168:171", "176:179", "180:183") ATT_OPS(KO, VO))
#define ATT_ODD(KO, VO) asm volatile(ATT_FULL("176:179", "180:183", "164:167", "168:171") ATT_OPS(KO, VO))
#define ATT_ODD_LAST() asm volatile(ATT_LAST("176:179", "180:183") ATT_OPS(0, 0))
DI f32x16 neg_row_max(f32x16 s) {
    float m = __builtin_fmaxf(s[0], s[1]);
#pragma unroll
    for (int i = 2; i < 16; i += 2) m = __builtin_fmaxf(__builtin_fmaxf(m, s[i]), s[i + 1]);
    const auto sw = __builtin_amdgcn_permlane32_swap(__float_as_uint(m), __float_as_uint(m), false, false);
    m = -__builtin_fmaxf(__uint_as_float(sw[0]), __uint_as_float(sw[1]));
    f32x16 c;
#pragma unroll
    for (int i = 0; i < 16; ++i) c[i] = m;
    PIN(c);
    return c;
}

__global__ __launch_bounds__(512, 2) void fused_fwd(const void* __restrict__ xin, const void* __restrict__ wqkv, const void* __restrict__ wo,
                                                    const void* __restrict__ w1, const unsigned short* __restrict__ b1, const void* __restrict__ w2,
                                                    const unsigned short* __restrict__ b2, unsigned short* __restrict__ out, char* wsp) {
    __shared__ __attribute__((aligned(16))) char lds_raw[L_TOTAL];
    const lptr lds = (lptr)lds_raw;
    const unsigned lds0 = (unsigned)(uintptr_t)lds;
    const int w_ = __builtin_amdgcn_readfirstlane(threadIdx.x >> 6);
#define PHASE_IDS \
    const int lane = opaque(threadIdx.x & 63), l31 = lane & 31, hh = lane >> 5; \
    const int w = sopaque(w_), bid = sopaque(blockIdx.x), g = bid & 7, r = bid >> 3; \
    const int st64 = g * 4 + (r >> 3), dt64 = r & 7; \
    const int dt1 = (g & 1) * 4 + (r & 3), ft1 = (g >> 1) * 7 + (r >> 2); \
    const int own_s32 = st64 * 2 + (w & 1), own_d32 = dt64 * 2 + ((w >> 1) & 1); \
    (void)l31; (void)hh; (void)dt1; (void)ft1; (void)own_s32; (void)own_d32;
    const bool w1_on = (blockIdx.x >> 3) < 28;
    const rsrc_t ws = mk_rsrc(wsp, WS_END);

#define ISTARG(b)
    unsigned xres[8] = {};
    unsigned tag_lo = 0, tag_hi = 0;
    u32x2 nonce = {};
    u32x4 wc[12];
#define UNDEF_WC() do { _Pragma("unroll") for (int i_ = 0; i_ < 12; ++i_) asm volatile("" : "=v"(wc[i_])); } while (0)
    UNDEF_WC();

#pragma unroll 1
    for (int L = 0; L < 2; ++L) {
        bf16x8 qf;
        {
            PHASE_IDS
            const int gq = (lane >> 4) & 1, q = (lane >> 2) & 3, p = lane & 3;
            const unsigned kv_soff = WS_KV + (L * 32 + r) * 131072;
            STAMP(0);
            QkvAcc a; a.qk = f32x16{}; a.v = f32x16{};
            u32x4 sp[4];
            float rstd, mr, csv;
            f32x4 cs[4], rs[4], ms[4];
            asm volatile("" : "=v"(rstd), "=v"(mr), "=v"(rs[0]), "=v"(rs[1]), "=v"(rs[2]), "=v"(rs[3]), "=v"(ms[0]), "=v"(ms[1]), "=v"(ms[2]), "=v"(ms[3]));
#define LN_PREP() do { \
                const f32x4 tot = record_total(sp); \
                const float m0 = tot[0] * (1.f / 512.f), m1 = tot[2] * (1.f / 512.f); \
                const float r0 = __builtin_amdgcn_rsqf(__builtin_fmaxf(tot[1] * (1.f / 512.f) - m0 * m0, 0.f) + LN_EPS); \
                const float r1 = __builtin_amdgcn_rsqf(__builtin_fmaxf(tot[3] * (1.f / 512.f) - m1 * m1, 0.f) + LN_EPS); \
                const lptr sst = lds + L_SST + w * 256; \
                *(LAS f32x2*)(sst + hh * 128 + (lane & 15) * 8) = hh ? f32x2{m0 * r0, m1 * r1} : f32x2{r0, r1}; \
                rstd = *(LAS const float*)(sst + l31 * 4); mr = *(LAS const float*)(sst + 128 + l31 * 4); \
                _Pragma("unroll") for (int i = 0; i < 4; ++i) { \
                    rs[i] = *(LAS const f32x4*)(sst + (8 * i + 4 * hh) * 4); ms[i] = *(LAS const f32x4*)(sst + 128 + (8 * i + 4 * hh) * 4); } \
                SBAR(); } while (0)
#define CS_READ() do { \
                _Pragma("unroll") for (int i = 0; i < 4; ++i) cs[i] = *(LAS const f32x4*)(lds + L_CSF + (8 * i + 4 * hh) * 4); \
                csv = *(LAS const float*)(lds + L_CSF + 128 + (lane & 15) * 4); \
                SBAR(); } while (0)
            const unsigned sp_soff = WS_SP + (L * 64 + g * 8 + w) * 4096;
            if (L == 0) {
                const rsrc_t rx = mk_rsrc(xin, 2048 * 1024), rw = mk_rsrc(wqkv, 512 * 3072);
                unsigned wv[3];
#pragma unroll
                for (int ty = 0; ty < 3; ++ty) {
                    const int o = ty * 1024 + lane * 16, rg = o / 384, rem = o % 384;
                    wv[ty] = (4 * rg + ((rem >> 5) & 3)) * 3072 + (rem >> 7) * 1024 + (rem & 16);
                }
#define WQUART(j) do { if (w >= 4) { _Pragma("unroll") for (int ty = 0; ty < 3; ++ty) \
                       dma<1, 0, 0>(lds0 + L_WIMG + (4 * (j) + w - 4) * 3072 + ty * 1024, wv[ty], rw, (4 * (j) + w - 4) * 98304 + r * 32); } } while (0)
                const unsigned tokb = (g * 256 + w * 32) * 1024, stg = w < 5 ? 65536 + w * 12288 : (w == 5 ? 0 : L_REG + (w - 6) * 12288);
                const unsigned vA = (lane >> 3) * 1024 + (((lane & 7) ^ (lane >> 4)) << 4), vB = vA ^ 64;
#define XCHUNK(c) do { dma<2, 2048, 16384>(lds0 + stg + ((c) % 3) * 4096, vA, rx, tokb + (c) * 128); \
                       dma<2, 2048, 16384>(lds0 + stg + ((c) % 3) * 4096 + 1024, vB, rx, tokb + 8192 + (c) * 128); } while (0)
#define ROLE_WAIT(a, b) do { if (w >= 4) vm_wait<a>(); else vm_wait<b>(); } while (0)
                u32x2 xr[4];
                WQUART(0); XCHUNK(0);
                asm volatile("s_nop 4\n\tbuffer_load_dwordx2 %0, off, %1, 0 sc1" : "=v"(nonce) : "s"(ws) : "memory");
                if (w < 4) {
                    asm volatile("s_nop 4\n\tbuffer_load_dwordx2 %0, %4, %5, %6 offen\n\tbuffer_load_dwordx2 %1, %4, %5, %6 offen offset:16\n\t"
                                 "buffer_load_dwordx2 %2, %4, %5, %6 offen offset:32\n\tbuffer_load_dwordx2 %3, %4, %5, %6 offen offset:48"
                                 : "=&v"(xr[0]), "=&v"(xr[1]), "=&v"(xr[2]), "=&v"(xr[3]) : "v"(l31 * 1024 + hh * 8), "s"(rx), "s"(own_s32 * 32768 + own_d32 * 64) : "memory");
                } else {
#pragma unroll
                    for (int i = 0; i < 4; ++i) asm volatile("" : "=v"(xr[i]));
                }
#define WTOUCH(i0) do { const int i_ = (i0) + lane, qd_ = i_ / 96, rm_ = i_ % 96; \
                       touch<1, 0>(lds0 + L_SST + w * 256, (qd_ * 128 + rm_ / 24) * 3072 + (rm_ % 24) * 128, rw, r * 4 * 3072); } while (0)
                if (QTOUCH == 1 && w == 3) touch<1, 0>(lds0 + L_SST + w * 256, lane * 24576, rw, (r & 7) * 3072);
                if (QTOUCH == 2) {
                    if (w == 0) touch<1, 0>(lds0 + L_SST + w * 256, (lane & 7) * 1024 + (lane >> 3) * 128, rx, (g * 256 + r * 8) * 1024);
                    else if (w < 4) WTOUCH(192 + (w - 1) * 64);
                }
                XCHUNK(1); WQUART(1); XCHUNK(2);
                if (CODEPF && w == 3) {
                    const char* pc;
                    asm volatile("s_getpc_b64 %0" : "=s"(pc));
                    touch<1, 0>(lds0 + L_SST + w * 256, (lane & 15) * 128, mk_rsrc((const char*)((uintptr_t)pc & ~(uintptr_t)127), CODEPF * 1024), r * 2048);
                }
                const lptr pqk = lds + L_WIMG + hh * 768 + gq * 128 + q * 32 + p * 8, pv = lds + L_WIMG + hh * 768 + 256 + q * 32 + p * 8;
                lptr px[4];
#pragma unroll
                for (int t = 0; t < 4; ++t) px[t] = lds + stg + l31 * 128 + (((2 * t + hh) ^ ((l31 >> 1) & 7)) << 4);
                WFrag wf[2];
                u32x2 fl;
                const unsigned f0r = opaque(WS_F0 + r * 128 + (lane & 7) * 8), f5r = opaque(WS_F5 + (g * 8 + w) * 128 + (lane & 15) * 8), flr = hh ? f0r : f5r;
#pragma unroll
                for (int c = 0; c < 8; ++c) {
                    if (c == 0) ISTAMP(54);
                    if (c <= 5) ROLE_WAIT(11, 8); else if (c == 6) vm_wait<0>();
                    ISTAMP(55 + c);
                    if (c == 0) { BAR(); STAMP(1); wf[0].qk = tr_frag(pqk, pqk + 384); wf[0].v = tr_frag(pv, pv + 384); }
                    bf16x8 xf[4];
#pragma unroll
                    for (int t = 0; t < 4; ++t) xf[t] = lds_frag(px[t] + (c % 3) * 4096);
                    if (c == 5) asm volatile("s_nop 4\n\tbuffer_load_dwordx2 %0, %1, %2, 0 offen sc1" : "=v"(fl) : "v"(flr), "s"(ws) : "memory");
                    if (c + 3 < 8) { asm volatile("s_waitcnt lgkmcnt(0)" : "+v"(xf[0]), "+v"(xf[1]), "+v"(xf[2]), "+v"(xf[3]) :: "memory"); XCHUNK(c + 3); }
                    if (c == 1) WQUART(2);
                    if (c == 3) WQUART(3);
                    if (c == 0) {
                        asm volatile("" : "+v"(nonce), "+v"(xr[0]), "+v"(xr[1]), "+v"(xr[2]), "+v"(xr[3]));
                        unsigned long long z = ((unsigned long long)__builtin_amdgcn_readfirstlane(nonce[1]) << 32) | (unsigned)__builtin_amdgcn_readfirstlane(nonce[0]);
                        z = z * 0x9E3779B97F4A7C15ull + 0x632BE59BD9B4E019ull;
                        z ^= z >> 29; z *= 0xBF58476D1CE4E5B9ull; z ^= z >> 32;
                        tag_lo = (unsigned)z; tag_hi = (unsigned)(z >> 32);
#pragma unroll
                        for (int i = 0; i < 4; ++i) { xres[2 * i] = xr[i][0]; xres[2 * i + 1] = xr[i][1]; }
                        if (w < 4) {
                            __builtin_amdgcn_raw_buffer_store_b64(row_partial(xres), ws, hh ? OOB : l31 * 8u, WS_SP + (own_s32 * 16 + own_d32) * 256, AUX_SC1);
                            __builtin_amdgcn_raw_buffer_store_b64(u32x2{}, ws, l31 * 8 + hh * 0x40000, WS_ST + (own_s32 * 16 + own_d32) * 256, AUX_SC1);
                            const unsigned c1a = opaque(WS_C1 + (r * 8 + g) * 128 + lane * 32768), c3a = opaque(WS_C3 + bid * 256 + (lane - 3) * 128);
                            const unsigned c2a = sopaque(WS_C2 + (((r >> 4) * 64 + g * 8) * 2 + (r & 15)) * 128);
                            const unsigned ca = lane < 2 ? c1a : (lane == 2 ? c2a : c3a);
                            __builtin_amdgcn_raw_buffer_store_b32(0u, ws, (w == 0 && lane < 5) ? ca : OOB, 0, AUX_SC1);
                        }
                    }
                    if (c == 6) {
                        asm volatile("" : "+v"(fl));
                        if (!__all(((fl[0] ^ tag_lo) | (fl[1] ^ tag_hi)) == 0)) wait64(ws, flr, 0, tag_lo, tag_hi);
                        asm volatile("" ::: "memory");
#pragma unroll
                        for (int i = 0; i < 4; ++i) sp[i] = __builtin_amdgcn_raw_buffer_load_b128(ws, lane * 16 + i * 1024, sp_soff, AUX_SC1);
                        SBAR();
                    }
                    if (c == 6) {
                        float cq = 0.f, cv = 0.f;
#pragma unroll
                        for (int j = 0; j < 4; ++j) {
                            const int o = (4 * w + j) * 1536;
                            WFrag e; e.qk = tr_frag(pqk + o, pqk + o + 384); e.v = tr_frag(pv + o, pv + o + 384);
                            colsum_step(cq, cv, e);
                        }
                        *(LAS float*)(lds + L_CSP + (lane < 48 ? lane : lane - 16) * 32 + w * 4) = half_sum2(cq, cv);
                        if (w >= 4) {
#pragma unroll
                            for (int i = 0; i < 12; ++i) wc[i] = *(LAS const u32x4*)(lds + L_WIMG + (w - 4) * 12288 + i * 1024 + lane * 16);
                        }
                    }
                    if (c == 7) { BAR(); if (w == 0) colsum_final(lds, lane); LN_PREP(); }
#pragma unroll
                    for (int t = 0; t < 4; ++t) {
                        const int k = 4 * c + t;
                        if (k == 7) { ROLE_WAIT(15, 63); BAR(); }
                        if (k == 15) { ROLE_WAIT(11, 63); BAR(); }
                        if (k == 23) { ROLE_WAIT(4, 63); BAR(); }
                        qkv_step(a, xf[t], wf[k & 1], wf[(k + 1) & 1], k < 31, pqk, pv, (k + 1) * 1536, (k + 1) * 1536 + 384);
                    }
                    if (c == 1 && w < 4) {
                        VMWAIT(0);
                        const unsigned f5a = sopaque(WS_F5 + own_s32 * 128 + own_d32 * 8), f0a = sopaque(w == 0 ? WS_F0 + r * 128 + g * 8 : OOB);
                        __builtin_amdgcn_raw_buffer_store_b64(u32x2{tag_lo, tag_hi}, ws, lane == 0 ? f5a : (lane == 1 ? f0a : OOB), 0, AUX_SC1);
                    }
                }
                ISTAMP(63);
                BAR();
                CS_READ();
#undef WQUART
#undef ROLE_WAIT
#undef XCHUNK
            } else {
                if (w >= 4) {
#pragma unroll
                    for (int i = 0; i < 12; ++i) *(LAS u32x4*)(lds + L_WIMG + (w - 4) * 12288 + i * 1024 + lane * 16) = wc[i];
                }
                BAR();
                wait_cnt(ws, WS_C5 + ((g * 8 + w) * 2 + (r & 1)) * 128, 16, lane);
                STAMP(1);
                ISTAMP(32);
                bf16x8 xf[32];
                const unsigned x_soff = WS_X2 + (g * 8 + w) * 32768;
#define XLOAD(k) xf[k] = ld_frag(ws, lane * 16 + ((k) & 3) * 1024, x_soff + ((k) >> 2) * 4096)
#pragma unroll
                for (int i = 0; i < 4; ++i) sp[i] = __builtin_amdgcn_raw_buffer_load_b128(ws, lane * 16 + i * 1024, sp_soff, AUX_SC1);
#pragma unroll
                for (int k = 0; k < XN0; ++k) XLOAD(k);
                SBAR();
                ISTAMP(33);
                ISTAMP(34);
                const lptr pv = lds + L_WIMG + hh * 384 + q * 32 + p * 8 + 256, pqk = pv + gq * 128 - 256;
                WFrag wf[2];
                wf[0].qk = tr_frag(pqk, pqk + 768); wf[0].v = tr_frag(pv, pv + 768);
                int nl = XN0;
                if (QPRIO) __builtin_amdgcn_s_setprio(3);
#pragma unroll
                for (int k = 0; k < 32; ++k) {
                    qkv_step(a, xf[k], wf[k & 1], wf[(k + 1) & 1], k < 31, pqk, pv, (k + 1) * 1536, (k + 1) * 1536 + 768);
                    if (nl < 32 && nl < k + XPD) { XLOAD(nl); ++nl; }
                    if (nl < 32 && nl < k + 1 + XPD) { XLOAD(nl); ++nl; }
                    if (QPRIO && k == 11) __builtin_amdgcn_s_setprio(2);
                    if (QPRIO && k == 21) __builtin_amdgcn_s_setprio(1);
                    if (QPRIO && k == 27) __builtin_amdgcn_s_setprio(0);
                    if (k == LNK && w < 4) LN_PREP();
                    if (k == LNK2 && w >= 4) LN_PREP();
                    if (k == 24) CS_READ();
                    if (k == 0) ISTAMP(35);
                    if ((k & 3) == 3) ISTAMP(36 + (k >> 2));
                }
#undef XLOAD
                UNDEF_WC();
            }
            STAMP(2);
            if (APRIO) __builtin_amdgcn_s_setprio(3);
            float qk[16], vv[16];
#pragma unroll
            for (int i = 0; i < 16; ++i) qk[i] = a.qk[i] * rstd - mr * cs[i >> 2][i & 3];
            const u32x4 kpk = {pk2(qk[8], qk[9]), pk2(qk[10], qk[11]), pk2(qk[12], qk[13]), pk2(qk[14], qk[15])};
            const unsigned koff = (hh * 32 + ((l31 & 19) | ((l31 & 4) << 1) | ((l31 & 8) >> 1))) * 16;
            st_frag(kpk, ws, koff, kv_soff + (g * 8 + w) * 2048);
#pragma unroll
            for (int i = 0; i < 16; ++i) vv[i] = a.v[i] * rs[i >> 2][i & 3] - ms[i >> 2][i & 3] * csv;
            u32x4 vpk;
#pragma unroll
            for (int i = 0; i < 4; ++i) { const unsigned lo = pk2(vv[2 * i], vv[2 * i + 1]), hi = pk2(vv[8 + 2 * i], vv[9 + 2 * i]); vpk[i] = gq ? hi : lo; }
            const unsigned voff = gq * 512 + hh * 256 + (lane & 15) * 16;
            st_frag(vpk, ws, voff, kv_soff + (g * 8 + w) * 2048 + 1024);
            constexpr float QS = 0.25f * 1.4426950408889634f;
            const u32x4 qpk = {pk2(qk[0] * QS, qk[1] * QS), pk2(qk[2] * QS, qk[3] * QS), pk2(qk[4] * QS, qk[5] * QS), pk2(qk[6] * QS, qk[7] * QS)};
            qf = __builtin_bit_cast(bf16x8, qpk);
            if (L == 0) {
                if (w == 0) *(LAS unsigned*)(lds + L_PROG + (lane & 7) * 4) = 0u;
                const unsigned one = (lane & 15) ? 0u : 0x3f803f80u;
                *(LAS u32x4*)(lds + L_ONES + w * 512 + hh * 256 + (lane & 15) * 16) = u32x4{one, one, one, one};
            }
            *(LAS u32x4*)(lds + w * 1024 + koff) = kpk;
            *(LAS u32x4*)(lds + 8192 + w * 1024 + voff) = vpk;
            *(LAS u32x4*)(lds + L_REG + w * 3072 + lane * 16) = qpk;
            BAR();
            STAMP(3);
        }
        {
            PHASE_IDS
            const int gq = (lane >> 4) & 1;
            const unsigned kv_soff = WS_KV + (L * 32 + r) * 131072;
            const int idx = opaque(0xDDDD);
            lptr kp = lds + (w & 1) * 4096 + lane * 16;
            lptr vp = lds + (gq ? L_ONES : 8192 + (w & 1) * 4096) + hh * 256 + (lane & 15) * 16;
            const int vinc = gq ? 0 : 16384;
            const bf16x8 qf2 = lds_frag(lds + L_REG + (w ^ 1) * 3072 + lane * 16), k0 = lds_frag(lds + lane * 16);
            bf16x8 kf = lds_frag(kp), v0 = lds_frag(vp), v1 = lds_frag(vp + 512), v2, v3;
            asm volatile("" : "=v"(v2), "=v"(v3));
            f32x16 sA = MFMA(k0, qf, f32x16{}), sB = MFMA(k0, qf2, f32x16{}), o = {}, o2 = {};
            const f32x16 cneg = neg_row_max(sA), cneg2 = neg_row_max(sB);
#define KVCHUNK(j) dma_sc1<2, 8192, 1024>(lds0 + (j) * 16384 + w * 1024, lane * 16, ws, kv_soff + (((g + (j)) & 7) * 8 + w) * 2048)
            sA = MFMA(kf, qf, cneg);
            publish<1>(ws, WS_C1 + ((L * 32 + r) * 8 + w) * 128, lane);
            sA[0] = EX(sA[0]); sA[1] = EX(sA[1]);
#define PRIO(n) do { if (APRIO) __builtin_amdgcn_s_setprio(n); } while (0)
            {
                const unsigned c1o = WS_C1 + ((L * 32 + r) * 8 + w) * 128, pv0 = lane == 0 ? 0u : OOB;
                int pc = 0;
#define POLL_ISSUE() asm volatile("s_nop 4\n\tbuffer_atomic_add %0, %1, %2, %3 offen sc0 sc1" : "+v"(pc) : "v"(pv0), "s"(ws), "s"(c1o) : "memory")
#define POLL_READ() asm volatile("s_waitcnt vmcnt(0)" : "+v"(pc) :: "memory")
                PRIO(2);
                ATT_EVEN(1024, 1024);
                POLL_ISSUE();
                ATT_ODD(2048, 2048);
                STAMP(4);
                POLL_READ();
                if (__builtin_amdgcn_readfirstlane(pc) < 8) wait_cnt(ws, c1o, 8, lane);
                STAMP(5);
                KVCHUNK(1);
                PRIO(1);
                ATT_EVEN(3072, 3072);
                KVCHUNK(2); KVCHUNK(3);
                PRIO(0);
                ATT_ODD_LAST();
                vm_wait_bar<4>();
                STAMP(6);
                kp += 16384; vp += vinc;
                kf = lds_frag(kp); v0 = lds_frag(vp); v1 = lds_frag(vp + 512);
                KVCHUNK(4); KVCHUNK(5);
                sA = MFMA(kf, qf, cneg);
                sA[0] = EX(sA[0]); sA[1] = EX(sA[1]);
            }
#pragma unroll
            for (int c = 1; c < 8; ++c) {
                if (c == 1 || c == 3 || c == 4 || c == 7) PRIO(2);
                ATT_EVEN(1024, 1024);
                if (c == 1 || c == 3 || c == 4) PRIO(1);
                ATT_ODD(2048, 2048);
                if (c == 1 || c == 3 || c == 4) PRIO(0); else if (c == 7) PRIO(1);
                ATT_EVEN(3072, 3072);
                if (c == 1) { vm_wait_bar<4>(); KVCHUNK(6); KVCHUNK(7); }
                if (TLBW && c == 2 && L == 0) {
                    if (w == 0) touch<1, 0>(lds0 + L_SST, lane < 24 ? lane * 32768u + 0x7f80u : OOB, ws, 0x40000 + r * 24 * 32768);
                    if (w == 1) touch<1, 0>(lds0 + L_SST + 256, lane < 2 ? lane * 32768u : OOB, mk_rsrc(out, 2048 * 1024), r * 65536);
                    if (w == 2) touch<1, 0>(lds0 + L_SST + 512, lane < 28 ? lane * 128u : OOB, mk_rsrc(b1, 3584), 0);
                    if (w == 3) touch<1, 0>(lds0 + L_SST + 768, lane < 32 ? lane * 128u : OOB, mk_rsrc(b2, 4096), 0);
                }
                if (c == 3) vm_wait_bar<0>();
                if (c == 4) {
                    BAR();
                    dma<8, 1024, 8192>(lds0 + w * 8192, wvoff(lane, 1024), mk_rsrc(wo, 512 * 1024), ((w & 1) * 256 + (w >> 1) * 64) * 1024 + dt64 * 128);
                }
                if (c == WARM1) touch<1, 0>(lds0 + L_SST + w * 256, lane * 3584, mk_rsrc(w1, 2048 * 3584), ((r & 3) * 512 + w * 64) * 3584 + (w1_on ? ft1 : 0) * 128);
                if (c == 2 && L == 0) touch<1, 0>(lds0 + L_SST + w * 256, (lane < 28 ? lane : 0) * 4096, mk_rsrc(w2, 1792 * 4096), (dt64 * 224 + w * 28) * 4096 + st64 * 128);
                if (c < 7) { if (c != 2 && c < 5) PRIO(3); vp += vinc; ATT_ODD(16384, 0); kp += 16384; }
                else { PRIO(0); ATT_ODD_LAST(); }
            }
            STAMP(7);
            VMWAIT(0);
            STAMP(20);
            {
                const lptr mine = lds + L_REG + w * 3072 + lane * 16, other = lds + L_REG + (w ^ 1) * 3072 + lane * 16;
                *(LAS f32x4*)mine = f32x4{o2[0], o2[1], o2[2], o2[3]};
                *(LAS f32x4*)(mine + 1024) = f32x4{o2[4], o2[5], o2[6], o2[7]};
                *(LAS float*)(mine + 2048) = o2[8];
                BAR();
                STAMP(21);
                const f32x4 a0 = *(LAS const f32x4*)other, a1 = *(LAS const f32x4*)(other + 1024);
                o[8] += *(LAS const float*)(other + 2048);
#pragma unroll
                for (int i = 0; i < 4; ++i) { o[i] += a0[i]; o[4 + i] += a1[i]; }
            }
#define O_STORE() do { const float il = __builtin_amdgcn_rcpf(__uint_as_float(__builtin_amdgcn_permlane32_swap(__float_as_uint(o[8]), __float_as_uint(o[8]), false, false)[0])); \
                st_frag(u32x4{pk2(o[0] * il, o[1] * il), pk2(o[2] * il, o[3] * il), pk2(o[4] * il, o[5] * il), pk2(o[6] * il, o[7] * il)}, ws, lane * 16, \
                        WS_O + L * 0x200000 + ((g * 8 + w) * 32 + r) * 1024); SBAR(); } while (0)
            O_STORE();
            STAMP(22);
            float chk = __builtin_fabsf(o[8]);
#pragma unroll
            for (int i = 0; i < 8; ++i) chk += __builtin_fabsf(o[i]);
            if (__builtin_expect(__any(!(chk < 1e36f)), 0)) {
                float m = -__builtin_inff();
                o = f32x16{};
                const u32x4 one4 = {0x3f803f80u, 0x3f803f80u, 0x3f803f80u, 0x3f803f80u};
#pragma unroll 1
                for (int t = 0; t < 64; ++t) {
                    const bf16x8 kk = ld_frag(ws, lane * 16, kv_soff + t * 2048);
                    u32x4 a0 = __builtin_amdgcn_raw_buffer_load_b128(ws, hh * 256 + (lane & 15) * 16, kv_soff + t * 2048 + 1024, AUX_SC1);
                    u32x4 a1 = __builtin_amdgcn_raw_buffer_load_b128(ws, hh * 256 + (lane & 15) * 16, kv_soff + t * 2048 + 1536, AUX_SC1);
                    a0 = gq ? one4 : a0; a1 = gq ? one4 : a1;
                    f32x16 s = MFMA(kk, qf, f32x16{});
                    float tm = m;
#pragma unroll
                    for (int i = 0; i < 16; ++i) tm = __builtin_fmaxf(tm, s[i]);
                    const auto sw = __builtin_amdgcn_permlane32_swap(__float_as_uint(tm), __float_as_uint(tm), false, false);
                    tm = __builtin_fmaxf(__uint_as_float(sw[0]), __uint_as_float(sw[1]));
                    const float al = EX(m - tm);
                    m = tm;
#pragma unroll
                    for (int i = 0; i < 16; ++i) { o[i] *= al; s[i] = EX(s[i] - m); }
                    o = smf(__builtin_bit_cast(bf16x8, a0), lo8(s), o, idx);
                    o = smf(__builtin_bit_cast(bf16x8, a1), hi8(s), o, idx);
                }
                VMWAIT(0);
                O_STORE();
            }
            publish<1>(ws, WS_C2 + ((L * 64 + g * 8 + w) * 2 + (r >> 4)) * 128, lane);
            if (RDBK) touch<1, 0>(lds0 + L_SST + w * 256, (lane & 7) * 128, ws, WS_O + L * 0x200000 + ((g * 8 + w) * 32 + r) * 1024);
            STAMP(8);
        }
        if (w_ < 4) {
            f32x16 acc0, acc1;
            u32x2 bb[4];
            unsigned b2u;
            __builtin_amdgcn_s_setprio(CPRIO);
            {
                PHASE_IDS
                const int kh = w >> 1;
#pragma unroll
                for (int i = 0; i < 4; ++i) bb[i] = *(const u32x2*)(b1 + ((w1_on ? ft1 : 0) * 2 + kh) * 32 + 8 * i + 4 * hh);
                b2u = b2[own_s32 * 32 + l31];
#pragma unroll
                for (int i = 0; i < 8; ++i) { acc0[2 * i] = bf_lo(xres[i]); acc0[2 * i + 1] = bf_hi(xres[i]); }
                PIN(acc0);
                (wait_cnt(ws, WS_C2 + ((L * 64 + own_s32) * 2 + kh) * 128, 16, lane));
                STAMP(9);
                gemm_compute<4, 0, false, false, 48>(lds, lane, kh, 0, ws, WS_O + L * 0x200000 + (own_s32 * 32 + kh * 16) * 1024, 0, acc0, acc1 ISTARG(48));
                STAMP(10);
                exchange(lds, lane, w, w ^ 2, acc0, acc1);
#pragma unroll
                for (int i = 0; i < 8; ++i) xres[i] = pk2(acc0[2 * i], acc0[2 * i + 1]);
                const unsigned st_soff = WS_ST + (L * 64 + own_s32) * 4096, tg = L + 1;
                {
                    const u32x2 rp = row_partial(xres);
                    __builtin_amdgcn_raw_buffer_store_b64(u32x2{rp[0], (rp[1] & ~3u) | tg}, ws, hh ? OOB : l31 * 8u, st_soff + own_d32 * 256, AUX_SC1);
                    if (STCNT) arrive<1>(ws, WS_C6 + (L * 64 + own_s32) * 128, lane);
                }
                STAMP(11);
                const lptr tp = lds + L_SCR + (w ^ 2) * 4096;
                *(LAS u32x4*)(tp + l31 * 64 + hh * 32) = u32x4{xres[0], xres[1], xres[2], xres[3]};
                *(LAS u32x4*)(tp + l31 * 64 + hh * 32 + 16) = u32x4{xres[4], xres[5], xres[6], xres[7]};
                const lptr rp = tp + (4 * hh + ((lane >> 2) & 3)) * 64 + ((lane >> 4) & 1) * 32 + (lane & 3) * 8;
                float xt[16];
#pragma unroll
                for (int t = 0; t < 2; ++t) {
                    const u32x4 v = __builtin_bit_cast(u32x4, tr_frag(rp + t * 1024, rp + t * 1024 + 512));
#pragma unroll
                    for (int i = 0; i < 4; ++i) { xt[8 * t + 2 * i] = bf_lo(v[i]); xt[8 * t + 2 * i + 1] = bf_hi(v[i]); }
                }
#pragma unroll
                for (int i = 0; i < 16; ++i) PIN(xt[i]);
                if (STCNT) NOTE(30, wait_cnt(ws, WS_C6 + (L * 64 + own_s32) * 128, 16, lane)); else VMWAIT(0);
                u32x4 gv[4];
                unsigned it;
#pragma unroll 1
                for (it = 0; it < SPIN_CAP; ++it) {
                    unsigned bad = 0;
#pragma unroll
                    for (int i = 0; i < 4; ++i) gv[i] = __builtin_amdgcn_raw_buffer_load_b128(ws, lane * 16 + i * 1024, st_soff, AUX_SC1);
#pragma unroll
                    for (int i = 0; i < 4; ++i) bad |= (gv[i][1] ^ tg) | (gv[i][3] ^ tg);
                    if (__all((bad & 3) == 0)) break;
                    POLL_PAUSE();
                }
                (void)it;
                STAMP(12);
                {
                    const f32x4 tot = record_total(gv);
                    const float m0 = tot[0] * (1.f / 512.f), m1 = tot[2] * (1.f / 512.f);
                    const float r0 = __builtin_amdgcn_rsqf(__builtin_fmaxf(tot[1] * (1.f / 512.f) - m0 * m0, 0.f) * (1.f + LN_EPS) + LN_EPS * LN_EPS);
                    const float r1 = __builtin_amdgcn_rsqf(__builtin_fmaxf(tot[3] * (1.f / 512.f) - m1 * m1, 0.f) * (1.f + LN_EPS) + LN_EPS * LN_EPS);
                    *(LAS f32x2*)(tp + 2048 + hh * 128 + (lane & 15) * 8) = hh ? f32x2{m0 * r0, m1 * r1} : f32x2{r0, r1};
                }
                const unsigned hv = (hh * 32 + ((l31 & 3) | ((l31 & 12) << 1) | ((l31 & 16) >> 2))) * 16;
#pragma unroll
                for (int t = 0; t < 2; ++t) {
                    u32x4 h;
#pragma unroll
                    for (int j = 0; j < 2; ++j) {
                        const f32x4 sc = *(LAS const f32x4*)(tp + 2048 + (16 * t + 8 * j + 4 * hh) * 4), ms = *(LAS const f32x4*)(tp + 2176 + (16 * t + 8 * j + 4 * hh) * 4);
                        const float* x = xt + 8 * t + 4 * j;
                        h[2 * j] = pk2(x[0] * sc[0] - ms[0], x[1] * sc[1] - ms[1]); h[2 * j + 1] = pk2(x[2] * sc[2] - ms[2], x[3] * sc[3] - ms[3]);
                    }
                    st_frag(h, ws, hv, WS_H2 + L * 0x200000 + (own_d32 * 128 + own_s32 * 2 + t) * 1024);
                }
                STAMP(23);
                publish<2>(ws, WS_C3 + ((L * 16 + own_d32) * 2 + (own_s32 >> 5)) * 256, lane);
                if (L == 0 && bid == 0 && w == 0)
                    __builtin_amdgcn_raw_buffer_store_b64(u32x2{nonce[0] + 1u, nonce[1] + (nonce[0] == 0xffffffffu)}, ws, lane == 0 ? 0u : OOB, WS_NONCE, AUX_SC1);
                STAMP(13);
            }
            if (w1_on) {
                PHASE_IDS
                const int kh = w >> 1, d32 = dt1 * 2 + (w & 1), f32t = ft1 * 2 + kh;
#pragma unroll
                for (int i = 0; i < 16; ++i) { const unsigned bw = bb[i >> 2][(i >> 1) & 1]; acc0[i] = (i & 1) ? bf_hi(bw) : bf_lo(bw); }
                PIN(acc0);
                NOTE(28, wait_cnt(ws, WS_C3 + ((L * 16 + d32) * 2 + kh) * 256 + (g & 2) * 64, 32, lane));
                STAMP(14);
                gemm_compute<16, 4, false, true, 0>(lds, lane, kh, w & 1, ws, WS_H2 + L * 0x200000 + (d32 * 128 + kh * 64) * 1024, L * 32, acc0, acc1 ISTARG(0));
                STAMP(15);
                exchange(lds, lane, w, w ^ 2, acc0, acc1);
                float u[16];
#pragma unroll
                for (int i = 0; i < 16; ++i) {
                    const float z = acc0[i];
                    const float e = EX(z * __builtin_fmaf(z * z, -0.10294324f, -2.30220818f));
                    u[i] = z * __builtin_amdgcn_rcpf(1.f + e);
                }
#pragma unroll
                for (int t = 0; t < 2; ++t)
                    st_frag(u32x4{pk2(u[8 * t], u[8 * t + 1]), pk2(u[8 * t + 2], u[8 * t + 3]), pk2(u[8 * t + 4], u[8 * t + 5]), pk2(u[8 * t + 6], u[8 * t + 7])}, ws, lane * 16,
                            WS_U + L * 0x200000 + (d32 * 112 + f32t * 2 + t) * 1024);
                STAMP(24);
                publish<2>(ws, WS_C4 + ((L * 16 + d32) * 2 + ((f32t + 4) >> 5)) * 256, lane);
                STAMP(16);
            }
            else if (ASSIST) {
                PHASE_IDS
                const int kh = w >> 1, d32 = dt1 * 2 + (w & 1);
                wait_cnt(ws, WS_C3 + ((L * 16 + d32) * 2 + kh) * 256 + (g & 2) * 64, 32, lane);
                touch<8 - ASSIST, 8192>(lds0 + L_SST + w * 256, lane * 128, ws, WS_H2 + L * 0x200000 + (d32 * 128 + kh * 64 + 8 * ASSIST) * 1024);
            }
            {
                PHASE_IDS
                const int kh = w & 1;
                const float bias = __uint_as_float(b2u << 16);
#pragma unroll
                for (int i = 0; i < 8; ++i) { acc0[2 * i] = bf_lo(xres[i]) + bias; acc0[2 * i + 1] = bf_hi(xres[i]) + bias; }
                PIN(acc0);
                NOTE(29, wait_cnt(ws, WS_C4 + ((L * 16 + own_d32) * 2 + kh) * 256 + (g & 1) * 128, 28, lane));
                STAMP(17);
                gemm_compute<14, 4, true, true, 16>(lds, lane, kh, w >> 1, ws, WS_U + L * 0x200000 + (own_d32 * 112 + kh * 56) * 1024, L * 32 + 16, acc0, acc1 ISTARG(16));
                STAMP(18);
                exchange(lds, lane, w, w ^ 1, acc0, acc1);
#pragma unroll
                for (int i = 0; i < 8; ++i) xres[i] = pk2(acc0[2 * i], acc0[2 * i + 1]);
                if (L == 0) {
#pragma unroll
                    for (int t = 0; t < 2; ++t)
                        st_frag(u32x4{xres[4 * t], xres[4 * t + 1], xres[4 * t + 2], xres[4 * t + 3]}, ws, lane * 16, WS_X2 + (own_s32 * 32 + own_d32 * 2 + t) * 1024);
                    __builtin_amdgcn_raw_buffer_store_b64(row_partial(xres), ws, hh ? OOB : l31 * 8u, WS_SP + ((64 + own_s32) * 16 + own_d32) * 256, AUX_SC1);
                    STAMP(25);
                    publish<2>(ws, WS_C5 + own_s32 * 256, lane);
                    if (RDBK) {
                        const unsigned xa = opaque(WS_X2 + (own_s32 * 32 + own_d32 * 2) * 1024 + (lane & 15) * 128), sa = opaque(WS_SP + ((64 + own_s32) * 16 + own_d32) * 256 + (lane & 1) * 128);
                        touch<1, 0>(lds0 + L_SST + w * 256, hh ? sa : xa, ws, 0);
                    }
                } else {
                    char* op = (char*)out + (own_s32 * 32 + l31) * 1024 + own_d32 * 64 + hh * 16;
#pragma unroll
                    for (int t = 0; t < 2; ++t) {
                        const auto s0 = __builtin_amdgcn_permlane32_swap(xres[4 * t], xres[4 * t + 2], false, false);
                        const auto s1 = __builtin_amdgcn_permlane32_swap(xres[4 * t + 1], xres[4 * t + 3], false, false);
                        if (OUTAUX < 0) *(u32x4*)(op + t * 32) = u32x4{s0[0], s1[0], s0[1], s1[1]};
                        else {
                            const u32x4 v = {s0[0], s1[0], s0[1], s1[1]};
                            __builtin_amdgcn_raw_buffer_store_b128(v, mk_rsrc(out, 2048 * 1024), l31 * 1024 + hh * 16 + t * 32, own_s32 * 32768 + own_d32 * 64, OUTAUX < 0 ? 0 : OUTAUX);
                            asm volatile("s_nop 1" ::"v"(v) : "memory");
                        }
                    }
                }
                STAMP(19);
            }
            __builtin_amdgcn_s_setprio(0);
            UNDEF_WC();
        } else {
            PHASE_IDS
            const int lw = w - 4;
            const rsrc_t r1 = mk_rsrc(w1, 2048 * 3584), r2 = mk_rsrc(w2, 1792 * 4096);
            const unsigned v1o = wvoff(lane, 3584), v2o = wvoff(lane, 4096), lb = lds0 + lw * 4096;
            const unsigned g1 = ((lw >> 1) * 1024 + (lw & 1) * 32) * 3584 + ft1 * 128, g2 = ((lw >> 1) * 896 + (lw & 1) * 32) * 4096 + st64 * 128;
            const lptr lp = lds + lw * 4096 + lane * 16, fill = lds + L_FILL + lw * 4, prog = lds + L_PROG + (lw >> 1) * 8;
            u32x4 rg[8][4];
#define W2WARM() do { if (WARM2) touch<1, 0>(lds0 + L_SST + w * 256, (lane < 56 ? lane : 0) * 4096, r2, (dt64 * 224 + lw * 56) * 4096 + st64 * 128); } while (0)
            if (w1_on) {
                ring_prefetch<0, 4, 4, 3584, TS1, 0>(lb, rg, r1, v1o, g1, fill, L * 32 ISTARG(0));
                BAR();
                ring_prefetch<4, 16, 4, 3584, TS1, 0>(lb, rg, r1, v1o, g1, fill, L * 32 ISTARG(0));
                W2WARM();
                ring_refill<16, 4, 0>(lp, rg, prog, fill, L * 32 ISTARG(0));
            }
            else W2WARM();
            BAR();
            ring_prefetch<0, 14, 4, 4096, TS2, 16>(lb, rg, r2, v2o, g2, fill, L * 32 + 16 ISTARG(16));
            ring_refill<14, 4, 16>(lp, rg, prog, fill, L * 32 + 16 ISTARG(16));
            BAR();
        }
    }
}

extern "C" void kernel_launch(void* const* d_in, const int* in_sizes, int n_in, void* d_out, int out_size, void* d_ws, size_t ws_size,
                              hipStream_t stream) {
    fused_fwd<<<256, 512, 0, stream>>>(d_in[0], d_in[1], d_in[2], d_in[4], (const unsigned short*)d_in[5], d_in[6], (const unsigned short*)d_in[7],
                                       (unsigned short*)d_out, (char*)d_ws + WSOFF);
}
```

```cpp
#include <hip/hip_runtime.h>
#include <hip/hip_bf16.h>
#include <stdint.h>

#define WARM1 5
#define WARM2 1
#define AN0 6
#define APD 12
#define XN0 6
#define XPD 12
#define CPRIO 2
#define APRIO 1
#define TS1 1
#define TS2 3
#define RDBK 1
#define QPRIO 1
#define STCNT 1
#define GLA 2
#define NOWR 0
#define KVS 0
#define WARM2C 2
#define LNK2 2
#define LNK 6
#define INNER 15
#define STAMPS 0

typedef __attribute__((ext_vector_type(16))) float f32x16;
typedef __attribute__((ext_vector_type(8))) float f32x8;
typedef __attribute__((ext_vector_type(4))) float f32x4;
typedef __attribute__((ext_vector_type(2))) float f32x2;
typedef __attribute__((ext_vector_type(16))) __bf16 bf16x16;
typedef __attribute__((ext_vector_type(8))) __bf16 bf16x8;
typedef __attribute__((ext_vector_type(2))) __bf16 bf16x2;
typedef __attribute__((ext_vector_type(4))) unsigned u32x4;
typedef __attribute__((ext_vector_type(2))) unsigned u32x2;
typedef __attribute__((ext_vector_type(4))) short s16x4;
typedef __amdgpu_buffer_rsrc_t rsrc_t;
#define LAS __attribute__((address_space(3)))
typedef LAS char* lptr;
#define DI __device__ __forceinline__

constexpr unsigned WS_NONCE = 0x0;
constexpr unsigned WS_F0 = 0x1000;
constexpr unsigned WS_F5 = 0x2000;
constexpr unsigned WS_C1 = 0x10000;
constexpr unsigned WS_C2 = 0x20000;
constexpr unsigned WS_C3 = 0x28000;
constexpr unsigned WS_C4 = 0x2C000;
constexpr unsigned WS_C5 = 0x30000;
constexpr unsigned WS_C6 = 0x34000;
constexpr unsigned WS_ST = 0x40000;
constexpr unsigned WS_SP = 0x140000;
constexpr unsigned WS_KV = 0x200000;
constexpr unsigned WS_O = 0xA00000;
constexpr unsigned WS_H2 = 0xE00000;
constexpr unsigned WS_U = 0x1200000;
constexpr unsigned WS_X2 = 0x1600000;
constexpr unsigned WS_DBG = 0x1800000;
constexpr unsigned WS_END = 0x1A00000;
constexpr unsigned L_WIMG = 16384;
constexpr unsigned L_ONES = 131072;
constexpr unsigned L_CSP = 135168;
constexpr unsigned L_PROG = L_CSP + 1792;
constexpr unsigned L_FILL = L_PROG + 16;
constexpr unsigned L_SST = L_CSP + 2048;
constexpr unsigned L_REG = 139264;
constexpr unsigned L_SCR = 147456;
constexpr unsigned L_TOTAL = 163840;
constexpr int AUX_SC1 = 16;
constexpr unsigned OOB = 0x80000000u;
constexpr unsigned SPIN_CAP = 1u << 16;
constexpr float LN_EPS = 1e-5f;

#define SBAR() __builtin_amdgcn_sched_barrier(0)
#define PIN(x) asm volatile("" : "+v"(x))
#define MFMA(a, b, c) __builtin_amdgcn_mfma_f32_32x32x16_bf16(a, b, c, 0, 0, 0)
#define EX(x) __builtin_amdgcn_exp2f(x)
#define VMWAIT(n) asm volatile("s_waitcnt vmcnt(" #n ")" ::: "memory")
#define BAR() asm volatile("s_waitcnt lgkmcnt(0)\n\ts_barrier" ::: "memory")

template <int N> DI void vm_wait() { asm volatile("s_waitcnt vmcnt(%0)" ::"i"(N) : "memory"); }
template <int N> DI void vm_wait_bar() { asm volatile("s_waitcnt vmcnt(%0) lgkmcnt(0)\n\ts_barrier" ::"i"(N) : "memory"); }
DI rsrc_t mk_rsrc(const void* p, unsigned bytes) { asm volatile("" : "+s"(p)); return __builtin_amdgcn_make_buffer_rsrc((void*)p, 0, bytes, 0x00020000); }
template <int N, int LSTEP, int GSTEP> DI void dma(unsigned lds, unsigned voff, rsrc_t rs, unsigned soff) {
    unsigned t;
    asm volatile("s_mov_b32 m0, %1\n\ts_mov_b32 %0, %4\n\ts_nop 2\n\tbuffer_load_dwordx4 %2, %3, %0 offen lds\n\t"
                 ".rept %5\n\ts_add_u32 m0, m0, %6\n\ts_add_u32 %0, %0, %7\n\tbuffer_load_dwordx4 %2, %3, %0 offen lds\n\t.endr"
                 : "=&s"(t) : "s"(lds), "v"(voff), "s"(rs), "s"(soff), "i"(N - 1), "i"(LSTEP), "i"(GSTEP) : "memory", "scc");
}
template <int N, int LSTEP, int GSTEP> DI void dma_sc1(unsigned lds, unsigned voff, rsrc_t rs, unsigned soff) {
    unsigned t;
    asm volatile("s_mov_b32 m0, %1\n\ts_mov_b32 %0, %4\n\ts_nop 2\n\tbuffer_load_dwordx4 %2, %3, %0 offen sc1 lds\n\t"
                 ".rept %5\n\ts_add_u32 m0, m0, %6\n\ts_add_u32 %0, %0, %7\n\tbuffer_load_dwordx4 %2, %3, %0 offen sc1 lds\n\t.endr"
                 : "=&s"(t) : "s"(lds), "v"(voff), "s"(rs), "s"(soff), "i"(N - 1), "i"(LSTEP), "i"(GSTEP) : "memory", "scc");
}
template <int N, int GSTEP> DI void touch(unsigned pad, unsigned voff, rsrc_t rs, unsigned soff) {
    unsigned t;
    asm volatile("s_mov_b32 m0, %1\n\ts_mov_b32 %0, %4\n\ts_nop 2\n\tbuffer_load_dword %2, %3, %0 offen sc1 lds\n\t"
                 ".rept %5\n\ts_add_u32 %0, %0, %6\n\tbuffer_load_dword %2, %3, %0 offen sc1 lds\n\t.endr"
                 : "=&s"(t) : "s"(pad), "v"(voff), "s"(rs), "s"(soff), "i"(N - 1), "i"(GSTEP) : "memory", "scc");
}
DI bf16x8 ld_frag(rsrc_t rs, unsigned voff, unsigned soff) { return __builtin_bit_cast(bf16x8, __builtin_amdgcn_raw_buffer_load_b128(rs, voff, soff, AUX_SC1)); }
DI void st_frag(u32x4 v, rsrc_t rs, unsigned voff, unsigned soff) {
    __builtin_amdgcn_raw_buffer_store_b128(v, rs, voff, soff, AUX_SC1);
    asm volatile("s_nop 1" ::"v"(v) : "memory");
}
DI bf16x8 lds_frag(lptr p) { return *(LAS const bf16x8*)p; }
DI s16x4 lds_tr(lptr p) { return __builtin_amdgcn_ds_read_tr16_b64_v4i16((LAS s16x4*)p); }
DI bf16x8 tr_frag(lptr p0, lptr p1) {
    const s16x4 a = lds_tr(p0), b = lds_tr(p1);
    return __builtin_bit_cast(bf16x8, __builtin_shufflevector(a, b, 0, 1, 2, 3, 4, 5, 6, 7));
}
DI unsigned pk2(float a, float b) { const f32x2 v = {a, b}; return __builtin_bit_cast(unsigned, __builtin_convertvector(v, bf16x2)); }
DI float bf_lo(unsigned u) { return __uint_as_float(u << 16); }
DI float bf_hi(unsigned u) { return __uint_as_float(u & 0xffff0000u); }
DI float half_sum2(float a, float b) { const auto r = __builtin_amdgcn_permlane32_swap(__float_as_uint(a), __float_as_uint(b), false, false); return __uint_as_float(r[0]) + __uint_as_float(r[1]); }
DI f32x8 lo8(f32x16 v) { return __builtin_shufflevector(v, v, 0, 1, 2, 3, 4, 5, 6, 7); }
DI f32x8 hi8(f32x16 v) { return __builtin_shufflevector(v, v, 8, 9, 10, 11, 12, 13, 14, 15); }
DI f32x16 smf(bf16x8 a, f32x8 p, f32x16 c, int idx) { return __builtin_amdgcn_smfmac_f32_32x32x32_bf16(a, __builtin_bit_cast(bf16x16, p), c, idx, 0, 0); }
DI int opaque(int v) { asm volatile("" : "+v"(v)); return v; }
DI int sopaque(int v) { asm volatile("" : "+s"(v)); return v; }

#define PSLEEP 0
#define POLL_PAUSE() do { if (PSLEEP) __builtin_amdgcn_s_sleep(PSLEEP); asm volatile("" ::: "memory"); } while (0)
DI unsigned wait_cnt(rsrc_t ws, unsigned soff, unsigned target, int lane) {
    unsigned i;
#pragma unroll 1
    for (i = 0; i < SPIN_CAP; ++i) {
        const unsigned v = __builtin_amdgcn_raw_ptr_buffer_atomic_add_i32(0, ws, lane == 0 ? 0u : OOB, soff, AUX_SC1);
        if ((unsigned)__builtin_amdgcn_readfirstlane(v) >= target) break;
        POLL_PAUSE();
    }
    asm volatile("" ::: "memory");
    return i;
}
template <int NREP> DI void arrive(rsrc_t ws, unsigned soff, int lane) {
    __builtin_amdgcn_raw_ptr_buffer_atomic_add_i32(1, ws, lane < NREP ? lane * 128u : OOB, soff, AUX_SC1);
    asm volatile("" ::: "memory");
}
template <int NREP> DI void publish(rsrc_t ws, unsigned soff, int lane) { VMWAIT(0); arrive<NREP>(ws, soff, lane); }
DI unsigned wait64(rsrc_t ws, unsigned voff, unsigned soff, unsigned tlo, unsigned thi) {
    unsigned i;
#pragma unroll 1
    for (i = 0; i < SPIN_CAP; ++i) {
        const u32x2 v = __builtin_amdgcn_raw_buffer_load_b64(ws, voff, soff, AUX_SC1);
        if (__all(((v[0] ^ tlo) | (v[1] ^ thi)) == 0)) break;
        POLL_PAUSE();
    }
    asm volatile("" ::: "memory");
    return i;
}

#define ISTAMP(i) do { } while (0)
#define ISTPAR
#define NOTE(i, v) (void)(v)
#define STAMP(i) do { } while (0)

DI unsigned wvoff(int lane, unsigned ldw) { return (lane >> 3) * ldw + (((lane & 7) ^ (((lane >> 4) & 1) << 2)) << 4); }
DI void lds_post(lptr p, unsigned v) { *(volatile LAS unsigned*)p = v; }
DI unsigned lds_min2(lptr p) {
    const u32x2 v = *(volatile LAS u32x2*)p;
    const unsigned a = __builtin_amdgcn_readfirstlane(v[0]), b = __builtin_amdgcn_readfirstlane(v[1]);
    return a < b ? a : b;
}
template <int SLEEP> DI void lds_need(lptr p, unsigned& have, unsigned need) {
#pragma unroll 1
    for (unsigned i = 0; have < need && i < (SPIN_CAP << 4); ++i) { have = lds_min2(p); if (SLEEP && have < need) __builtin_amdgcn_s_sleep(SLEEP); }
    asm volatile("" ::: "memory");
}
template <int S0, int S1, int SLOT0, int LDW, int TS, int ib> DI void ring_prefetch(unsigned lb, u32x4 (&rg)[8][4], rsrc_t rw, unsigned voff, unsigned gb, lptr fill, unsigned base ISTPAR) {
#pragma unroll
    for (int s = S0; s < S1; ++s) {
        if (!(s & 1)) ISTAMP(ib + (s >> 1));
        const unsigned so = gb + s * (64 * LDW);
#pragma unroll
        for (int h = 0; h < 2; ++h) {
            if (s < 8) dma<2, 1024, 8 * LDW>(lb + ((s + SLOT0) & 7) * 16384 + h * 2048, voff, rw, so + h * 16 * LDW);
            else {
#pragma unroll
                for (int p = 2 * h; p < 2 * h + 2; ++p)
                    asm volatile("s_nop 4\n\tbuffer_load_dwordx4 %0, %1, %2, %3 offen" : "=v"(rg[s & 7][p]) : "v"(voff), "s"(rw), "s"(so + p * 8 * LDW) : "memory");
            }
            vm_wait<4 * TS>();
        }
        if (s >= TS && s < 8 + TS) lds_post(fill, base + s + 1 - TS);
    }
}
template <int NST, int SLOT0, int TS, int ib> DI void ring_refill(lptr lp, u32x4 (&rg)[8][4], lptr prog, lptr fill, unsigned base ISTPAR) {
    unsigned have = 0;
#pragma unroll
    for (int s = 8; s < NST; ++s) {
        lds_need<1>(prog, have, base + s - 7);
        if (s == NST - TS) asm volatile("s_waitcnt vmcnt(0)" : "+v"(rg[s & 7][0]), "+v"(rg[s & 7][1]), "+v"(rg[s & 7][2]), "+v"(rg[s & 7][3]) :: "memory");
        else asm volatile("" : "+v"(rg[s & 7][0]), "+v"(rg[s & 7][1]), "+v"(rg[s & 7][2]), "+v"(rg[s & 7][3]) :: "memory");
#pragma unroll
        for (int p = 0; p < 4; ++p) if (!NOWR) *(LAS u32x4*)(lp + ((s + SLOT0) & 7) * 16384 + p * 1024) = rg[s & 7][p];
        asm volatile("" ::: "memory");
        lds_post(fill, base + s + 1);
        ISTAMP(ib + s);
    }
}
template <int NST, int SLOT0, bool ACT_A, bool RING, int ib>
DI void gemm_compute(lptr lds, int lane, int kh, int ah, rsrc_t ws, unsigned act_soff, unsigned base, f32x16& acc0, f32x16& acc1 ISTPAR) {
    constexpr int NK = NST * 4, LA = GLA;
    const unsigned av = lane * 16;
    ISTAMP(ib);
    bf16x8 f[NK], w0[NK], w1[NK];
#define ALOAD(i) f[i] = ld_frag(ws, av + ((i) & 3) * 1024, act_soff + ((i) >> 2) * 4096)
#pragma unroll
    for (int i = 0; i < AN0; ++i) ALOAD(i);
    SBAR();
    const int hh = lane >> 5, gq = (lane >> 4) & 1, q = (lane >> 2) & 3, p = lane & 3;
    const unsigned a0 = kh * 8192 + (4 * hh + q) * 128 + (((4 * kh + 2 * gq + (p >> 1)) ^ (((q >> 1) & 1) << 2)) << 4) + (p & 1) * 8;
    const lptr b0 = lds + a0, b1 = lds + (a0 ^ 64), b0h = b0 + 65536, b1h = b1 + 65536;
    const lptr fill = lds + L_FILL + kh * 8, prog = lds + L_PROG + kh * 8 + ah * 4;
    unsigned have = 0;
#define WREAD(dst, pl, ph, k) do { const int sl_ = (((k) >> 2) + SLOT0) & 7, o_ = (sl_ & 3) * 16384 + ((k) & 3) * 2048; \
        const lptr p_ = (sl_ & 4) ? ph : pl; dst[k] = tr_frag(p_ + o_, p_ + o_ + 1024); } while (0)
    acc1 = f32x16{};
    ISTAMP(ib + 1);
    if (RING) lds_need<0>(fill, have, base + 1);
    ISTAMP(ib + 2);
#pragma unroll
    for (int k = 0; k < LA; ++k) { WREAD(w1, b1, b1h, k); WREAD(w0, b0, b0h, k); }
    int nl = AN0;
#pragma unroll
    for (int k = 0; k < NK; ++k) {
        if (RING && ((k + LA) & 3) == 0 && k + LA < NK) lds_need<0>(fill, have, base + ((k + LA) >> 2) + 1);
        SBAR();
        acc1 = ACT_A ? MFMA(f[k], w1[k], acc1) : MFMA(w1[k], f[k], acc1);
        SBAR();
        if (k + LA < NK) WREAD(w1, b1, b1h, k + LA);
        if (nl < NK && nl < k + APD) { ALOAD(nl); ++nl; }
        SBAR();
        acc0 = ACT_A ? MFMA(f[k], w0[k], acc0) : MFMA(w0[k], f[k], acc0);
        SBAR();
        if (RING && (k & 3) == 3 && (k >> 2) + 8 < NST) lds_post(prog, base + (k >> 2) + 1);
        if (k + LA < NK) WREAD(w0, b0, b0h, k + LA);
        if (nl < NK && nl < k + 1 + APD) { ALOAD(nl); ++nl; }
        if (k == 0) ISTAMP(ib + 3);
        if ((k & 7) == 7) ISTAMP(ib + 4 + (k >> 3));
    }
    SBAR();
#undef WREAD
#undef ALOAD
}
DI void exchange(lptr lds, int lane, int w, int partner, f32x16& acc0, const f32x16& acc1) {
    LAS f32x4* dst = (LAS f32x4*)(lds + L_SCR + w * 4096 + lane * 16);
#pragma unroll
    for (int i = 0; i < 4; ++i) dst[i * 64] = f32x4{acc1[4 * i], acc1[4 * i + 1], acc1[4 * i + 2], acc1[4 * i + 3]};
    BAR();
    LAS const f32x4* src = (LAS const f32x4*)(lds + L_SCR + partner * 4096 + lane * 16);
#pragma unroll
    for (int i = 0; i < 4; ++i) { const f32x4 v = src[i * 64]; acc0[4 * i] += v[0]; acc0[4 * i + 1] += v[1]; acc0[4 * i + 2] += v[2]; acc0[4 * i + 3] += v[3]; }
}

#define MFMA16(a, b, c) __builtin_amdgcn_mfma_f32_16x16x32_bf16(a, b, c, 0, 0, 0)
struct QkvAcc { f32x4 q[2], k[2], v[2], cq, ck, cv; };
struct WFrag { s16x4 h[3][2]; };
DI bf16x8 wpart(const WFrag& f, int i) { return __builtin_bit_cast(bf16x8, __builtin_shufflevector(f.h[i][0], f.h[i][1], 0, 1, 2, 3, 4, 5, 6, 7)); }
DI void qkv_wread(WFrag& n, lptr p0, lptr p1, int o) {
#pragma unroll
    for (int i = 0; i < 3; ++i) { n.h[i][0] = lds_tr(p0 + o + i * 128); n.h[i][1] = lds_tr(p1 + o + i * 128); }
}
template <bool CS> DI void qkv_step(QkvAcc& a, bf16x8 x0, bf16x8 x1, const WFrag& f, WFrag& n, bool more, lptr p0, lptr p1, int o, bf16x8 ones) {
    const bf16x8 wq = wpart(f, 0), wk = wpart(f, 1), wv = wpart(f, 2);
    SBAR(); a.q[0] = MFMA16(wq, x0, a.q[0]); SBAR(); if (more) n.h[0][0] = lds_tr(p0 + o);
    SBAR(); a.q[1] = MFMA16(wq, x1, a.q[1]); SBAR(); if (more) n.h[0][1] = lds_tr(p1 + o);
    SBAR(); a.k[0] = MFMA16(wk, x0, a.k[0]); SBAR(); if (more) n.h[1][0] = lds_tr(p0 + o + 128);
    SBAR(); a.k[1] = MFMA16(wk, x1, a.k[1]); SBAR(); if (more) n.h[1][1] = lds_tr(p1 + o + 128);
    SBAR(); a.v[0] = MFMA16(x0, wv, a.v[0]); SBAR(); if (more) n.h[2][0] = lds_tr(p0 + o + 256);
    SBAR(); a.v[1] = MFMA16(x1, wv, a.v[1]); SBAR(); if (more) n.h[2][1] = lds_tr(p1 + o + 256);
    if (CS) { SBAR(); a.cq = MFMA16(wq, ones, a.cq); a.ck = MFMA16(wk, ones, a.ck); a.cv = MFMA16(ones, wv, a.cv); }
    SBAR();
}
DI u32x4 join16(f32x4 a, f32x4 b) {
    const auto s0 = __builtin_amdgcn_permlane16_swap(pk2(a[0], a[1]), pk2(b[0], b[1]), false, false);
    const auto s1 = __builtin_amdgcn_permlane16_swap(pk2(a[2], a[3]), pk2(b[2], b[3]), false, false);
    return u32x4{s0[0], s1[0], s0[1], s1[1]};
}
DI u32x2 row_partial(const unsigned (&x)[8]) {
    float sum = 0.f, sq = 0.f;
#pragma unroll
    for (int i = 0; i < 8; ++i) { const float a = bf_lo(x[i]), b = bf_hi(x[i]); sum += a + b; sq = __builtin_fmaf(a, a, __builtin_fmaf(b, b, sq)); }
    const unsigned t = __float_as_uint(half_sum2(sum, sq));
    const auto sw = __builtin_amdgcn_permlane32_swap(t, t, false, false);
    return u32x2{sw[0], sw[1]};
}
DI f32x4 record_total(const u32x4 (&p)[4]) {
    f32x4 t;
#pragma unroll
    for (int j = 0; j < 4; ++j) {
        const unsigned v = __float_as_uint((__uint_as_float(p[0][j]) + __uint_as_float(p[1][j])) + (__uint_as_float(p[2][j]) + __uint_as_float(p[3][j])));
        const auto a = __builtin_amdgcn_permlane32_swap(v, v, false, false);
        const unsigned u = __float_as_uint(__uint_as_float(a[0]) + __uint_as_float(a[1]));
        const auto b = __builtin_amdgcn_permlane16_swap(u, u, false, false);
        t[j] = __uint_as_float(b[0]) + __uint_as_float(b[1]);
    }
    return t;
}
#define XE(r) "v_exp_f32 v" #r ", v" #r "\n\t"
#define SMF(d, a, b) "v_smfmac_f32_32x32x32_bf16 v[" d "], v[" a "], v[" b "], v173\n\t"
#define ATT_HEAD XE(66) XE(67) XE(68) XE(69) "v_mfma_f32_32x32x16_bf16 v[80:95], v[160:163], v[156:159], v[0:15]\n\t"
#define ATT_FULL(C0, C1, N0, N1) ATT_HEAD \
    "ds_read_b128 v[160:163], %[kp] offset:%[ko]\n\t" XE(70) XE(71) "ds_read_b128 v[" N0 "], %[vp] offset:%[vo]\n\t" XE(72) XE(73) SMF("32:47", C0, "64:71") \
    "ds_read_b128 v[" N1 "], %[vp] offset:%[vo2]\n\t" XE(74) XE(75) XE(76) XE(77) XE(78) XE(79) XE(80) XE(81) SMF("32:47", C1, "72:79") \
    XE(82) XE(83) XE(84) "s_waitcnt lgkmcnt(0)\n\tv_mfma_f32_32x32x16_bf16 v[64:79], v[160:163], v[152:155], v[48:63]\n\t" \
    XE(85) XE(86) XE(87) XE(88) XE(89) SMF("16:31", C0, "80:87") \
    XE(90) XE(91) XE(92) XE(93) XE(94) XE(95) XE(64) XE(65) SMF("16:31", C1, "88:95")
#define ATT_LAST(C0, C1) ATT_HEAD \
    XE(70) XE(71) XE(72) XE(73) SMF("32:47", C0, "64:71") XE(74) XE(75) XE(76) XE(77) XE(78) XE(79) "s_nop 0\n\t" XE(80) XE(81) SMF("32:47", C1, "72:79") \
    XE(82) XE(83) XE(84) XE(85) XE(86) XE(87) XE(88) XE(89) SMF("16:31", C0, "80:87") \
    XE(90) XE(91) XE(92) XE(93) XE(94) XE(95) "s_nop 1\n\t" SMF("16:31", C1, "88:95") "s_nop 11"
#define ATT_OPS(KO, VO) \
    : "+{v[64:79]}"(sA), "+{v[80:95]}"(sB), "+{v[32:47]}"(o), "+{v[16:31]}"(o2), "+{v[160:163]}"(kf), \
      "+{v[164:167]}"(v0), "+{v[168:171]}"(v1), "+{v[176:179]}"(v2), "+{v[180:183]}"(v3) \
    : "{v[48:63]}"(cneg), "{v[0:15]}"(cneg2), "{v[152:155]}"(qf), "{v[156:159]}"(qf2), "{v173}"(idx), \
      [kp] "v"(kp), [vp] "v"(vp), [ko] "i"(KO), [vo] "i"(VO), [vo2] "i"((VO) + 512) : "memory"
#define ATT_EVEN(KO, VO) asm volatile(ATT_FULL("164:167", "168:171", "176:179", "180:183") ATT_OPS(KO, VO))
#define ATT_ODD(KO, VO) asm volatile(ATT_FULL("176:179", "180:183", "164:167", "168:171") ATT_OPS(KO, VO))
#define ATT_ODD_LAST() asm volatile(ATT_LAST("176:179", "180:183") ATT_OPS(0, 0))
DI float neg_row_max(f32x16 s) {
    float m0 = __builtin_fmaxf(s[0], s[1]), m1 = __builtin_fmaxf(s[2], s[3]);
#pragma unroll
    for (int i = 4; i < 16; i += 4) { m0 = __builtin_fmaxf(__builtin_fmaxf(m0, s[i]), s[i + 1]); m1 = __builtin_fmaxf(__builtin_fmaxf(m1, s[i + 2]), s[i + 3]); }
    const float m = __builtin_fmaxf(m0, m1);
    const auto sw = __builtin_amdgcn_permlane32_swap(__float_as_uint(m), __float_as_uint(m), false, false);
    return -__builtin_fmaxf(__uint_as_float(sw[0]), __uint_as_float(sw[1]));
}
DI f32x16 splat16(float m) {
    f32x2 m2 = {m, m};
    PIN(m2);
    f32x16 c;
#pragma unroll
    for (int i = 0; i < 8; ++i) { f32x2 t; asm volatile("v_mov_b64 %0, %1" : "=v"(t) : "v"(m2)); c[2 * i] = t[0]; c[2 * i + 1] = t[1]; }
    PIN(c);
    return c;
}

__global__ __launch_bounds__(512, 2) void fused_fwd(const void* __restrict__ xin, const void* __restrict__ wqkv, const void* __restrict__ wo,
                                                    const void* __restrict__ w1, const unsigned short* __restrict__ b1, const void* __restrict__ w2,
                                                    const unsigned short* __restrict__ b2, unsigned short* __restrict__ out, char* wsp) {
    __shared__ __attribute__((aligned(16))) char lds_raw[L_TOTAL];
    const lptr lds = (lptr)lds_raw;
    const unsigned lds0 = (unsigned)(uintptr_t)lds;
    const int w_ = __builtin_amdgcn_readfirstlane(threadIdx.x >> 6);
#define PHASE_IDS \
    const int lane = opaque(threadIdx.x & 63), l31 = lane & 31, hh = lane >> 5; \
    const int w = sopaque(w_), bid = sopaque(blockIdx.x), g = bid & 7, r = bid >> 3; \
    const int st64 = g * 4 + (r >> 3), dt64 = r & 7; \
    const int dt1 = (g & 1) * 4 + (r & 3), ft1 = (g >> 1) * 7 + (r >> 2); \
    const int own_s32 = st64 * 2 + (w & 1), own_d32 = dt64 * 2 + ((w >> 1) & 1); \
    (void)l31; (void)hh; (void)dt1; (void)ft1; (void)own_s32; (void)own_d32;
    const bool w1_on = (blockIdx.x >> 3) < 28;
    const rsrc_t ws = mk_rsrc(wsp, WS_END);

#define ISTARG(b)
    unsigned xres[8] = {};
    unsigned tag_lo = 0, tag_hi = 0;
    u32x2 nonce = {};
    u32x4 wc[12];
#define UNDEF_WC() do { _Pragma("unroll") for (int i_ = 0; i_ < 12; ++i_) asm volatile("" : "=v"(wc[i_])); } while (0)
    UNDEF_WC();

#pragma unroll 1
    for (int L = 0; L < 2; ++L) {
        bf16x8 qf;
        f32x16 o, o2, cneg;
        {
            PHASE_IDS
            const int gq = (lane >> 4) & 1, q = (lane >> 2) & 3, p = lane & 3, l15 = lane & 15, kg = lane >> 4;
            const unsigned kv_soff = WS_KV + (L * 32 + r) * 131072;
            STAMP(0);
            QkvAcc a = {};
            u32x4 sp[4];
            float rt[2], mt[2];
            f32x4 rs[2], ms[2];
            asm volatile("" : "=v"(rt[0]), "=v"(rt[1]), "=v"(mt[0]), "=v"(mt[1]), "=v"(rs[0]), "=v"(rs[1]), "=v"(ms[0]), "=v"(ms[1]));
#define LN_PREP() do { \
                const f32x4 tot = record_total(sp); \
                const float m0 = tot[0] * (1.f / 512.f), m1 = tot[2] * (1.f / 512.f); \
                const float r0 = __builtin_amdgcn_rsqf(__builtin_fmaxf(tot[1] * (1.f / 512.f) - m0 * m0, 0.f) + LN_EPS); \
                const float r1 = __builtin_amdgcn_rsqf(__builtin_fmaxf(tot[3] * (1.f / 512.f) - m1 * m1, 0.f) + LN_EPS); \
                const lptr sst = lds + L_SST + w * 256; \
                *(LAS f32x2*)(sst + hh * 128 + l15 * 8) = hh ? f32x2{m0 * r0, m1 * r1} : f32x2{r0, r1}; \
                _Pragma("unroll") for (int t = 0; t < 2; ++t) { \
                    rt[t] = *(LAS const float*)(sst + t * 64 + l15 * 4); mt[t] = *(LAS const float*)(sst + 128 + t * 64 + l15 * 4); \
                    rs[t] = *(LAS const f32x4*)(sst + t * 64 + kg * 16); ms[t] = *(LAS const f32x4*)(sst + 128 + t * 64 + kg * 16); } \
                SBAR(); } while (0)
            const unsigned sp_soff = WS_SP + (L * 64 + g * 8 + w) * 4096;
            if (L == 0) {
                const rsrc_t rx = mk_rsrc(xin, 2048 * 1024), rw = mk_rsrc(wqkv, 512 * 3072);
                unsigned wv[3];
#define WQUART(j) do { if (w >= 4) { _Pragma("unroll") for (int ty = 0; ty < 3; ++ty) \
                       dma<1, 0, 0>(lds0 + L_WIMG + (4 * (j) + w - 4) * 3072 + ty * 1024, wv[ty], rw, (4 * (j) + w - 4) * 98304 + r * 32); } } while (0)
                const unsigned tokb = (g * 256 + w * 32) * 1024, stg = w < 5 ? 65536 + w * 12288 : (w == 5 ? 0 : L_REG + (w - 6) * 12288);
                const unsigned vA = (lane >> 3) * 1024 + (((lane & 7) ^ (lane >> 4)) << 4), vB = vA ^ 64;
#define XCHUNK(c) do { dma<2, 2048, 16384>(lds0 + stg + ((c) % 3) * 4096, vA, rx, tokb + (c) * 128); \
                       dma<2, 2048, 16384>(lds0 + stg + ((c) % 3) * 4096 + 1024, vB, rx, tokb + 8192 + (c) * 128); } while (0)
#define ROLE_WAIT(a, b) do { if (w >= 4) vm_wait<a>(); else vm_wait<b>(); } while (0)
                u32x2 xr[4];
                XCHUNK(0);
#pragma unroll
                for (int ty = 0; ty < 3; ++ty) {
                    const unsigned u = ty * 64 + lane, rg = __umul24(u, 171) >> 12, rem = u - __umul24(rg, 24);
                    wv[ty] = __umul24(4 * (rg ^ ((rg >> 1) & 1)) + ((rem >> 1) & 3), 3072) + (rem >> 3) * 1024 + (rem & 1) * 16;
                }
                WQUART(0);
                asm volatile("s_nop 4\n\tbuffer_load_dwordx2 %0, off, %1, 0 sc1" : "=v"(nonce) : "s"(ws) : "memory");
                if (w < 4) {
                    asm volatile("s_nop 4\n\tbuffer_load_dwordx2 %0, %4, %5, %6 offen\n\tbuffer_load_dwordx2 %1, %4, %5, %6 offen offset:16\n\t"
                                 "buffer_load_dwordx2 %2, %4, %5, %6 offen offset:32\n\tbuffer_load_dwordx2 %3, %4, %5, %6 offen offset:48"
                                 : "=&v"(xr[0]), "=&v"(xr[1]), "=&v"(xr[2]), "=&v"(xr[3]) : "v"(l31 * 1024 + hh * 8), "s"(rx), "s"(own_s32 * 32768 + own_d32 * 64) : "memory");
                } else {
#pragma unroll
                    for (int i = 0; i < 4; ++i) asm volatile("" : "=v"(xr[i]));
                }
                XCHUNK(1); WQUART(1); XCHUNK(2);
                const lptr p0 = lds + L_WIMG + (4 * hh + 3 * gq) * 384 + q * 32 + p * 8, p1 = lds + L_WIMG + (4 * hh + 1 + gq) * 384 + q * 32 + p * 8;
                lptr px[4];
#pragma unroll
                for (int t = 0; t < 4; ++t) px[t] = lds + stg + ((t & 1) * 16 + l15) * 128 + (((4 * (t >> 1) + kg) ^ (l15 >> 1)) << 4);
                WFrag wf[2];
                const bf16x8 ones = __builtin_bit_cast(bf16x8, u32x4{0x3f803f80u, 0x3f803f80u, 0x3f803f80u, 0x3f803f80u});
                u32x2 fl;
                const unsigned f0r = opaque(WS_F0 + r * 128 + (lane & 7) * 8), f5r = opaque(WS_F5 + (g * 8 + w) * 128 + (lane & 15) * 8), flr = hh ? f0r : f5r;
#pragma unroll
                for (int c = 0; c < 8; ++c) {
                    if (c == 0) ISTAMP(54);
                    if (c <= 5) ROLE_WAIT(11, 8); else if (c == 6) vm_wait<0>();
                    ISTAMP(55 + c);
                    if (c == 0) { BAR(); STAMP(1); qkv_wread(wf[0], p0, p1, 0); }
                    bf16x8 xf[4];
#pragma unroll
                    for (int t = 0; t < 4; ++t) xf[t] = lds_frag(px[t] + (c % 3) * 4096);
                    if (c == 5) asm volatile("s_nop 4\n\tbuffer_load_dwordx2 %0, %1, %2, 0 offen sc1" : "=v"(fl) : "v"(flr), "s"(ws) : "memory");
                    if (c + 3 < 8) { asm volatile("s_waitcnt lgkmcnt(0)" : "+v"(xf[0]), "+v"(xf[1]), "+v"(xf[2]), "+v"(xf[3]) :: "memory"); XCHUNK(c + 3); }
                    if (c == 1) WQUART(2);
                    if (c == 3) WQUART(3);
                    if (c == 0) {
                        asm volatile("" : "+v"(nonce), "+v"(xr[0]), "+v"(xr[1]), "+v"(xr[2]), "+v"(xr[3]));
                        unsigned long long z = ((unsigned long long)__builtin_amdgcn_readfirstlane(nonce[1]) << 32) | (unsigned)__builtin_amdgcn_readfirstlane(nonce[0]);
                        z = z * 0x9E3779B97F4A7C15ull + 0x632BE59BD9B4E019ull;
                        z ^= z >> 29; z *= 0xBF58476D1CE4E5B9ull; z ^= z >> 32;
                        tag_lo = (unsigned)z; tag_hi = (unsigned)(z >> 32);
#pragma unroll
                        for (int i = 0; i < 4; ++i) { xres[2 * i] = xr[i][0]; xres[2 * i + 1] = xr[i][1]; }
                        if (w < 4) {
                            __builtin_amdgcn_raw_buffer_store_b64(row_partial(xres), ws, hh ? OOB : l31 * 8u, WS_SP + (own_s32 * 16 + own_d32) * 256, AUX_SC1);
                            __builtin_amdgcn_raw_buffer_store_b64(u32x2{}, ws, l31 * 8 + hh * 0x40000, WS_ST + (own_s32 * 16 + own_d32) * 256, AUX_SC1);
                            const unsigned c1a = opaque(WS_C1 + (r * 8 + g) * 128 + lane * 32768), c3a = opaque(WS_C3 + bid * 256 + (lane - 3) * 128);
                            const unsigned c2a = sopaque(WS_C2 + (((r >> 4) * 64 + g * 8) * 2 + (r & 15)) * 128);
                            const unsigned ca = lane < 2 ? c1a : (lane == 2 ? c2a : c3a);
                            __builtin_amdgcn_raw_buffer_store_b32(0u, ws, (w == 0 && lane < 5) ? ca : OOB, 0, AUX_SC1);
                        }
                    }
                    if (c == 6) {
                        asm volatile("" : "+v"(fl));
                        if (!__all(((fl[0] ^ tag_lo) | (fl[1] ^ tag_hi)) == 0)) wait64(ws, flr, 0, tag_lo, tag_hi);
                        asm volatile("" ::: "memory");
#pragma unroll
                        for (int i = 0; i < 4; ++i) sp[i] = __builtin_amdgcn_raw_buffer_load_b128(ws, lane * 16 + i * 1024, sp_soff, AUX_SC1);
                        SBAR();
                    }
                    if (c == 6) {
                        if (w >= 4) {
#pragma unroll
                            for (int i = 0; i < 12; ++i) wc[i] = *(LAS const u32x4*)(lds + L_WIMG + (w - 4) * 12288 + i * 1024 + lane * 16);
                        }
                    }
#pragma unroll
                    for (int t = 0; t < 2; ++t) {
                        const int k = 2 * c + t;
                        if (k == 3) { ROLE_WAIT(15, 63); BAR(); }
                        if (k == 7) { ROLE_WAIT(11, 63); BAR(); }
                        if (k == 11) { ROLE_WAIT(4, 63); BAR(); }
                        qkv_step<true>(a, xf[2 * t], xf[2 * t + 1], wf[k & 1], wf[(k + 1) & 1], k < 15, p0, p1, (k + 1) * 3072, ones);
                        if ((k == 13 && w >= 4) || (k == 14 && w < 4)) LN_PREP();
                    }
                    if (c == 1 && w < 4) {
                        VMWAIT(0);
                        const unsigned f5a = sopaque(WS_F5 + own_s32 * 128 + own_d32 * 8), f0a = sopaque(w == 0 ? WS_F0 + r * 128 + g * 8 : OOB);
                        __builtin_amdgcn_raw_buffer_store_b64(u32x2{tag_lo, tag_hi}, ws, lane == 0 ? f5a : (lane == 1 ? f0a : OOB), 0, AUX_SC1);
                    }
                }
                ISTAMP(63);
                if (w == 0) {
                    const unsigned ca = opaque(L_CSP + kg * 16), cd = opaque(stg + lane * 16), cw = l15 ? cd : ca;
                    *(LAS f32x4*)(lds + cw) = a.cq;
                    *(LAS f32x4*)(lds + cw + 64) = a.ck;
                    *(LAS float*)(lds + L_CSP + 128 + l15 * 4) = a.cv[0];
                }
                BAR();
#undef WQUART
#undef ROLE_WAIT
#undef XCHUNK
            } else {
                if (w >= 4) {
#pragma unroll
                    for (int i = 0; i < 12; ++i) *(LAS u32x4*)(lds + L_WIMG + (w - 4) * 12288 + i * 1024 + lane * 16) = wc[i];
                }
                BAR();
                wait_cnt(ws, WS_C5 + ((g * 8 + w) * 2 + (r & 1)) * 128, 16, lane);
                STAMP(1);
                ISTAMP(32);
                bf16x8 xf[32];
                const unsigned x_soff = WS_X2 + (g * 8 + w) * 32768;
#define XLOAD(k) xf[k] = ld_frag(ws, lane * 16 + ((k) & 3) * 1024, x_soff + ((k) >> 2) * 4096)
#pragma unroll
                for (int i = 0; i < 4; ++i) sp[i] = __builtin_amdgcn_raw_buffer_load_b128(ws, lane * 16 + i * 1024, sp_soff, AUX_SC1);
#pragma unroll
                for (int k = 0; k < XN0; ++k) XLOAD(k);
                SBAR();
                ISTAMP(33);
                ISTAMP(34);
                const lptr p0 = lds + L_WIMG + (4 * hh + gq) * 384 + q * 32 + p * 8, p1 = lds + L_WIMG + (4 * hh + 3 - gq) * 384 + q * 32 + p * 8;
                WFrag wf[2];
                qkv_wread(wf[0], p0, p1, 0);
                int nl = XN0;
                if (QPRIO) __builtin_amdgcn_s_setprio(3);
#pragma unroll
                for (int k = 0; k < 16; ++k) {
                    qkv_step<false>(a, xf[2 * k], xf[2 * k + 1], wf[k & 1], wf[(k + 1) & 1], k < 15, p0, p1, (k + 1) * 3072, xf[2 * k]);
#pragma unroll
                    for (int i = 0; i < 3; ++i) if (nl < 32 && nl < 2 * k + 2 + XPD) { XLOAD(nl); ++nl; }
                    if (QPRIO && k == 5) __builtin_amdgcn_s_setprio(2);
                    if (QPRIO && k == 10) __builtin_amdgcn_s_setprio(1);
                    if (QPRIO && k == 13) __builtin_amdgcn_s_setprio(0);
                    if (k == LNK && w < 4) LN_PREP();
                    if (k == LNK2 && w >= 4) LN_PREP();
                    if (k == 12) {
                        a.cq = *(LAS const f32x4*)(lds + L_CSP + kg * 16); a.ck = *(LAS const f32x4*)(lds + L_CSP + 64 + kg * 16);
                        a.cv[0] = *(LAS const float*)(lds + L_CSP + 128 + l15 * 4);
                    }
                    if (k == 0) ISTAMP(35);
                    if (k & 1) ISTAMP(36 + (k >> 1));
                }
#undef XLOAD
                UNDEF_WC();
            }
            STAMP(2);
            if (APRIO) __builtin_amdgcn_s_setprio(3);
            const u32x4 kpk = join16(a.k[0] * rt[0] - mt[0] * a.ck, a.k[1] * rt[1] - mt[1] * a.ck);
            const unsigned koff = lane * 16;
            st_frag(kpk, ws, koff, kv_soff + (g * 8 + w) * 2048);
            const u32x4 vpk = join16(a.v[0] * rs[0] - ms[0] * a.cv[0], a.v[1] * rs[1] - ms[1] * a.cv[0]);
            const unsigned voff = gq * 512 + hh * 256 + l15 * 16;
            st_frag(vpk, ws, voff, kv_soff + (g * 8 + w) * 2048 + 1024);
            constexpr float QS = 0.25f * 1.4426950408889634f;
            const u32x4 qpk = join16((a.q[0] * rt[0] - mt[0] * a.cq) * QS, (a.q[1] * rt[1] - mt[1] * a.cq) * QS);
            qf = __builtin_bit_cast(bf16x8, qpk);
            const f32x16 sd = MFMA(__builtin_bit_cast(bf16x8, kpk), qf, f32x16{});
            if (L == 0) {
                if (w == 0) *(LAS unsigned*)(lds + L_PROG + (lane & 7) * 4) = 0u;
                const unsigned one = (lane & 15) ? 0u : 0x3f803f80u;
                *(LAS u32x4*)(lds + L_ONES + w * 512 + hh * 256 + (lane & 15) * 16) = u32x4{one, one, one, one};
            }
            *(LAS u32x4*)(lds + w * 1024 + koff) = kpk;
            *(LAS u32x4*)(lds + 8192 + w * 1024 + voff) = vpk;
            *(LAS u32x4*)(lds + L_REG + w * 3072 + lane * 16) = qpk;
            o = f32x16{}; o2 = f32x16{};
            asm volatile("" : "+{v[32:47]}"(o), "+{v[16:31]}"(o2));
            {
                const float nm = neg_row_max(sd);
                *(LAS float*)(lds + L_REG + w * 3072 + 1024 + lane * 4) = nm;
                cneg = splat16(nm);
            }
            BAR();
            STAMP(3);
        }
        {
            PHASE_IDS
            const int gq = (lane >> 4) & 1;
            const unsigned kv_soff = WS_KV + (L * 32 + r) * 131072;
            const int idx = opaque(0xDDDD);
            lptr kp = lds + (w & 1) * 4096 + lane * 16;
            lptr vp = lds + (gq ? L_ONES : 8192 + (w & 1) * 4096) + hh * 256 + (lane & 15) * 16;
            const int vinc = gq ? 0 : 16384;
            bf16x8 kf = lds_frag(kp);
            const float nm2 = *(LAS const float*)(lds + L_REG + (w ^ 1) * 3072 + 1024 + lane * 4);
            const bf16x8 qf2 = lds_frag(lds + L_REG + (w ^ 1) * 3072 + lane * 16);
            bf16x8 v0 = lds_frag(vp), v1 = lds_frag(vp + 512), v2, v3;
            f32x16 sA, sB;
            asm volatile("" : "=v"(v2), "=v"(v3), "=v"(sB));
#define KVCHUNK(j) dma_sc1<2, 8192, 1024>(lds0 + (j) * 16384 + w * 1024, lane * 16, ws, kv_soff + (((g + (j)) & 7) * 8 + w) * 2048)
            sA = MFMA(kf, qf, cneg);
            const f32x16 cneg2 = splat16(nm2);
            publish<1>(ws, WS_C1 + ((L * 32 + r) * 8 + w) * 128, lane);
            sA[0] = EX(sA[0]); sA[1] = EX(sA[1]);
#define PRIO(n) do { if (APRIO) __builtin_amdgcn_s_setprio(n); } while (0)
            {
                const unsigned c1o = WS_C1 + ((L * 32 + r) * 8 + w) * 128, pv0 = lane == 0 ? 0u : OOB;
                int pc = 0;
#define POLL_ISSUE() asm volatile("s_nop 4\n\tbuffer_atomic_add %0, %1, %2, %3 offen sc0 sc1" : "+v"(pc) : "v"(pv0), "s"(ws), "s"(c1o) : "memory")
#define POLL_READ() asm volatile("s_waitcnt vmcnt(0)" : "+v"(pc) :: "memory")
                PRIO(2);
                ATT_EVEN(1024, 1024);
                POLL_ISSUE();
                ATT_ODD(2048, 2048);
                STAMP(4);
                POLL_READ();
                if (__builtin_amdgcn_readfirstlane(pc) < 8) wait_cnt(ws, c1o, 8, lane);
                STAMP(5);
                KVCHUNK(1);
                PRIO(1);
                ATT_EVEN(3072, 3072);
                if (KVS == 1) KVCHUNK(2);
                PRIO(0);
                ATT_ODD_LAST();
                vm_wait_bar<KVS == 1 ? 2 : 0>();
                STAMP(6);
                kp += 16384; vp += vinc;
                kf = lds_frag(kp); v0 = lds_frag(vp); v1 = lds_frag(vp + 512);
                if (KVS == 1) KVCHUNK(3); else KVCHUNK(2);
                sA = MFMA(kf, qf, cneg);
                sA[0] = EX(sA[0]); sA[1] = EX(sA[1]);
            }
#pragma unroll
            for (int c = 1; c < 8; ++c) {
                if (c == 1 || c == 3 || c == 4 || c == 7) PRIO(2);
                ATT_EVEN(1024, 1024);
                if (c == 1 && KVS != 1) KVCHUNK(3);
                if (c == 2) KVCHUNK(5);
                if (c == 1 || c == 3 || c == 4) PRIO(1);
                ATT_ODD(2048, 2048);
                if (c == 2) KVCHUNK(6);
                if (c == 1 || c == 3 || c == 4) PRIO(0); else if (c == 7) PRIO(1);
                ATT_EVEN(3072, 3072);
                if (c == 1) { vm_wait_bar<0>(); KVCHUNK(4); }
                if (c == 2) KVCHUNK(7);
                if (c == 3) vm_wait_bar<0>();
                if (c == 4) {
                    BAR();
                    dma<8, 1024, 8192>(lds0 + w * 8192, wvoff(lane, 1024), mk_rsrc(wo, 512 * 1024), ((w & 1) * 256 + (w >> 1) * 64) * 1024 + dt64 * 128);
                }
                if (c == WARM1) touch<1, 0>(lds0 + L_SST + w * 256, lane * 3584, mk_rsrc(w1, 2048 * 3584), ((r & 3) * 512 + w * 64) * 3584 + (w1_on ? ft1 : 0) * 128);
                if (c == WARM2C && L == 0) touch<1, 0>(lds0 + L_SST + w * 256, (lane < 28 ? lane : 0) * 4096, mk_rsrc(w2, 1792 * 4096), (dt64 * 224 + w * 28) * 4096 + st64 * 128);
                if (c < 7) { if (c != 2 && c < 5) PRIO(3); vp += vinc; ATT_ODD(16384, 0); kp += 16384; }
                else { PRIO(0); ATT_ODD_LAST(); }
            }
            STAMP(7);
            VMWAIT(0);
            STAMP(20);
            {
                const lptr mine = lds + L_REG + w * 3072 + lane * 16, other = lds + L_REG + (w ^ 1) * 3072 + lane * 16;
                *(LAS f32x4*)mine = f32x4{o2[0], o2[1], o2[2], o2[3]};
                *(LAS f32x4*)(mine + 1024) = f32x4{o2[4], o2[5], o2[6], o2[7]};
                *(LAS float*)(mine + 2048) = o2[8];
                BAR();
                STAMP(21);
                const f32x4 a0 = *(LAS const f32x4*)other, a1 = *(LAS const f32x4*)(other + 1024);
                o[8] += *(LAS const float*)(other + 2048);
#pragma unroll
                for (int i = 0; i < 4; ++i) { o[i] += a0[i]; o[4 + i] += a1[i]; }
            }
#define O_STORE() do { const float il = __builtin_amdgcn_rcpf(__uint_as_float(__builtin_amdgcn_permlane32_swap(__float_as_uint(o[8]), __float_as_uint(o[8]), false, false)[0])); \
                st_frag(u32x4{pk2(o[0] * il, o[1] * il), pk2(o[2] * il, o[3] * il), pk2(o[4] * il, o[5] * il), pk2(o[6] * il, o[7] * il)}, ws, lane * 16, \
                        WS_O + L * 0x200000 + ((g * 8 + w) * 32 + r) * 1024); SBAR(); } while (0)
            O_STORE();
            STAMP(22);
            float chk = __builtin_fabsf(o[8]);
#pragma unroll
            for (int i = 0; i < 8; ++i) chk += __builtin_fabsf(o[i]);
            if (__builtin_expect(__any(!(chk < 1e36f)), 0)) {
                float m = -__builtin_inff();
                o = f32x16{};
                const u32x4 one4 = {0x3f803f80u, 0x3f803f80u, 0x3f803f80u, 0x3f803f80u};
#pragma unroll 1
                for (int t = 0; t < 64; ++t) {
                    const bf16x8 kk = ld_frag(ws, lane * 16, kv_soff + t * 2048);
                    u32x4 a0 = __builtin_amdgcn_raw_buffer_load_b128(ws, hh * 256 + (lane & 15) * 16, kv_soff + t * 2048 + 1024, AUX_SC1);
                    u32x4 a1 = __builtin_amdgcn_raw_buffer_load_b128(ws, hh * 256 + (lane & 15) * 16, kv_soff + t * 2048 + 1536, AUX_SC1);
                    a0 = gq ? one4 : a0; a1 = gq ? one4 : a1;
                    f32x16 s = MFMA(kk, qf, f32x16{});
                    float tm = m;
#pragma unroll
                    for (int i = 0; i < 16; ++i) tm = __builtin_fmaxf(tm, s[i]);
                    const auto sw = __builtin_amdgcn_permlane32_swap(__float_as_uint(tm), __float_as_uint(tm), false, false);
                    tm = __builtin_fmaxf(__uint_as_float(sw[0]), __uint_as_float(sw[1]));
                    const float al = EX(m - tm);
                    m = tm;
#pragma unroll
                    for (int i = 0; i < 16; ++i) { o[i] *= al; s[i] = EX(s[i] - m); }
                    o = smf(__builtin_bit_cast(bf16x8, a0), lo8(s), o, idx);
                    o = smf(__builtin_bit_cast(bf16x8, a1), hi8(s), o, idx);
                }
                VMWAIT(0);
                O_STORE();
            }
            publish<1>(ws, WS_C2 + ((L * 64 + g * 8 + w) * 2 + (r >> 4)) * 128, lane);
            if (RDBK) touch<1, 0>(lds0 + L_SST + w * 256, (lane & 7) * 128, ws, WS_O + L * 0x200000 + ((g * 8 + w) * 32 + r) * 1024);
            STAMP(8);
        }
        if (w_ < 4) {
            f32x16 acc0, acc1;
            u32x2 bb[4];
            unsigned b2u;
            __builtin_amdgcn_s_setprio(CPRIO);
            {
                PHASE_IDS
                const int kh = w >> 1;
#pragma unroll
                for (int i = 0; i < 4; ++i) bb[i] = *(const u32x2*)(b1 + ((w1_on ? ft1 : 0) * 2 + kh) * 32 + 8 * i + 4 * hh);
                b2u = b2[own_s32 * 32 + l31];
#pragma unroll
                for (int i = 0; i < 8; ++i) { acc0[2 * i] = bf_lo(xres[i]); acc0[2 * i + 1] = bf_hi(xres[i]); }
                PIN(acc0);
                (wait_cnt(ws, WS_C2 + ((L * 64 + own_s32) * 2 + kh) * 128, 16, lane));
                STAMP(9);
                gemm_compute<4, 0, false, false, 48>(lds, lane, kh, 0, ws, WS_O + L * 0x200000 + (own_s32 * 32 + kh * 16) * 1024, 0, acc0, acc1 ISTARG(48));
                STAMP(10);
                exchange(lds, lane, w, w ^ 2, acc0, acc1);
#pragma unroll
                for (int i = 0; i < 8; ++i) xres[i] = pk2(acc0[2 * i], acc0[2 * i + 1]);
                const unsigned st_soff = WS_ST + (L * 64 + own_s32) * 4096, tg = L + 1;
                {
                    const u32x2 rp = row_partial(xres);
                    __builtin_amdgcn_raw_buffer_store_b64(u32x2{rp[0], (rp[1] & ~3u) | tg}, ws, hh ? OOB : l31 * 8u, st_soff + own_d32 * 256, AUX_SC1);
                    if (STCNT) arrive<1>(ws, WS_C6 + (L * 64 + own_s32) * 128, lane);
                }
                STAMP(11);
                const lptr tp = lds + L_SCR + (w ^ 2) * 4096;
                *(LAS u32x4*)(tp + l31 * 64 + hh * 32) = u32x4{xres[0], xres[1], xres[2], xres[3]};
                *(LAS u32x4*)(tp + l31 * 64 + hh * 32 + 16) = u32x4{xres[4], xres[5], xres[6], xres[7]};
                const lptr rp = tp + (4 * hh + ((lane >> 2) & 3)) * 64 + ((lane >> 4) & 1) * 32 + (lane & 3) * 8;
                float xt[16];
#pragma unroll
                for (int t = 0; t < 2; ++t) {
                    const u32x4 v = __builtin_bit_cast(u32x4, tr_frag(rp + t * 1024, rp + t * 1024 + 512));
#pragma unroll
                    for (int i = 0; i < 4; ++i) { xt[8 * t + 2 * i] = bf_lo(v[i]); xt[8 * t + 2 * i + 1] = bf_hi(v[i]); }
                }
#pragma unroll
                for (int i = 0; i < 16; ++i) PIN(xt[i]);
                if (STCNT) NOTE(30, wait_cnt(ws, WS_C6 + (L * 64 + own_s32) * 128, 16, lane)); else VMWAIT(0);
                u32x4 gv[4];
                unsigned it;
#pragma unroll 1
                for (it = 0; it < SPIN_CAP; ++it) {
                    unsigned bad = 0;
#pragma unroll
                    for (int i = 0; i < 4; ++i) gv[i] = __builtin_amdgcn_raw_buffer_load_b128(ws, lane * 16 + i * 1024, st_soff, AUX_SC1);
#pragma unroll
                    for (int i = 0; i < 4; ++i) bad |= (gv[i][1] ^ tg) | (gv[i][3] ^ tg);
                    if (__all((bad & 3) == 0)) break;
                    POLL_PAUSE();
                }
                (void)it;
                STAMP(12);
                {
                    const f32x4 tot = record_total(gv);
                    const float m0 = tot[0] * (1.f / 512.f), m1 = tot[2] * (1.f / 512.f);
                    const float r0 = __builtin_amdgcn_rsqf(__builtin_fmaxf(tot[1] * (1.f / 512.f) - m0 * m0, 0.f) * (1.f + LN_EPS) + LN_EPS * LN_EPS);
                    const float r1 = __builtin_amdgcn_rsqf(__builtin_fmaxf(tot[3] * (1.f / 512.f) - m1 * m1, 0.f) * (1.f + LN_EPS) + LN_EPS * LN_EPS);
                    *(LAS f32x2*)(tp + 2048 + hh * 128 + (lane & 15) * 8) = hh ? f32x2{m0 * r0, m1 * r1} : f32x2{r0, r1};
                }
                const unsigned hv = (hh * 32 + ((l31 & 3) | ((l31 & 12) << 1) | ((l31 & 16) >> 2))) * 16;
#pragma unroll
                for (int t = 0; t < 2; ++t) {
                    u32x4 h;
#pragma unroll
                    for (int j = 0; j < 2; ++j) {
                        const f32x4 sc = *(LAS const f32x4*)(tp + 2048 + (16 * t + 8 * j + 4 * hh) * 4), ms = *(LAS const f32x4*)(tp + 2176 + (16 * t + 8 * j + 4 * hh) * 4);
                        const float* x = xt + 8 * t + 4 * j;
                        h[2 * j] = pk2(x[0] * sc[0] - ms[0], x[1] * sc[1] - ms[1]); h[2 * j + 1] = pk2(x[2] * sc[2] - ms[2], x[3] * sc[3] - ms[3]);
                    }
                    st_frag(h, ws, hv, WS_H2 + L * 0x200000 + (own_d32 * 128 + own_s32 * 2 + t) * 1024);
                }
                STAMP(23);
                publish<2>(ws, WS_C3 + ((L * 16 + own_d32) * 2 + (own_s32 >> 5)) * 256, lane);
                if (L == 0 && bid == 0 && w == 0)
                    __builtin_amdgcn_raw_buffer_store_b64(u32x2{nonce[0] + 1u, nonce[1] + (nonce[0] == 0xffffffffu)}, ws, lane == 0 ? 0u : OOB, WS_NONCE, AUX_SC1);
                STAMP(13);
            }
            if (w1_on) {
                PHASE_IDS
                const int kh = w >> 1, d32 = dt1 * 2 + (w & 1), f32t = ft1 * 2 + kh;
#pragma unroll
                for (int i = 0; i < 16; ++i) { const unsigned bw = bb[i >> 2][(i >> 1) & 1]; acc0[i] = (i & 1) ? bf_hi(bw) : bf_lo(bw); }
                PIN(acc0);
                NOTE(28, wait_cnt(ws, WS_C3 + ((L * 16 + d32) * 2 + kh) * 256 + (g & 2) * 64, 32, lane));
                STAMP(14);
                gemm_compute<16, 4, false, true, 0>(lds, lane, kh, w & 1, ws, WS_H2 + L * 0x200000 + (d32 * 128 + kh * 64) * 1024, L * 32, acc0, acc1 ISTARG(0));
                STAMP(15);
                exchange(lds, lane, w, w ^ 2, acc0, acc1);
                float u[16];
#pragma unroll
                for (int i = 0; i < 16; ++i) {
                    const float z = acc0[i];
                    const float e = EX(z * __builtin_fmaf(z * z, -0.10294324f, -2.30220818f));
                    u[i] = z * __builtin_amdgcn_rcpf(1.f + e);
                }
#pragma unroll
                for (int t = 0; t < 2; ++t)
                    st_frag(u32x4{pk2(u[8 * t], u[8 * t + 1]), pk2(u[8 * t + 2], u[8 * t + 3]), pk2(u[8 * t + 4], u[8 * t + 5]), pk2(u[8 * t + 6], u[8 * t + 7])}, ws, lane * 16,
                            WS_U + L * 0x200000 + (d32 * 112 + f32t * 2 + t) * 1024);
                STAMP(24);
                publish<2>(ws, WS_C4 + ((L * 16 + d32) * 2 + ((f32t + 4) >> 5)) * 256, lane);
                STAMP(16);
            }
            {
                PHASE_IDS
                const int kh = w & 1;
                const float bias = __uint_as_float(b2u << 16);
#pragma unroll
                for (int i = 0; i < 8; ++i) { acc0[2 * i] = bf_lo(xres[i]) + bias; acc0[2 * i + 1] = bf_hi(xres[i]) + bias; }
                PIN(acc0);
                NOTE(29, wait_cnt(ws, WS_C4 + ((L * 16 + own_d32) * 2 + kh) * 256 + (g & 1) * 128, 28, lane));
                STAMP(17);
                gemm_compute<14, 4, true, true, 16>(lds, lane, kh, w >> 1, ws, WS_U + L * 0x200000 + (own_d32 * 112 + kh * 56) * 1024, L * 32 + 16, acc0, acc1 ISTARG(16));
                STAMP(18);
                exchange(lds, lane, w, w ^ 1, acc0, acc1);
#pragma unroll
                for (int i = 0; i < 8; ++i) xres[i] = pk2(acc0[2 * i], acc0[2 * i + 1]);
                if (L == 0) {
#pragma unroll
                    for (int t = 0; t < 2; ++t)
                        st_frag(u32x4{xres[4 * t], xres[4 * t + 1], xres[4 * t + 2], xres[4 * t + 3]}, ws, (l31 >> 4) * 1024 + ((lane & 15) + 16 * hh) * 16 + t * 512,
                                WS_X2 + (own_s32 * 32 + own_d32 * 2) * 1024);
                    __builtin_amdgcn_raw_buffer_store_b64(row_partial(xres), ws, hh ? OOB : l31 * 8u, WS_SP + ((64 + own_s32) * 16 + own_d32) * 256, AUX_SC1);
                    STAMP(25);
                    publish<2>(ws, WS_C5 + own_s32 * 256, lane);
                    if (RDBK) {
                        const unsigned xa = opaque(WS_X2 + (own_s32 * 32 + own_d32 * 2) * 1024 + (lane & 15) * 128), sa = opaque(WS_SP + ((64 + own_s32) * 16 + own_d32) * 256 + (lane & 1) * 128);
                        touch<1, 0>(lds0 + L_SST + w * 256, hh ? sa : xa, ws, 0);
                    }
                } else {
                    char* op = (char*)out + (own_s32 * 32 + l31) * 1024 + own_d32 * 64 + hh * 16;
#pragma unroll
                    for (int t = 0; t < 2; ++t) {
                        const auto s0 = __builtin_amdgcn_permlane32_swap(xres[4 * t], xres[4 * t + 2], false, false);
                        const auto s1 = __builtin_amdgcn_permlane32_swap(xres[4 * t + 1], xres[4 * t + 3], false, false);
                        *(u32x4*)(op + t * 32) = u32x4{s0[0], s1[0], s0[1], s1[1]};
                    }
                }
                STAMP(19);
            }
            __builtin_amdgcn_s_setprio(0);
            UNDEF_WC();
        } else {
            PHASE_IDS
            const int lw = w - 4;
            const rsrc_t r1 = mk_rsrc(w1, 2048 * 3584), r2 = mk_rsrc(w2, 1792 * 4096);
            const unsigned v1o = wvoff(lane, 3584), v2o = wvoff(lane, 4096), lb = lds0 + lw * 4096;
            const unsigned g1 = ((lw >> 1) * 1024 + (lw & 1) * 32) * 3584 + ft1 * 128, g2 = ((lw >> 1) * 896 + (lw & 1) * 32) * 4096 + st64 * 128;
            const lptr lp = lds + lw * 4096 + lane * 16, fill = lds + L_FILL + lw * 4, prog = lds + L_PROG + (lw >> 1) * 8;
            u32x4 rg[8][4];
#define W2WARM() do { if (WARM2) touch<1, 0>(lds0 + L_SST + w * 256, (lane < 56 ? lane : 0) * 4096, r2, (dt64 * 224 + lw * 56) * 4096 + st64 * 128); } while (0)
            if (w1_on) {
                ring_prefetch<0, 4, 4, 3584, TS1, 0>(lb, rg, r1, v1o, g1, fill, L * 32 ISTARG(0));
                BAR();
                ring_prefetch<4, 16, 4, 3584, TS1, 0>(lb, rg, r1, v1o, g1, fill, L * 32 ISTARG(0));
                W2WARM();
                ring_refill<16, 4, TS1, 0>(lp, rg, prog, fill, L * 32 ISTARG(0));
            }
            else W2WARM();
            BAR();
            ring_prefetch<0, 14, 4, 4096, TS2, 16>(lb, rg, r2, v2o, g2, fill, L * 32 + 16 ISTARG(16));
            ring_refill<14, 4, TS2, 16>(lp, rg, prog, fill, L * 32 + 16 ISTARG(16));
            BAR();
        }
    }
}

extern "C" void kernel_launch(void* const* d_in, const int* in_sizes, int n_in, void* d_out, int out_size, void* d_ws, size_t ws_size,
                              hipStream_t stream) {
    fused_fwd<<<256, 512, 0, stream>>>(d_in[0], d_in[1], d_in[2], d_in[4], (const unsigned short*)d_in[5], d_in[6], (const unsigned short*)d_in[7],
                                       (unsigned short*)d_out, (char*)d_ws);
}
```

```cpp
#include <hip/hip_runtime.h>
#include <hip/hip_bf16.h>
#include <stdint.h>

#define WARM1 5
#define WARM2 1
#define AN0 6
#define APD 12
#define XN0 6
#define XPD 12
#define CPRIO 2
#define APRIO 1
#define TS1 1
#define TS2 3
#define RDBK 1
#define QPRIO 1
#define STCNT 1
#define GLA 2
#define NOWR 0
#define KVS 0
#define WARM2C 2
#define X0F 0
#define CSB 1
#define LNK2 2
#define LNK 6
#define INNER 15
#define STAMPS 0

typedef __attribute__((ext_vector_type(16))) float f32x16;
typedef __attribute__((ext_vector_type(8))) float f32x8;
typedef __attribute__((ext_vector_type(4))) float f32x4;
typedef __attribute__((ext_vector_type(2))) float f32x2;
typedef __attribute__((ext_vector_type(16))) __bf16 bf16x16;
typedef __attribute__((ext_vector_type(8))) __bf16 bf16x8;
typedef __attribute__((ext_vector_type(2))) __bf16 bf16x2;
typedef __attribute__((ext_vector_type(4))) unsigned u32x4;
typedef __attribute__((ext_vector_type(2))) unsigned u32x2;
typedef __attribute__((ext_vector_type(4))) short s16x4;
typedef __amdgpu_buffer_rsrc_t rsrc_t;
#define LAS __attribute__((address_space(3)))
typedef LAS char* lptr;
#define DI __device__ __forceinline__

constexpr unsigned WS_NONCE = 0x0;
constexpr unsigned WS_F0 = 0x1000;
constexpr unsigned WS_F5 = 0x2000;
constexpr unsigned WS_C1 = 0x10000;
constexpr unsigned WS_C2 = 0x20000;
constexpr unsigned WS_C3 = 0x28000;
constexpr unsigned WS_C4 = 0x2C000;
constexpr unsigned WS_C5 = 0x30000;
constexpr unsigned WS_C6 = 0x34000;
constexpr unsigned WS_ST = 0x40000;
constexpr unsigned WS_SP = 0x140000;
constexpr unsigned WS_KV = 0x200000;
constexpr unsigned WS_O = 0xA00000;
constexpr unsigned WS_H2 = 0xE00000;
constexpr unsigned WS_U = 0x1200000;
constexpr unsigned WS_X2 = 0x1600000;
constexpr unsigned WS_DBG = 0x1800000;
constexpr unsigned WS_END = 0x1A00000;
constexpr unsigned L_WIMG = 16384;
constexpr unsigned L_ONES = 131072;
constexpr unsigned L_CSP = 135168;
constexpr unsigned L_PROG = L_CSP + 1792;
constexpr unsigned L_FILL = L_PROG + 16;
constexpr unsigned L_SST = L_CSP + 2048;
constexpr unsigned L_REG = 139264;
constexpr unsigned L_SCR = 147456;
constexpr unsigned L_TOTAL = 163840;
constexpr int AUX_SC1 = 16;
constexpr unsigned OOB = 0x80000000u;
constexpr unsigned SPIN_CAP = 1u << 16;
constexpr float LN_EPS = 1e-5f;

#define SBAR() __builtin_amdgcn_sched_barrier(0)
#define PIN(x) asm volatile("" : "+v"(x))
#define MFMA(a, b, c) __builtin_amdgcn_mfma_f32_32x32x16_bf16(a, b, c, 0, 0, 0)
#define EX(x) __builtin_amdgcn_exp2f(x)
#define VMWAIT(n) asm volatile("s_waitcnt vmcnt(" #n ")" ::: "memory")
#define BAR() asm volatile("s_waitcnt lgkmcnt(0)\n\ts_barrier" ::: "memory")

template <int N> DI void vm_wait() { asm volatile("s_waitcnt vmcnt(%0)" ::"i"(N) : "memory"); }
template <int N> DI void vm_wait_bar() { asm volatile("s_waitcnt vmcnt(%0) lgkmcnt(0)\n\ts_barrier" ::"i"(N) : "memory"); }
DI rsrc_t mk_rsrc(const void* p, unsigned bytes) { asm volatile("" : "+s"(p)); return __builtin_amdgcn_make_buffer_rsrc((void*)p, 0, bytes, 0x00020000); }
template <int N, int LSTEP, int GSTEP> DI void dma(unsigned lds, unsigned voff, rsrc_t rs, unsigned soff) {
    unsigned t;
    asm volatile("s_mov_b32 m0, %1\n\ts_mov_b32 %0, %4\n\ts_nop 2\n\tbuffer_load_dwordx4 %2, %3, %0 offen lds\n\t"
                 ".rept %5\n\ts_add_u32 m0, m0, %6\n\ts_add_u32 %0, %0, %7\n\tbuffer_load_dwordx4 %2, %3, %0 offen lds\n\t.endr"
                 : "=&s"(t) : "s"(lds), "v"(voff), "s"(rs), "s"(soff), "i"(N - 1), "i"(LSTEP), "i"(GSTEP) : "memory", "scc");
}
template <int N, int LSTEP, int GSTEP> DI void dma_sc1(unsigned lds, unsigned voff, rsrc_t rs, unsigned soff) {
    unsigned t;
    asm volatile("s_mov_b32 m0, %1\n\ts_mov_b32 %0, %4\n\ts_nop 2\n\tbuffer_load_dwordx4 %2, %3, %0 offen sc1 lds\n\t"
                 ".rept %5\n\ts_add_u32 m0, m0, %6\n\ts_add_u32 %0, %0, %7\n\tbuffer_load_dwordx4 %2, %3, %0 offen sc1 lds\n\t.endr"
                 : "=&s"(t) : "s"(lds), "v"(voff), "s"(rs), "s"(soff), "i"(N - 1), "i"(LSTEP), "i"(GSTEP) : "memory", "scc");
}
template <int N, int GSTEP> DI void touch(unsigned pad, unsigned voff, rsrc_t rs, unsigned soff) {
    unsigned t;
    asm volatile("s_mov_b32 m0, %1\n\ts_mov_b32 %0, %4\n\ts_nop 2\n\tbuffer_load_dword %2, %3, %0 offen sc1 lds\n\t"
                 ".rept %5\n\ts_add_u32 %0, %0, %6\n\tbuffer_load_dword %2, %3, %0 offen sc1 lds\n\t.endr"
                 : "=&s"(t) : "s"(pad), "v"(voff), "s"(rs), "s"(soff), "i"(N - 1), "i"(GSTEP) : "memory", "scc");
}
DI bf16x8 ld_frag(rsrc_t rs, unsigned voff, unsigned soff) { return __builtin_bit_cast(bf16x8, __builtin_amdgcn_raw_buffer_load_b128(rs, voff, soff, AUX_SC1)); }
DI void st_frag(u32x4 v, rsrc_t rs, unsigned voff, unsigned soff) {
    __builtin_amdgcn_raw_buffer_store_b128(v, rs, voff, soff, AUX_SC1);
    asm volatile("s_nop 1" ::"v"(v) : "memory");
}
DI bf16x8 lds_frag(lptr p) { return *(LAS const bf16x8*)p; }
DI s16x4 lds_tr(lptr p) { return __builtin_amdgcn_ds_read_tr16_b64_v4i16((LAS s16x4*)p); }
DI bf16x8 tr_frag(lptr p0, lptr p1) {
    const s16x4 a = lds_tr(p0), b = lds_tr(p1);
    return __builtin_bit_cast(bf16x8, __builtin_shufflevector(a, b, 0, 1, 2, 3, 4, 5, 6, 7));
}
DI unsigned pk2(float a, float b) { const f32x2 v = {a, b}; return __builtin_bit_cast(unsigned, __builtin_convertvector(v, bf16x2)); }
DI float bf_lo(unsigned u) { return __uint_as_float(u << 16); }
DI float bf_hi(unsigned u) { return __uint_as_float(u & 0xffff0000u); }
DI float half_sum2(float a, float b) { const auto r = __builtin_amdgcn_permlane32_swap(__float_as_uint(a), __float_as_uint(b), false, false); return __uint_as_float(r[0]) + __uint_as_float(r[1]); }
DI f32x8 lo8(f32x16 v) { return __builtin_shufflevector(v, v, 0, 1, 2, 3, 4, 5, 6, 7); }
DI f32x8 hi8(f32x16 v) { return __builtin_shufflevector(v, v, 8, 9, 10, 11, 12, 13, 14, 15); }
DI f32x16 smf(bf16x8 a, f32x8 p, f32x16 c, int idx) { return __builtin_amdgcn_smfmac_f32_32x32x32_bf16(a, __builtin_bit_cast(bf16x16, p), c, idx, 0, 0); }
DI int opaque(int v) { asm volatile("" : "+v"(v)); return v; }
DI int sopaque(int v) { asm volatile("" : "+s"(v)); return v; }

#define PSLEEP 0
#define POLL_PAUSE() do { if (PSLEEP) __builtin_amdgcn_s_sleep(PSLEEP); asm volatile("" ::: "memory"); } while (0)
DI unsigned wait_cnt(rsrc_t ws, unsigned soff, unsigned target, int lane) {
    unsigned i;
#pragma unroll 1
    for (i = 0; i < SPIN_CAP; ++i) {
        const unsigned v = __builtin_amdgcn_raw_ptr_buffer_atomic_add_i32(0, ws, lane == 0 ? 0u : OOB, soff, AUX_SC1);
        if ((unsigned)__builtin_amdgcn_readfirstlane(v) >= target) break;
        POLL_PAUSE();
    }
    asm volatile("" ::: "memory");
    return i;
}
template <int NREP> DI void arrive(rsrc_t ws, unsigned soff, int lane) {
    __builtin_amdgcn_raw_ptr_buffer_atomic_add_i32(1, ws, lane < NREP ? lane * 128u : OOB, soff, AUX_SC1);
    asm volatile("" ::: "memory");
}
template <int NREP> DI void publish(rsrc_t ws, unsigned soff, int lane) { VMWAIT(0); arrive<NREP>(ws, soff, lane); }
DI unsigned wait64(rsrc_t ws, unsigned voff, unsigned soff, unsigned tlo, unsigned thi) {
    unsigned i;
#pragma unroll 1
    for (i = 0; i < SPIN_CAP; ++i) {
        const u32x2 v = __builtin_amdgcn_raw_buffer_load_b64(ws, voff, soff, AUX_SC1);
        if (__all(((v[0] ^ tlo) | (v[1] ^ thi)) == 0)) break;
        POLL_PAUSE();
    }
    asm volatile("" ::: "memory");
    return i;
}

#define ISTAMP(i) do { } while (0)
#define ISTPAR
#define NOTE(i, v) (void)(v)
#define STAMP(i) do { } while (0)

DI unsigned wvoff(int lane, unsigned ldw) { return (lane >> 3) * ldw + (((lane & 7) ^ (((lane >> 4) & 1) << 2)) << 4); }
DI void lds_post(lptr p, unsigned v) { *(volatile LAS unsigned*)p = v; }
DI unsigned lds_min2(lptr p) {
    const u32x2 v = *(volatile LAS u32x2*)p;
    const unsigned a = __builtin_amdgcn_readfirstlane(v[0]), b = __builtin_amdgcn_readfirstlane(v[1]);
    return a < b ? a : b;
}
template <int SLEEP> DI void lds_need(lptr p, unsigned& have, unsigned need) {
#pragma unroll 1
    for (unsigned i = 0; have < need && i < (SPIN_CAP << 4); ++i) { have = lds_min2(p); if (SLEEP && have < need) __builtin_amdgcn_s_sleep(SLEEP); }
    asm volatile("" ::: "memory");
}
template <int S0, int S1, int SLOT0, int LDW, int TS, int ib> DI void ring_prefetch(unsigned lb, u32x4 (&rg)[8][4], rsrc_t rw, unsigned voff, unsigned gb, lptr fill, unsigned base ISTPAR) {
#pragma unroll
    for (int s = S0; s < S1; ++s) {
        if (!(s & 1)) ISTAMP(ib + (s >> 1));
        const unsigned so = gb + s * (64 * LDW);
#pragma unroll
        for (int h = 0; h < 2; ++h) {
            if (s < 8) dma<2, 1024, 8 * LDW>(lb + ((s + SLOT0) & 7) * 16384 + h * 2048, voff, rw, so + h * 16 * LDW);
            else {
#pragma unroll
                for (int p = 2 * h; p < 2 * h + 2; ++p)
                    asm volatile("s_nop 4\n\tbuffer_load_dwordx4 %0, %1, %2, %3 offen" : "=v"(rg[s & 7][p]) : "v"(voff), "s"(rw), "s"(so + p * 8 * LDW) : "memory");
            }
            vm_wait<4 * TS>();
        }
        if (s >= TS && s < 8 + TS) lds_post(fill, base + s + 1 - TS);
    }
}
template <int NST, int SLOT0, int TS, int ib> DI void ring_refill(lptr lp, u32x4 (&rg)[8][4], lptr prog, lptr fill, unsigned base ISTPAR) {
    unsigned have = 0;
#pragma unroll
    for (int s = 8; s < NST; ++s) {
        lds_need<1>(prog, have, base + s - 7);
        if (s == NST - TS) asm volatile("s_waitcnt vmcnt(0)" : "+v"(rg[s & 7][0]), "+v"(rg[s & 7][1]), "+v"(rg[s & 7][2]), "+v"(rg[s & 7][3]) :: "memory");
        else asm volatile("" : "+v"(rg[s & 7][0]), "+v"(rg[s & 7][1]), "+v"(rg[s & 7][2]), "+v"(rg[s & 7][3]) :: "memory");
#pragma unroll
        for (int p = 0; p < 4; ++p) if (!NOWR) *(LAS u32x4*)(lp + ((s + SLOT0) & 7) * 16384 + p * 1024) = rg[s & 7][p];
        asm volatile("" ::: "memory");
        lds_post(fill, base + s + 1);
        ISTAMP(ib + s);
    }
}
template <int NST, int SLOT0, bool ACT_A, bool RING, int ib>
DI void gemm_compute(lptr lds, int lane, int kh, int ah, rsrc_t ws, unsigned act_soff, unsigned base, f32x16& acc0, f32x16& acc1 ISTPAR) {
    constexpr int NK = NST * 4, LA = GLA;
    const unsigned av = lane * 16;
    ISTAMP(ib);
    bf16x8 f[NK], w0[NK], w1[NK];
#define ALOAD(i) f[i] = ld_frag(ws, av + ((i) & 3) * 1024, act_soff + ((i) >> 2) * 4096)
#pragma unroll
    for (int i = 0; i < AN0; ++i) ALOAD(i);
    SBAR();
    const int hh = lane >> 5, gq = (lane >> 4) & 1, q = (lane >> 2) & 3, p = lane & 3;
    const unsigned a0 = kh * 8192 + (4 * hh + q) * 128 + (((4 * kh + 2 * gq + (p >> 1)) ^ (((q >> 1) & 1) << 2)) << 4) + (p & 1) * 8;
    const lptr b0 = lds + a0, b1 = lds + (a0 ^ 64), b0h = b0 + 65536, b1h = b1 + 65536;
    const lptr fill = lds + L_FILL + kh * 8, prog = lds + L_PROG + kh * 8 + ah * 4;
    unsigned have = 0;
#define WREAD(dst, pl, ph, k) do { const int sl_ = (((k) >> 2) + SLOT0) & 7, o_ = (sl_ & 3) * 16384 + ((k) & 3) * 2048; \
        const lptr p_ = (sl_ & 4) ? ph : pl; dst[k] = tr_frag(p_ + o_, p_ + o_ + 1024); } while (0)
    acc1 = f32x16{};
    ISTAMP(ib + 1);
    if (RING) lds_need<0>(fill, have, base + 1);
    ISTAMP(ib + 2);
#pragma unroll
    for (int k = 0; k < LA; ++k) { WREAD(w1, b1, b1h, k); WREAD(w0, b0, b0h, k); }
    int nl = AN0;
#pragma unroll
    for (int k = 0; k < NK; ++k) {
        if (RING && ((k + LA) & 3) == 0 && k + LA < NK) lds_need<0>(fill, have, base + ((k + LA) >> 2) + 1);
        SBAR();
        acc1 = ACT_A ? MFMA(f[k], w1[k], acc1) : MFMA(w1[k], f[k], acc1);
        SBAR();
        if (k + LA < NK) WREAD(w1, b1, b1h, k + LA);
        if (nl < NK && nl < k + APD) { ALOAD(nl); ++nl; }
        SBAR();
        acc0 = ACT_A ? MFMA(f[k], w0[k], acc0) : MFMA(w0[k], f[k], acc0);
        SBAR();
        if (RING && (k & 3) == 3 && (k >> 2) + 8 < NST) lds_post(prog, base + (k >> 2) + 1);
        if (k + LA < NK) WREAD(w0, b0, b0h, k + LA);
        if (nl < NK && nl < k + 1 + APD) { ALOAD(nl); ++nl; }
        if (k == 0) ISTAMP(ib + 3);
        if ((k & 7) == 7) ISTAMP(ib + 4 + (k >> 3));
    }
    SBAR();
#undef WREAD
#undef ALOAD
}
DI void exchange(lptr lds, int lane, int w, int partner, f32x16& acc0, const f32x16& acc1) {
    LAS f32x4* dst = (LAS f32x4*)(lds + L_SCR + w * 4096 + lane * 16);
#pragma unroll
    for (int i = 0; i < 4; ++i) dst[i * 64] = f32x4{acc1[4 * i], acc1[4 * i + 1], acc1[4 * i + 2], acc1[4 * i + 3]};
    BAR();
    LAS const f32x4* src = (LAS const f32x4*)(lds + L_SCR + partner * 4096 + lane * 16);
#pragma unroll
    for (int i = 0; i < 4; ++i) { const f32x4 v = src[i * 64]; acc0[4 * i] += v[0]; acc0[4 * i + 1] += v[1]; acc0[4 * i + 2] += v[2]; acc0[4 * i + 3] += v[3]; }
}

#define MFMA16(a, b, c) __builtin_amdgcn_mfma_f32_16x16x32_bf16(a, b, c, 0, 0, 0)
struct QkvAcc { f32x4 q[2], k[2], v[2], cq, ck, cv; };
struct WFrag { s16x4 h[3][2]; };
DI bf16x8 wpart(const WFrag& f, int i) { return __builtin_bit_cast(bf16x8, __builtin_shufflevector(f.h[i][0], f.h[i][1], 0, 1, 2, 3, 4, 5, 6, 7)); }
DI void qkv_wread(WFrag& n, lptr p0, lptr p1, int o) {
#pragma unroll
    for (int i = 0; i < 3; ++i) { n.h[i][0] = lds_tr(p0 + o + i * 128); n.h[i][1] = lds_tr(p1 + o + i * 128); }
}
DI void qkv_step(QkvAcc& a, bf16x8 x0, bf16x8 x1, const WFrag& f, WFrag& n, bool more, lptr p0, lptr p1, int o) {
    const bf16x8 wq = wpart(f, 0), wk = wpart(f, 1), wv = wpart(f, 2);
    SBAR(); a.q[0] = MFMA16(wq, x0, a.q[0]); SBAR(); if (more) n.h[0][0] = lds_tr(p0 + o);
    SBAR(); a.q[1] = MFMA16(wq, x1, a.q[1]); SBAR(); if (more) n.h[0][1] = lds_tr(p1 + o);
    SBAR(); a.k[0] = MFMA16(wk, x0, a.k[0]); SBAR(); if (more) n.h[1][0] = lds_tr(p0 + o + 128);
    SBAR(); a.k[1] = MFMA16(wk, x1, a.k[1]); SBAR(); if (more) n.h[1][1] = lds_tr(p1 + o + 128);
    SBAR(); a.v[0] = MFMA16(x0, wv, a.v[0]); SBAR(); if (more) n.h[2][0] = lds_tr(p0 + o + 256);
    SBAR(); a.v[1] = MFMA16(x1, wv, a.v[1]); SBAR(); if (more) n.h[2][1] = lds_tr(p1 + o + 256);
    SBAR();
}
DI u32x4 join16(f32x4 a, f32x4 b) {
    const auto s0 = __builtin_amdgcn_permlane16_swap(pk2(a[0], a[1]), pk2(b[0], b[1]), false, false);
    const auto s1 = __builtin_amdgcn_permlane16_swap(pk2(a[2], a[3]), pk2(b[2], b[3]), false, false);
    return u32x4{s0[0], s1[0], s0[1], s1[1]};
}
DI u32x2 row_partial(const unsigned (&x)[8]) {
    float sum = 0.f, sq = 0.f;
#pragma unroll
    for (int i = 0; i < 8; ++i) { const float a = bf_lo(x[i]), b = bf_hi(x[i]); sum += a + b; sq = __builtin_fmaf(a, a, __builtin_fmaf(b, b, sq)); }
    const unsigned t = __float_as_uint(half_sum2(sum, sq));
    const auto sw = __builtin_amdgcn_permlane32_swap(t, t, false, false);
    return u32x2{sw[0], sw[1]};
}
DI f32x4 record_total(const u32x4 (&p)[4]) {
    f32x4 t;
#pragma unroll
    for (int j = 0; j < 4; ++j) {
        const unsigned v = __float_as_uint((__uint_as_float(p[0][j]) + __uint_as_float(p[1][j])) + (__uint_as_float(p[2][j]) + __uint_as_float(p[3][j])));
        const auto a = __builtin_amdgcn_permlane32_swap(v, v, false, false);
        const unsigned u = __float_as_uint(__uint_as_float(a[0]) + __uint_as_float(a[1]));
        const auto b = __builtin_amdgcn_permlane16_swap(u, u, false, false);
        t[j] = __uint_as_float(b[0]) + __uint_as_float(b[1]);
    }
    return t;
}
#define XE(r) "v_exp_f32 v" #r ", v" #r "\n\t"
#define SMF(d, a, b) "v_smfmac_f32_32x32x32_bf16 v[" d "], v[" a "], v[" b "], v173\n\t"
#define ATT_HEAD XE(66) XE(67) XE(68) XE(69) "v_mfma_f32_32x32x16_bf16 v[80:95], v[160:163], v[156:159], v[0:15]\n\t"
#define ATT_FULL(C0, C1, N0, N1) ATT_HEAD \
    "ds_read_b128 v[160:163], %[kp] offset:%[ko]\n\t" XE(70) XE(71) "ds_read_b128 v[" N0 "], %[vp] offset:%[vo]\n\t" XE(72) XE(73) SMF("32:47", C0, "64:71") \
    "ds_read_b128 v[" N1 "], %[vp] offset:%[vo2]\n\t" XE(74) XE(75) XE(76) XE(77) XE(78) XE(79) XE(80) XE(81) SMF("32:47", C1, "72:79") \
    XE(82) XE(83) XE(84) "s_waitcnt lgkmcnt(0)\n\tv_mfma_f32_32x32x16_bf16 v[64:79], v[160:163], v[152:155], v[48:63]\n\t" \
    XE(85) XE(86) XE(87) XE(88) XE(89) SMF("16:31", C0, "80:87") \
    XE(90) XE(91) XE(92) XE(93) XE(94) XE(95) XE(64) XE(65) SMF("16:31", C1, "88:95")
#define ATT_LAST(C0, C1) ATT_HEAD \
    XE(70) XE(71) XE(72) XE(73) SMF("32:47", C0, "64:71") XE(74) XE(75) XE(76) XE(77) XE(78) XE(79) "s_nop 0\n\t" XE(80) XE(81) SMF("32:47", C1, "72:79") \
    XE(82) XE(83) XE(84) XE(85) XE(86) XE(87) XE(88) XE(89) SMF("16:31", C0, "80:87") \
    XE(90) XE(91) XE(92) XE(93) XE(94) XE(95) "s_nop 1\n\t" SMF("16:31", C1, "88:95") "s_nop 11"
#define ATT_OPS(KO, VO) \
    : "+{v[64:79]}"(sA), "+{v[80:95]}"(sB), "+{v[32:47]}"(o), "+{v[16:31]}"(o2), "+{v[160:163]}"(kf), \
      "+{v[164:167]}"(v0), "+{v[168:171]}"(v1), "+{v[176:179]}"(v2), "+{v[180:183]}"(v3) \
    : "{v[48:63]}"(cneg), "{v[0:15]}"(cneg2), "{v[152:155]}"(qf), "{v[156:159]}"(qf2), "{v173}"(idx), \
      [kp] "v"(kp), [vp] "v"(vp), [ko] "i"(KO), [vo] "i"(VO), [vo2] "i"((VO) + 512) : "memory"
#define ATT_EVEN(KO, VO) asm volatile(ATT_FULL("164:167", "168:171", "176:179", "180:183") ATT_OPS(KO, VO))
#define ATT_ODD(KO, VO) asm volatile(ATT_FULL("176:179", "180:183", "164:167", "168:171") ATT_OPS(KO, VO))
#define ATT_ODD_LAST() asm volatile(ATT_LAST("176:179", "180:183") ATT_OPS(0, 0))
DI float neg_row_max(f32x16 s) {
    float m0 = __builtin_fmaxf(s[0], s[1]), m1 = __builtin_fmaxf(s[2], s[3]);
#pragma unroll
    for (int i = 4; i < 16; i += 4) { m0 = __builtin_fmaxf(__builtin_fmaxf(m0, s[i]), s[i + 1]); m1 = __builtin_fmaxf(__builtin_fmaxf(m1, s[i + 2]), s[i + 3]); }
    const float m = __builtin_fmaxf(m0, m1);
    const auto sw = __builtin_amdgcn_permlane32_swap(__float_as_uint(m), __float_as_uint(m), false, false);
    return -__builtin_fmaxf(__uint_as_float(sw[0]), __uint_as_float(sw[1]));
}
DI f32x16 splat16(float m) {
    f32x2 m2 = {m, m};
    PIN(m2);
    f32x16 c;
#pragma unroll
    for (int i = 0; i < 8; ++i) { f32x2 t; asm volatile("v_mov_b64 %0, %1" : "=v"(t) : "v"(m2)); c[2 * i] = t[0]; c[2 * i + 1] = t[1]; }
    PIN(c);
    return c;
}

__global__ __launch_bounds__(512, 2) void fused_fwd(const void* __restrict__ xin, const void* __restrict__ wqkv, const void* __restrict__ wo,
                                                    const void* __restrict__ w1, const unsigned short* __restrict__ b1, const void* __restrict__ w2,
                                                    const unsigned short* __restrict__ b2, unsigned short* __restrict__ out, char* wsp) {
    __shared__ __attribute__((aligned(16))) char lds_raw[L_TOTAL];
    const lptr lds = (lptr)lds_raw;
    const unsigned lds0 = (unsigned)(uintptr_t)lds;
    const int w_ = __builtin_amdgcn_readfirstlane(threadIdx.x >> 6);
#define PHASE_IDS \
    const int lane = opaque(threadIdx.x & 63), l31 = lane & 31, hh = lane >> 5; \
    const int w = sopaque(w_), bid = sopaque(blockIdx.x), g = bid & 7, r = bid >> 3; \
    const int st64 = g * 4 + (r >> 3), dt64 = r & 7; \
    const int dt1 = (g & 1) * 4 + (r & 3), ft1 = (g >> 1) * 7 + (r >> 2); \
    const int own_s32 = st64 * 2 + (w & 1), own_d32 = dt64 * 2 + ((w >> 1) & 1); \
    (void)l31; (void)hh; (void)dt1; (void)ft1; (void)own_s32; (void)own_d32;
    const bool w1_on = (blockIdx.x >> 3) < 28;
    const rsrc_t ws = mk_rsrc(wsp, WS_END);

#define ISTARG(b)
    unsigned xres[8] = {};
    unsigned tag_lo = 0, tag_hi = 0;
    u32x2 nonce = {};
    u32x4 wc[12];
#define UNDEF_WC() do { _Pragma("unroll") for (int i_ = 0; i_ < 12; ++i_) asm volatile("" : "=v"(wc[i_])); } while (0)
    UNDEF_WC();

#pragma unroll 1
    for (int L = 0; L < 2; ++L) {
        bf16x8 qf;
        f32x16 o, o2, cneg;
        {
            PHASE_IDS
            const int gq = (lane >> 4) & 1, q = (lane >> 2) & 3, p = lane & 3, l15 = lane & 15, kg = lane >> 4;
            const unsigned kv_soff = WS_KV + (L * 32 + r) * 131072;
            STAMP(0);
            QkvAcc a = {};
            u32x4 sp[4];
            float rt[2], mt[2];
            f32x4 rs[2], ms[2];
            asm volatile("" : "=v"(rt[0]), "=v"(rt[1]), "=v"(mt[0]), "=v"(mt[1]), "=v"(rs[0]), "=v"(rs[1]), "=v"(ms[0]), "=v"(ms[1]));
#define LN_PREP() do { \
                const f32x4 tot = record_total(sp); \
                const float m0 = tot[0] * (1.f / 512.f), m1 = tot[2] * (1.f / 512.f); \
                const float r0 = __builtin_amdgcn_rsqf(__builtin_fmaxf(tot[1] * (1.f / 512.f) - m0 * m0, 0.f) + LN_EPS); \
                const float r1 = __builtin_amdgcn_rsqf(__builtin_fmaxf(tot[3] * (1.f / 512.f) - m1 * m1, 0.f) + LN_EPS); \
                const lptr sst = lds + L_SST + w * 256; \
                *(LAS f32x2*)(sst + hh * 128 + l15 * 8) = hh ? f32x2{m0 * r0, m1 * r1} : f32x2{r0, r1}; \
                _Pragma("unroll") for (int t = 0; t < 2; ++t) { \
                    rt[t] = *(LAS const float*)(sst + t * 64 + l15 * 4); mt[t] = *(LAS const float*)(sst + 128 + t * 64 + l15 * 4); \
                    rs[t] = *(LAS const f32x4*)(sst + t * 64 + kg * 16); ms[t] = *(LAS const f32x4*)(sst + 128 + t * 64 + kg * 16); } \
                SBAR(); } while (0)
#define CS_READ() do { a.cq = *(LAS const f32x4*)(lds + L_CSP + kg * 16); a.ck = *(LAS const f32x4*)(lds + L_CSP + 64 + kg * 16); \
                a.cv[0] = *(LAS const float*)(lds + L_CSP + 128 + l15 * 4); } while (0)
            const unsigned sp_soff = WS_SP + (L * 64 + g * 8 + w) * 4096;
            if (L == 0) {
                const rsrc_t rx = mk_rsrc(xin, 2048 * 1024), rw = mk_rsrc(wqkv, 512 * 3072);
                const unsigned tokb = (g * 256 + w * 32) * 1024, stg = w < 5 ? 65536 + w * 12288 : (w == 5 ? 0 : L_REG + (w - 6) * 12288);
                const unsigned vA = (lane >> 3) * 1024 + (((lane & 7) ^ (lane >> 4)) << 4), vB = vA ^ 64;
#define XCHUNK(c) do { dma<2, 2048, 16384>(lds0 + stg + ((c) % 3) * 4096, vA, rx, tokb + (c) * 128); \
                       dma<2, 2048, 16384>(lds0 + stg + ((c) % 3) * 4096 + 1024, vB, rx, tokb + 8192 + (c) * 128); } while (0)
#define NONCE_TAG() do { \
                    unsigned long long z = ((unsigned long long)__builtin_amdgcn_readfirstlane(nonce[1]) << 32) | (unsigned)__builtin_amdgcn_readfirstlane(nonce[0]); \
                    z = z * 0x9E3779B97F4A7C15ull + 0x632BE59BD9B4E019ull; \
                    z ^= z >> 29; z *= 0xBF58476D1CE4E5B9ull; z ^= z >> 32; \
                    tag_lo = (unsigned)z; tag_hi = (unsigned)(z >> 32); } while (0)
                asm volatile("s_nop 4\n\tbuffer_load_dwordx2 %0, off, %1, 0 sc1" : "=v"(nonce) : "s"(ws) : "memory");
                if (w < 4) {
#pragma unroll
                    for (int i = 0; i < 4; ++i) {
                        const u32x2 v = __builtin_amdgcn_raw_buffer_load_b64(rx, l31 * 1024 + hh * 8 + i * 16, own_s32 * 32768 + own_d32 * 64, 0);
                        xres[2 * i] = v[0]; xres[2 * i + 1] = v[1];
                    }
                    __builtin_amdgcn_raw_buffer_store_b64(u32x2{}, ws, l31 * 8 + hh * 0x40000, WS_ST + (own_s32 * 16 + own_d32) * 256, AUX_SC1);
                    const unsigned c1a = opaque(WS_C1 + (r * 8 + g) * 128 + lane * 32768), c3a = opaque(WS_C3 + bid * 256 + (lane - 3) * 128);
                    const unsigned c2a = sopaque(WS_C2 + (((r >> 4) * 64 + g * 8) * 2 + (r & 15)) * 128);
                    const unsigned ca = lane < 2 ? c1a : (lane == 2 ? c2a : c3a);
                    __builtin_amdgcn_raw_buffer_store_b32(0u, ws, (w == 0 && lane < 5) ? ca : OOB, 0, AUX_SC1);
                    __builtin_amdgcn_raw_buffer_store_b64(row_partial(xres), ws, hh ? OOB : l31 * 8u, WS_SP + (own_s32 * 16 + own_d32) * 256, AUX_SC1);
                    if (X0F) XCHUNK(0);
                    asm volatile("s_waitcnt vmcnt(0)" : "+v"(nonce) :: "memory");
                    NONCE_TAG();
                    const unsigned f5a = sopaque(WS_F5 + own_s32 * 128 + own_d32 * 8), f0a = sopaque(w == 0 ? WS_F0 + r * 128 + g * 8 : OOB);
                    __builtin_amdgcn_raw_buffer_store_b64(u32x2{tag_lo, tag_hi}, ws, lane == 0 ? f5a : (lane == 1 ? f0a : OOB), 0, AUX_SC1);
                    if (!X0F) XCHUNK(0);
                } else {
#pragma unroll
                    for (int ty = 0; ty < 3; ++ty) {
                        const unsigned u = ty * 64 + lane, rg = __umul24(u, 171) >> 12, rem = u - __umul24(rg, 24);
                        const unsigned wv = __umul24(4 * (rg ^ ((rg >> 1) & 1)) + ((rem >> 1) & 3), 3072) + (rem >> 3) * 1024 + (rem & 1) * 16;
                        dma<4, 3072, 98304>(lds0 + L_WIMG + (w - 4) * 12288 + ty * 1024, wv, rw, (w - 4) * 393216 + r * 32);
                    }
                    XCHUNK(0);
                }
                XCHUNK(1); XCHUNK(2);
                const lptr p0 = lds + L_WIMG + (4 * hh + 3 * gq) * 384 + q * 32 + p * 8, p1 = lds + L_WIMG + (4 * hh + 1 + gq) * 384 + q * 32 + p * 8;
                lptr px[4];
#pragma unroll
                for (int t = 0; t < 4; ++t) px[t] = lds + stg + ((t & 1) * 16 + l15) * 128 + (((4 * (t >> 1) + kg) ^ (l15 >> 1)) << 4);
                WFrag wf[2];
                const bf16x8 ones = __builtin_bit_cast(bf16x8, u32x4{0x3f803f80u, 0x3f803f80u, 0x3f803f80u, 0x3f803f80u});
                u32x2 fl;
                const unsigned f0r = opaque(WS_F0 + r * 128 + (lane & 7) * 8), f5r = opaque(WS_F5 + (g * 8 + w) * 128 + (lane & 15) * 8), flr = hh ? f0r : f5r;
#pragma unroll
                for (int c = 0; c < 8; ++c) {
                    if (c == 0) ISTAMP(54);
                    if (c <= 5) vm_wait<8>(); else if (c == 6) vm_wait<0>();
                    ISTAMP(55 + c);
                    if (c == 0) { BAR(); STAMP(1);
                        f32x4 c3[3] = {};
#pragma unroll
                        for (int j = 0; j < 2; ++j) {
                            qkv_wread(wf[j], p0 + w * 6144, p1 + w * 6144, j * 3072);
#pragma unroll
                            for (int i = 0; i < 3; ++i) c3[i] = MFMA16(ones, wpart(wf[j], i), c3[i]);
                        }
                        qkv_wread(wf[0], p0, p1, 0);
                        *(LAS float*)(lds + L_CSP + 256 + ((kg < 3 ? kg : 2) * 16 + l15) * 32 + w * 4) = kg == 0 ? c3[0][0] : (kg == 1 ? c3[1][0] : c3[2][0]);
                    }
                    if (c == CSB) {
                        BAR();
                        if (w == 0) {
                            const int n = lane < 48 ? lane : lane - 16;
                            const f32x4 s0 = *(LAS const f32x4*)(lds + L_CSP + 256 + n * 32), s1 = *(LAS const f32x4*)(lds + L_CSP + 256 + n * 32 + 16);
                            *(LAS float*)(lds + L_CSP + n * 4) = ((s0[0] + s0[1]) + (s0[2] + s0[3])) + ((s1[0] + s1[1]) + (s1[2] + s1[3]));
                        }
                    }
                    bf16x8 xf[4];
#pragma unroll
                    for (int t = 0; t < 4; ++t) xf[t] = lds_frag(px[t] + (c % 3) * 4096);
                    if (c == 5) asm volatile("s_nop 4\n\tbuffer_load_dwordx2 %0, %1, %2, 0 offen sc1" : "=v"(fl) : "v"(flr), "s"(ws) : "memory");
                    if (c + 3 < 8) { asm volatile("s_waitcnt lgkmcnt(0)" : "+v"(xf[0]), "+v"(xf[1]), "+v"(xf[2]), "+v"(xf[3]) :: "memory"); XCHUNK(c + 3); }
                    if (c == 0) { asm volatile("" : "+v"(nonce)); NONCE_TAG(); }
                    if (c == 6) {
                        asm volatile("" : "+v"(fl));
                        if (!__all(((fl[0] ^ tag_lo) | (fl[1] ^ tag_hi)) == 0)) wait64(ws, flr, 0, tag_lo, tag_hi);
                        asm volatile("" ::: "memory");
#pragma unroll
                        for (int i = 0; i < 4; ++i) sp[i] = __builtin_amdgcn_raw_buffer_load_b128(ws, lane * 16 + i * 1024, sp_soff, AUX_SC1);
                        SBAR();
                    }
                    if (c == 6) {
                        if (w >= 4) {
#pragma unroll
                            for (int i = 0; i < 12; ++i) wc[i] = *(LAS const u32x4*)(lds + L_WIMG + (w - 4) * 12288 + i * 1024 + lane * 16);
                        }
                    }
#pragma unroll
                    for (int t = 0; t < 2; ++t) {
                        const int k = 2 * c + t;
                        qkv_step(a, xf[2 * t], xf[2 * t + 1], wf[k & 1], wf[(k + 1) & 1], k < 15, p0, p1, (k + 1) * 3072);
                        if ((k == 13 && w >= 4) || (k == 14 && w < 4)) LN_PREP();
                    }
                }
                ISTAMP(63);
                BAR();
                CS_READ();
#undef XCHUNK
            } else {
                if (w >= 4) {
#pragma unroll
                    for (int i = 0; i < 12; ++i) *(LAS u32x4*)(lds + L_WIMG + (w - 4) * 12288 + i * 1024 + lane * 16) = wc[i];
                }
                BAR();
                wait_cnt(ws, WS_C5 + ((g * 8 + w) * 2 + (r & 1)) * 128, 16, lane);
                STAMP(1);
                ISTAMP(32);
                bf16x8 xf[32];
                const unsigned x_soff = WS_X2 + (g * 8 + w) * 32768;
#define XLOAD(k) xf[k] = ld_frag(ws, lane * 16 + ((k) & 3) * 1024, x_soff + ((k) >> 2) * 4096)
#pragma unroll
                for (int i = 0; i < 4; ++i) sp[i] = __builtin_amdgcn_raw_buffer_load_b128(ws, lane * 16 + i * 1024, sp_soff, AUX_SC1);
#pragma unroll
                for (int k = 0; k < XN0; ++k) XLOAD(k);
                SBAR();
                ISTAMP(33);
                ISTAMP(34);
                const lptr p0 = lds + L_WIMG + (4 * hh + gq) * 384 + q * 32 + p * 8, p1 = lds + L_WIMG + (4 * hh + 3 - gq) * 384 + q * 32 + p * 8;
                WFrag wf[2];
                qkv_wread(wf[0], p0, p1, 0);
                int nl = XN0;
                if (QPRIO) __builtin_amdgcn_s_setprio(3);
#pragma unroll
                for (int k = 0; k < 16; ++k) {
                    qkv_step(a, xf[2 * k], xf[2 * k + 1], wf[k & 1], wf[(k + 1) & 1], k < 15, p0, p1, (k + 1) * 3072);
#pragma unroll
                    for (int i = 0; i < 3; ++i) if (nl < 32 && nl < 2 * k + 2 + XPD) { XLOAD(nl); ++nl; }
                    if (QPRIO && k == 5) __builtin_amdgcn_s_setprio(2);
                    if (QPRIO && k == 10) __builtin_amdgcn_s_setprio(1);
                    if (QPRIO && k == 13) __builtin_amdgcn_s_setprio(0);
                    if (k == LNK && w < 4) LN_PREP();
                    if (k == LNK2 && w >= 4) LN_PREP();
                    if (k == 12) CS_READ();
                    if (k == 0) ISTAMP(35);
                    if (k & 1) ISTAMP(36 + (k >> 1));
                }
#undef XLOAD
                UNDEF_WC();
            }
            STAMP(2);
            if (APRIO) __builtin_amdgcn_s_setprio(3);
            const u32x4 kpk = join16(a.k[0] * rt[0] - mt[0] * a.ck, a.k[1] * rt[1] - mt[1] * a.ck);
            const unsigned koff = lane * 16;
            st_frag(kpk, ws, koff, kv_soff + (g * 8 + w) * 2048);
            const u32x4 vpk = join16(a.v[0] * rs[0] - ms[0] * a.cv[0], a.v[1] * rs[1] - ms[1] * a.cv[0]);
            const unsigned voff = gq * 512 + hh * 256 + l15 * 16;
            st_frag(vpk, ws, voff, kv_soff + (g * 8 + w) * 2048 + 1024);
            constexpr float QS = 0.25f * 1.4426950408889634f;
            const u32x4 qpk = join16((a.q[0] * rt[0] - mt[0] * a.cq) * QS, (a.q[1] * rt[1] - mt[1] * a.cq) * QS);
            qf = __builtin_bit_cast(bf16x8, qpk);
            const f32x16 sd = MFMA(__builtin_bit_cast(bf16x8, kpk), qf, f32x16{});
            if (L == 0) {
                if (w == 0) *(LAS unsigned*)(lds + L_PROG + (lane & 7) * 4) = 0u;
                const unsigned one = (lane & 15) ? 0u : 0x3f803f80u;
                *(LAS u32x4*)(lds + L_ONES + w * 512 + hh * 256 + (lane & 15) * 16) = u32x4{one, one, one, one};
            }
            *(LAS u32x4*)(lds + w * 1024 + koff) = kpk;
            *(LAS u32x4*)(lds + 8192 + w * 1024 + voff) = vpk;
            *(LAS u32x4*)(lds + L_REG + w * 3072 + lane * 16) = qpk;
            o = f32x16{}; o2 = f32x16{};
            asm volatile("" : "+{v[32:47]}"(o), "+{v[16:31]}"(o2));
            {
                const float nm = neg_row_max(sd);
                *(LAS float*)(lds + L_REG + w * 3072 + 1024 + lane * 4) = nm;
                cneg = splat16(nm);
            }
            BAR();
            STAMP(3);
        }
        {
            PHASE_IDS
            const int gq = (lane >> 4) & 1;
            const unsigned kv_soff = WS_KV + (L * 32 + r) * 131072;
            const int idx = opaque(0xDDDD);
            lptr kp = lds + (w & 1) * 4096 + lane * 16;
            lptr vp = lds + (gq ? L_ONES : 8192 + (w & 1) * 4096) + hh * 256 + (lane & 15) * 16;
            const int vinc = gq ? 0 : 16384;
            bf16x8 kf = lds_frag(kp);
            const float nm2 = *(LAS const float*)(lds + L_REG + (w ^ 1) * 3072 + 1024 + lane * 4);
            const bf16x8 qf2 = lds_frag(lds + L_REG + (w ^ 1) * 3072 + lane * 16);
            bf16x8 v0 = lds_frag(vp), v1 = lds_frag(vp + 512), v2, v3;
            f32x16 sA, sB;
            asm volatile("" : "=v"(v2), "=v"(v3), "=v"(sB));
#define KVCHUNK(j) dma_sc1<2, 8192, 1024>(lds0 + (j) * 16384 + w * 1024, lane * 16, ws, kv_soff + (((g + (j)) & 7) * 8 + w) * 2048)
            sA = MFMA(kf, qf, cneg);
            const f32x16 cneg2 = splat16(nm2);
            publish<1>(ws, WS_C1 + ((L * 32 + r) * 8 + w) * 128, lane);
            sA[0] = EX(sA[0]); sA[1] = EX(sA[1]);
#define PRIO(n) do { if (APRIO) __builtin_amdgcn_s_setprio(n); } while (0)
            {
                const unsigned c1o = WS_C1 + ((L * 32 + r) * 8 + w) * 128, pv0 = lane == 0 ? 0u : OOB;
                int pc = 0;
#define POLL_ISSUE() asm volatile("s_nop 4\n\tbuffer_atomic_add %0, %1, %2, %3 offen sc0 sc1" : "+v"(pc) : "v"(pv0), "s"(ws), "s"(c1o) : "memory")
#define POLL_READ() asm volatile("s_waitcnt vmcnt(0)" : "+v"(pc) :: "memory")
                PRIO(2);
                ATT_EVEN(1024, 1024);
                POLL_ISSUE();
                ATT_ODD(2048, 2048);
                STAMP(4);
                POLL_READ();
                if (__builtin_amdgcn_readfirstlane(pc) < 8) wait_cnt(ws, c1o, 8, lane);
                STAMP(5);
                KVCHUNK(1);
                PRIO(1);
                ATT_EVEN(3072, 3072);
                if (KVS == 1) KVCHUNK(2);
                PRIO(0);
                ATT_ODD_LAST();
                vm_wait_bar<KVS == 1 ? 2 : 0>();
                STAMP(6);
                kp += 16384; vp += vinc;
                kf = lds_frag(kp); v0 = lds_frag(vp); v1 = lds_frag(vp + 512);
                if (KVS == 1) KVCHUNK(3); else KVCHUNK(2);
                sA = MFMA(kf, qf, cneg);
                sA[0] = EX(sA[0]); sA[1] = EX(sA[1]);
            }
#pragma unroll
            for (int c = 1; c < 8; ++c) {
                if (c == 1 || c == 3 || c == 4 || c == 7) PRIO(2);
                ATT_EVEN(1024, 1024);
                if (c == 1 && KVS != 1) KVCHUNK(3);
                if (c == 2) KVCHUNK(5);
                if (c == 1 || c == 3 || c == 4) PRIO(1);
                ATT_ODD(2048, 2048);
                if (c == 2) KVCHUNK(6);
                if (c == 1 || c == 3 || c == 4) PRIO(0); else if (c == 7) PRIO(1);
                ATT_EVEN(3072, 3072);
                if (c == 1) { vm_wait_bar<0>(); KVCHUNK(4); }
                if (c == 2) KVCHUNK(7);
                if (c == 3) vm_wait_bar<0>();
                if (c == 4) {
                    BAR();
                    dma<8, 1024, 8192>(lds0 + w * 8192, wvoff(lane, 1024), mk_rsrc(wo, 512 * 1024), ((w & 1) * 256 + (w >> 1) * 64) * 1024 + dt64 * 128);
                }
                if (c == WARM1) touch<1, 0>(lds0 + L_SST + w * 256, lane * 3584, mk_rsrc(w1, 2048 * 3584), ((r & 3) * 512 + w * 64) * 3584 + (w1_on ? ft1 : 0) * 128);
                if (c == WARM2C && L == 0) touch<1, 0>(lds0 + L_SST + w * 256, (lane < 28 ? lane : 0) * 4096, mk_rsrc(w2, 1792 * 4096), (dt64 * 224 + w * 28) * 4096 + st64 * 128);
                if (c < 7) { if (c != 2 && c < 5) PRIO(3); vp += vinc; ATT_ODD(16384, 0); kp += 16384; }
                else { PRIO(0); ATT_ODD_LAST(); }
            }
            STAMP(7);
            VMWAIT(0);
            STAMP(20);
            {
                const lptr mine = lds + L_REG + w * 3072 + lane * 16, other = lds + L_REG + (w ^ 1) * 3072 + lane * 16;
                *(LAS f32x4*)mine = f32x4{o2[0], o2[1], o2[2], o2[3]};
                *(LAS f32x4*)(mine + 1024) = f32x4{o2[4], o2[5], o2[6], o2[7]};
                *(LAS float*)(mine + 2048) = o2[8];
                BAR();
                STAMP(21);
                const f32x4 a0 = *(LAS const f32x4*)other, a1 = *(LAS const f32x4*)(other + 1024);
                o[8] += *(LAS const float*)(other + 2048);
#pragma unroll
                for (int i = 0; i < 4; ++i) { o[i] += a0[i]; o[4 + i] += a1[i]; }
            }
#define O_STORE() do { const float il = __builtin_amdgcn_rcpf(__uint_as_float(__builtin_amdgcn_permlane32_swap(__float_as_uint(o[8]), __float_as_uint(o[8]), false, false)[0])); \
                st_frag(u32x4{pk2(o[0] * il, o[1] * il), pk2(o[2] * il, o[3] * il), pk2(o[4] * il, o[5] * il), pk2(o[6] * il, o[7] * il)}, ws, lane * 16, \
                        WS_O + L * 0x200000 + ((g * 8 + w) * 32 + r) * 1024); SBAR(); } while (0)
            O_STORE();
            STAMP(22);
            float chk = __builtin_fabsf(o[8]);
#pragma unroll
            for (int i = 0; i < 8; ++i) chk += __builtin_fabsf(o[i]);
            if (__builtin_expect(__any(!(chk < 1e36f)), 0)) {
                float m = -__builtin_inff();
                o = f32x16{};
                const u32x4 one4 = {0x3f803f80u, 0x3f803f80u, 0x3f803f80u, 0x3f803f80u};
#pragma unroll 1
                for (int t = 0; t < 64; ++t) {
                    const bf16x8 kk = ld_frag(ws, lane * 16, kv_soff + t * 2048);
                    u32x4 a0 = __builtin_amdgcn_raw_buffer_load_b128(ws, hh * 256 + (lane & 15) * 16, kv_soff + t * 2048 + 1024, AUX_SC1);
                    u32x4 a1 = __builtin_amdgcn_raw_buffer_load_b128(ws, hh * 256 + (lane & 15) * 16, kv_soff + t * 2048 + 1536, AUX_SC1);
                    a0 = gq ? one4 : a0; a1 = gq ? one4 : a1;
                    f32x16 s = MFMA(kk, qf, f32x16{});
                    float tm = m;
#pragma unroll
                    for (int i = 0; i < 16; ++i) tm = __builtin_fmaxf(tm, s[i]);
                    const auto sw = __builtin_amdgcn_permlane32_swap(__float_as_uint(tm), __float_as_uint(tm), false, false);
                    tm = __builtin_fmaxf(__uint_as_float(sw[0]), __uint_as_float(sw[1]));
                    const float al = EX(m - tm);
                    m = tm;
#pragma unroll
                    for (int i = 0; i < 16; ++i) { o[i] *= al; s[i] = EX(s[i] - m); }
                    o = smf(__builtin_bit_cast(bf16x8, a0), lo8(s), o, idx);
                    o = smf(__builtin_bit_cast(bf16x8, a1), hi8(s), o, idx);
                }
                VMWAIT(0);
                O_STORE();
            }
            publish<1>(ws, WS_C2 + ((L * 64 + g * 8 + w) * 2 + (r >> 4)) * 128, lane);
            if (RDBK) touch<1, 0>(lds0 + L_SST + w * 256, (lane & 7) * 128, ws, WS_O + L * 0x200000 + ((g * 8 + w) * 32 + r) * 1024);
            STAMP(8);
        }
        if (w_ < 4) {
            f32x16 acc0, acc1;
            u32x2 bb[4];
            unsigned b2u;
            __builtin_amdgcn_s_setprio(CPRIO);
            {
                PHASE_IDS
                const int kh = w >> 1;
#pragma unroll
                for (int i = 0; i < 4; ++i) bb[i] = *(const u32x2*)(b1 + ((w1_on ? ft1 : 0) * 2 + kh) * 32 + 8 * i + 4 * hh);
                b2u = b2[own_s32 * 32 + l31];
#pragma unroll
                for (int i = 0; i < 8; ++i) { acc0[2 * i] = bf_lo(xres[i]); acc0[2 * i + 1] = bf_hi(xres[i]); }
                PIN(acc0);
                (wait_cnt(ws, WS_C2 + ((L * 64 + own_s32) * 2 + kh) * 128, 16, lane));
                STAMP(9);
                gemm_compute<4, 0, false, false, 48>(lds, lane, kh, 0, ws, WS_O + L * 0x200000 + (own_s32 * 32 + kh * 16) * 1024, 0, acc0, acc1 ISTARG(48));
                STAMP(10);
                exchange(lds, lane, w, w ^ 2, acc0, acc1);
#pragma unroll
                for (int i = 0; i < 8; ++i) xres[i] = pk2(acc0[2 * i], acc0[2 * i + 1]);
                const unsigned st_soff = WS_ST + (L * 64 + own_s32) * 4096, tg = L + 1;
                {
                    const u32x2 rp = row_partial(xres);
                    __builtin_amdgcn_raw_buffer_store_b64(u32x2{rp[0], (rp[1] & ~3u) | tg}, ws, hh ? OOB : l31 * 8u, st_soff + own_d32 * 256, AUX_SC1);
                    if (STCNT) arrive<1>(ws, WS_C6 + (L * 64 + own_s32) * 128, lane);
                }
                STAMP(11);
                const lptr tp = lds + L_SCR + (w ^ 2) * 4096;
                *(LAS u32x4*)(tp + l31 * 64 + hh * 32) = u32x4{xres[0], xres[1], xres[2], xres[3]};
                *(LAS u32x4*)(tp + l31 * 64 + hh * 32 + 16) = u32x4{xres[4], xres[5], xres[6], xres[7]};
                const lptr rp = tp + (4 * hh + ((lane >> 2) & 3)) * 64 + ((lane >> 4) & 1) * 32 + (lane & 3) * 8;
                float xt[16];
#pragma unroll
                for (int t = 0; t < 2; ++t) {
                    const u32x4 v = __builtin_bit_cast(u32x4, tr_frag(rp + t * 1024, rp + t * 1024 + 512));
#pragma unroll
                    for (int i = 0; i < 4; ++i) { xt[8 * t + 2 * i] = bf_lo(v[i]); xt[8 * t + 2 * i + 1] = bf_hi(v[i]); }
                }
#pragma unroll
                for (int i = 0; i < 16; ++i) PIN(xt[i]);
                if (STCNT) NOTE(30, wait_cnt(ws, WS_C6 + (L * 64 + own_s32) * 128, 16, lane)); else VMWAIT(0);
                u32x4 gv[4];
                unsigned it;
#pragma unroll 1
                for (it = 0; it < SPIN_CAP; ++it) {
                    unsigned bad = 0;
#pragma unroll
                    for (int i = 0; i < 4; ++i) gv[i] = __builtin_amdgcn_raw_buffer_load_b128(ws, lane * 16 + i * 1024, st_soff, AUX_SC1);
#pragma unroll
                    for (int i = 0; i < 4; ++i) bad |= (gv[i][1] ^ tg) | (gv[i][3] ^ tg);
                    if (__all((bad & 3) == 0)) break;
                    POLL_PAUSE();
                }
                (void)it;
                STAMP(12);
                {
                    const f32x4 tot = record_total(gv);
                    const float m0 = tot[0] * (1.f / 512.f), m1 = tot[2] * (1.f / 512.f);
                    const float r0 = __builtin_amdgcn_rsqf(__builtin_fmaxf(tot[1] * (1.f / 512.f) - m0 * m0, 0.f) * (1.f + LN_EPS) + LN_EPS * LN_EPS);
                    const float r1 = __builtin_amdgcn_rsqf(__builtin_fmaxf(tot[3] * (1.f / 512.f) - m1 * m1, 0.f) * (1.f + LN_EPS) + LN_EPS * LN_EPS);
                    *(LAS f32x2*)(tp + 2048 + hh * 128 + (lane & 15) * 8) = hh ? f32x2{m0 * r0, m1 * r1} : f32x2{r0, r1};
                }
                const unsigned hv = (hh * 32 + ((l31 & 3) | ((l31 & 12) << 1) | ((l31 & 16) >> 2))) * 16;
#pragma unroll
                for (int t = 0; t < 2; ++t) {
                    u32x4 h;
#pragma unroll
                    for (int j = 0; j < 2; ++j) {
                        const f32x4 sc = *(LAS const f32x4*)(tp + 2048 + (16 * t + 8 * j + 4 * hh) * 4), ms = *(LAS const f32x4*)(tp + 2176 + (16 * t + 8 * j + 4 * hh) * 4);
                        const float* x = xt + 8 * t + 4 * j;
                        h[2 * j] = pk2(x[0] * sc[0] - ms[0], x[1] * sc[1] - ms[1]); h[2 * j + 1] = pk2(x[2] * sc[2] - ms[2], x[3] * sc[3] - ms[3]);
                    }
                    st_frag(h, ws, hv, WS_H2 + L * 0x200000 + (own_d32 * 128 + own_s32 * 2 + t) * 1024);
                }
                STAMP(23);
                publish<2>(ws, WS_C3 + ((L * 16 + own_d32) * 2 + (own_s32 >> 5)) * 256, lane);
                if (L == 0 && bid == 0 && w == 0)
                    __builtin_amdgcn_raw_buffer_store_b64(u32x2{nonce[0] + 1u, nonce[1] + (nonce[0] == 0xffffffffu)}, ws, lane == 0 ? 0u : OOB, WS_NONCE, AUX_SC1);
                STAMP(13);
            }
            if (w1_on) {
                PHASE_IDS
                const int kh = w >> 1, d32 = dt1 * 2 + (w & 1), f32t = ft1 * 2 + kh;
#pragma unroll
                for (int i = 0; i < 16; ++i) { const unsigned bw = bb[i >> 2][(i >> 1) & 1]; acc0[i] = (i & 1) ? bf_hi(bw) : bf_lo(bw); }
                PIN(acc0);
                NOTE(28, wait_cnt(ws, WS_C3 + ((L * 16 + d32) * 2 + kh) * 256 + (g & 2) * 64, 32, lane));
                STAMP(14);
                gemm_compute<16, 4, false, true, 0>(lds, lane, kh, w & 1, ws, WS_H2 + L * 0x200000 + (d32 * 128 + kh * 64) * 1024, L * 32, acc0, acc1 ISTARG(0));
                STAMP(15);
                exchange(lds, lane, w, w ^ 2, acc0, acc1);
                float u[16];
#pragma unroll
                for (int i = 0; i < 16; ++i) {
                    const float z = acc0[i];
                    const float e = EX(z * __builtin_fmaf(z * z, -0.10294324f, -2.30220818f));
                    u[i] = z * __builtin_amdgcn_rcpf(1.f + e);
                }
#pragma unroll
                for (int t = 0; t < 2; ++t)
                    st_frag(u32x4{pk2(u[8 * t], u[8 * t + 1]), pk2(u[8 * t + 2], u[8 * t + 3]), pk2(u[8 * t + 4], u[8 * t + 5]), pk2(u[8 * t + 6], u[8 * t + 7])}, ws, lane * 16,
                            WS_U + L * 0x200000 + (d32 * 112 + f32t * 2 + t) * 1024);
                STAMP(24);
                publish<2>(ws, WS_C4 + ((L * 16 + d32) * 2 + ((f32t + 4) >> 5)) * 256, lane);
                STAMP(16);
            }
            {
                PHASE_IDS
                const int kh = w & 1;
                const float bias = __uint_as_float(b2u << 16);
#pragma unroll
                for (int i = 0; i < 8; ++i) { acc0[2 * i] = bf_lo(xres[i]) + bias; acc0[2 * i + 1] = bf_hi(xres[i]) + bias; }
                PIN(acc0);
                NOTE(29, wait_cnt(ws, WS_C4 + ((L * 16 + own_d32) * 2 + kh) * 256 + (g & 1) * 128, 28, lane));
                STAMP(17);
                gemm_compute<14, 4, true, true, 16>(lds, lane, kh, w >> 1, ws, WS_U + L * 0x200000 + (own_d32 * 112 + kh * 56) * 1024, L * 32 + 16, acc0, acc1 ISTARG(16));
                STAMP(18);
                exchange(lds, lane, w, w ^ 1, acc0, acc1);
#pragma unroll
                for (int i = 0; i < 8; ++i) xres[i] = pk2(acc0[2 * i], acc0[2 * i + 1]);
                if (L == 0) {
#pragma unroll
                    for (int t = 0; t < 2; ++t)
                        st_frag(u32x4{xres[4 * t], xres[4 * t + 1], xres[4 * t + 2], xres[4 * t + 3]}, ws, (l31 >> 4) * 1024 + ((lane & 15) + 16 * hh) * 16 + t * 512,
                                WS_X2 + (own_s32 * 32 + own_d32 * 2) * 1024);
                    __builtin_amdgcn_raw_buffer_store_b64(row_partial(xres), ws, hh ? OOB : l31 * 8u, WS_SP + ((64 + own_s32) * 16 + own_d32) * 256, AUX_SC1);
                    STAMP(25);
                    publish<2>(ws, WS_C5 + own_s32 * 256, lane);
                    if (RDBK) {
                        const unsigned xa = opaque(WS_X2 + (own_s32 * 32 + own_d32 * 2) * 1024 + (lane & 15) * 128), sa = opaque(WS_SP + ((64 + own_s32) * 16 + own_d32) * 256 + (lane & 1) * 128);
                        touch<1, 0>(lds0 + L_SST + w * 256, hh ? sa : xa, ws, 0);
                    }
                } else {
                    char* op = (char*)out + (own_s32 * 32 + l31) * 1024 + own_d32 * 64 + hh * 16;
#pragma unroll
                    for (int t = 0; t < 2; ++t) {
                        const auto s0 = __builtin_amdgcn_permlane32_swap(xres[4 * t], xres[4 * t + 2], false, false);
                        const auto s1 = __builtin_amdgcn_permlane32_swap(xres[4 * t + 1], xres[4 * t + 3], false, false);
                        *(u32x4*)(op + t * 32) = u32x4{s0[0], s1[0], s0[1], s1[1]};
                    }
                }
                STAMP(19);
            }
            __builtin_amdgcn_s_setprio(0);
            UNDEF_WC();
        } else {
            PHASE_IDS
            const int lw = w - 4;
            const rsrc_t r1 = mk_rsrc(w1, 2048 * 3584), r2 = mk_rsrc(w2, 1792 * 4096);
            const unsigned v1o = wvoff(lane, 3584), v2o = wvoff(lane, 4096), lb = lds0 + lw * 4096;
            const unsigned g1 = ((lw >> 1) * 1024 + (lw & 1) * 32) * 3584 + ft1 * 128, g2 = ((lw >> 1) * 896 + (lw & 1) * 32) * 4096 + st64 * 128;
            const lptr lp = lds + lw * 4096 + lane * 16, fill = lds + L_FILL + lw * 4, prog = lds + L_PROG + (lw >> 1) * 8;
            u32x4 rg[8][4];
#define W2WARM() do { if (WARM2) touch<1, 0>(lds0 + L_SST + w * 256, (lane < 56 ? lane : 0) * 4096, r2, (dt64 * 224 + lw * 56) * 4096 + st64 * 128); } while (0)
            if (w1_on) {
                ring_prefetch<0, 4, 4, 3584, TS1, 0>(lb, rg, r1, v1o, g1, fill, L * 32 ISTARG(0));
                BAR();
                ring_prefetch<4, 16, 4, 3584, TS1, 0>(lb, rg, r1, v1o, g1, fill, L * 32 ISTARG(0));
                W2WARM();
                ring_refill<16, 4, TS1, 0>(lp, rg, prog, fill, L * 32 ISTARG(0));
            }
            else W2WARM();
            BAR();
            ring_prefetch<0, 14, 4, 4096, TS2, 16>(lb, rg, r2, v2o, g2, fill, L * 32 + 16 ISTARG(16));
            ring_refill<14, 4, TS2, 16>(lp, rg, prog, fill, L * 32 + 16 ISTARG(16));
            BAR();
        }
    }
}

extern "C" void kernel_launch(void* const* d_in, const int* in_sizes, int n_in, void* d_out, int out_size, void* d_ws, size_t ws_size,
                              hipStream_t stream) {
    fused_fwd<<<256, 512, 0, stream>>>(d_in[0], d_in[1], d_in[2], d_in[4], (const unsigned short*)d_in[5], d_in[6], (const unsigned short*)d_in[7],
                                       (unsigned short*)d_out, (char*)d_ws);
}
```

```cpp
#include <hip/hip_runtime.h>
#include <hip/hip_bf16.h>
#include <stdint.h>

typedef __attribute__((ext_vector_type(16))) float f32x16;
typedef __attribute__((ext_vector_type(8))) float f32x8;
typedef __attribute__((ext_vector_type(4))) float f32x4;
typedef __attribute__((ext_vector_type(2))) float f32x2;
typedef __attribute__((ext_vector_type(16))) __bf16 bf16x16;
typedef __attribute__((ext_vector_type(8))) __bf16 bf16x8;
typedef __attribute__((ext_vector_type(2))) __bf16 bf16x2;
typedef __attribute__((ext_vector_type(4))) unsigned u32x4;
typedef __attribute__((ext_vector_type(2))) unsigned u32x2;
typedef __attribute__((ext_vector_type(4))) short s16x4;
typedef __amdgpu_buffer_rsrc_t rsrc_t;
#define LAS __attribute__((address_space(3)))
typedef LAS char* lptr;
#define DI __device__ __forceinline__

constexpr unsigned WS_NONCE = 0x0;
constexpr unsigned WS_F0 = 0x1000;
constexpr unsigned WS_F5 = 0x2000;
constexpr unsigned WS_C1 = 0x10000;
constexpr unsigned WS_C2 = 0x20000;
constexpr unsigned WS_C3 = 0x28000;
constexpr unsigned WS_C4 = 0x2C000;
constexpr unsigned WS_C5 = 0x30000;
constexpr unsigned WS_C6 = 0x34000;
constexpr unsigned WS_ST = 0x40000;
constexpr unsigned WS_SP = 0x140000;
constexpr unsigned WS_KV = 0x200000;
constexpr unsigned WS_O = 0xA00000;
constexpr unsigned WS_H2 = 0xE00000;
constexpr unsigned WS_U = 0x1200000;
constexpr unsigned WS_X2 = 0x1600000;
constexpr unsigned WS_END = 0x1800000;
constexpr unsigned L_WIMG = 16384;
constexpr unsigned L_ONES = 131072;
constexpr unsigned L_CSP = 135168;
constexpr unsigned L_PROG = L_CSP + 1792;
constexpr unsigned L_FILL = L_PROG + 16;
constexpr unsigned L_DONE = L_PROG + 32;
constexpr unsigned L_SST = L_CSP + 2048;
constexpr unsigned L_REG = 139264;
constexpr unsigned L_SCR = 147456;
constexpr unsigned L_TOTAL = 163840;
constexpr int AUX_SC1 = 16;
constexpr unsigned OOB = 0x80000000u;
constexpr unsigned SPIN_CAP = 1u << 16;
constexpr float LN_EPS = 1e-5f;

#define SBAR() __builtin_amdgcn_sched_barrier(0)
#define PIN(x) asm volatile("" : "+v"(x))
#define MFMA(a, b, c) __builtin_amdgcn_mfma_f32_32x32x16_bf16(a, b, c, 0, 0, 0)
#define EX(x) __builtin_amdgcn_exp2f(x)
#define VMWAIT(n) asm volatile("s_waitcnt vmcnt(" #n ")" ::: "memory")
#define BAR() asm volatile("s_waitcnt lgkmcnt(0)\n\ts_barrier" ::: "memory")

template <int N> DI void vm_wait() { asm volatile("s_waitcnt vmcnt(%0)" ::"i"(N) : "memory"); }
template <int N> DI void vm_wait_bar() { asm volatile("s_waitcnt vmcnt(%0) lgkmcnt(0)\n\ts_barrier" ::"i"(N) : "memory"); }
DI rsrc_t mk_rsrc(const void* p, unsigned bytes) { asm volatile("" : "+s"(p)); return __builtin_amdgcn_make_buffer_rsrc((void*)p, 0, bytes, 0x00020000); }
template <int N, int LSTEP, int GSTEP> DI void dma(unsigned lds, unsigned voff, rsrc_t rs, unsigned soff) {
    unsigned t;
    asm volatile("s_mov_b32 m0, %1\n\ts_mov_b32 %0, %4\n\ts_nop 2\n\tbuffer_load_dwordx4 %2, %3, %0 offen lds\n\t"
                 ".rept %5\n\ts_add_u32 m0, m0, %6\n\ts_add_u32 %0, %0, %7\n\tbuffer_load_dwordx4 %2, %3, %0 offen lds\n\t.endr"
                 : "=&s"(t) : "s"(lds), "v"(voff), "s"(rs), "s"(soff), "i"(N - 1), "i"(LSTEP), "i"(GSTEP) : "memory", "scc");
}
template <int N, int LSTEP, int GSTEP> DI void dma_sc1(unsigned lds, unsigned voff, rsrc_t rs, unsigned soff) {
    unsigned t;
    asm volatile("s_mov_b32 m0, %1\n\ts_mov_b32 %0, %4\n\ts_nop 2\n\tbuffer_load_dwordx4 %2, %3, %0 offen sc1 lds\n\t"
                 ".rept %5\n\ts_add_u32 m0, m0, %6\n\ts_add_u32 %0, %0, %7\n\tbuffer_load_dwordx4 %2, %3, %0 offen sc1 lds\n\t.endr"
                 : "=&s"(t) : "s"(lds), "v"(voff), "s"(rs), "s"(soff), "i"(N - 1), "i"(LSTEP), "i"(GSTEP) : "memory", "scc");
}
template <int N, int GSTEP> DI void touch(unsigned pad, unsigned voff, rsrc_t rs, unsigned soff) {
    unsigned t;
    asm volatile("s_mov_b32 m0, %1\n\ts_mov_b32 %0, %4\n\ts_nop 2\n\tbuffer_load_dword %2, %3, %0 offen sc1 lds\n\t"
                 ".rept %5\n\ts_add_u32 %0, %0, %6\n\tbuffer_load_dword %2, %3, %0 offen sc1 lds\n\t.endr"
                 : "=&s"(t) : "s"(pad), "v"(voff), "s"(rs), "s"(soff), "i"(N - 1), "i"(GSTEP) : "memory", "scc");
}
DI bf16x8 ld_frag(rsrc_t rs, unsigned voff, unsigned soff) { return __builtin_bit_cast(bf16x8, __builtin_amdgcn_raw_buffer_load_b128(rs, voff, soff, AUX_SC1)); }
DI void st_frag(u32x4 v, rsrc_t rs, unsigned voff, unsigned soff) {
    __builtin_amdgcn_raw_buffer_store_b128(v, rs, voff, soff, AUX_SC1);
    asm volatile("s_nop 1" ::"v"(v) : "memory");
}
DI bf16x8 lds_frag(lptr p) { return *(LAS const bf16x8*)p; }
DI s16x4 lds_tr(lptr p) { return __builtin_amdgcn_ds_read_tr16_b64_v4i16((LAS s16x4*)p); }
DI bf16x8 tr_frag(lptr p0, lptr p1) {
    const s16x4 a = lds_tr(p0), b = lds_tr(p1);
    return __builtin_bit_cast(bf16x8, __builtin_shufflevector(a, b, 0, 1, 2, 3, 4, 5, 6, 7));
}
DI unsigned pk2(float a, float b) { const f32x2 v = {a, b}; return __builtin_bit_cast(unsigned, __builtin_convertvector(v, bf16x2)); }
DI float bf_lo(unsigned u) { return __uint_as_float(u << 16); }
DI float bf_hi(unsigned u) { return __uint_as_float(u & 0xffff0000u); }
DI float half_sum2(float a, float b) { const auto r = __builtin_amdgcn_permlane32_swap(__float_as_uint(a), __float_as_uint(b), false, false); return __uint_as_float(r[0]) + __uint_as_float(r[1]); }
DI f32x8 lo8(f32x16 v) { return __builtin_shufflevector(v, v, 0, 1, 2, 3, 4, 5, 6, 7); }
DI f32x8 hi8(f32x16 v) { return __builtin_shufflevector(v, v, 8, 9, 10, 11, 12, 13, 14, 15); }
DI f32x16 smf(bf16x8 a, f32x8 p, f32x16 c, int idx) { return __builtin_amdgcn_smfmac_f32_32x32x32_bf16(a, __builtin_bit_cast(bf16x16, p), c, idx, 0, 0); }
DI int opaque(int v) { asm volatile("" : "+v"(v)); return v; }
DI int sopaque(int v) { asm volatile("" : "+s"(v)); return v; }

DI void wait_cnt(rsrc_t ws, unsigned soff, unsigned target, int lane) {
#pragma unroll 1
    for (unsigned i = 0; i < SPIN_CAP; ++i) {
        const unsigned v = __builtin_amdgcn_raw_ptr_buffer_atomic_add_i32(0, ws, lane == 0 ? 0u : OOB, soff, AUX_SC1);
        if ((unsigned)__builtin_amdgcn_readfirstlane(v) >= target) break;
        asm volatile("" ::: "memory");
    }
    asm volatile("" ::: "memory");
}
template <int NREP> DI void arrive(rsrc_t ws, unsigned soff, int lane) {
    __builtin_amdgcn_raw_ptr_buffer_atomic_add_i32(1, ws, lane < NREP ? lane * 128u : OOB, soff, AUX_SC1);
    asm volatile("" ::: "memory");
}
template <int NREP> DI void publish(rsrc_t ws, unsigned soff, int lane) { VMWAIT(0); arrive<NREP>(ws, soff, lane); }
DI void wait64(rsrc_t ws, unsigned voff, unsigned soff, unsigned tlo, unsigned thi) {
#pragma unroll 1
    for (unsigned i = 0; i < SPIN_CAP; ++i) {
        const u32x2 v = __builtin_amdgcn_raw_buffer_load_b64(ws, voff, soff, AUX_SC1);
        if (__all(((v[0] ^ tlo) | (v[1] ^ thi)) == 0)) break;
        asm volatile("" ::: "memory");
    }
    asm volatile("" ::: "memory");
}

DI unsigned wvoff(int lane, unsigned ldw) { return (lane >> 3) * ldw + (((lane & 7) ^ (((lane >> 4) & 1) << 2)) << 4); }
DI void lds_post(lptr p, unsigned v) { *(volatile LAS unsigned*)p = v; }
DI unsigned lds_min2(lptr p) {
    const u32x2 v = *(volatile LAS u32x2*)p;
    const unsigned a = __builtin_amdgcn_readfirstlane(v[0]), b = __builtin_amdgcn_readfirstlane(v[1]);
    return a < b ? a : b;
}
template <int SLEEP> DI void lds_need(lptr p, unsigned& have, unsigned need) {
#pragma unroll 1
    for (unsigned i = 0; have < need && i < (SPIN_CAP << 4); ++i) { have = lds_min2(p); if (SLEEP && have < need) __builtin_amdgcn_s_sleep(SLEEP); }
    asm volatile("" ::: "memory");
}
template <int S0, int S1, int SLOT0, int LDW, int TS> DI void ring_prefetch(unsigned lb, u32x4 (&rg)[8][4], rsrc_t rw, unsigned voff, unsigned gb, lptr fill, unsigned base) {
#pragma unroll
    for (int s = S0; s < S1; ++s) {
        const unsigned so = gb + s * (64 * LDW);
#pragma unroll
        for (int h = 0; h < 2; ++h) {
            if (s < 8) dma<2, 1024, 8 * LDW>(lb + ((s + SLOT0) & 7) * 16384 + h * 2048, voff, rw, so + h * 16 * LDW);
            else {
#pragma unroll
                for (int p = 2 * h; p < 2 * h + 2; ++p)
                    asm volatile("s_nop 4\n\tbuffer_load_dwordx4 %0, %1, %2, %3 offen" : "=v"(rg[s & 7][p]) : "v"(voff), "s"(rw), "s"(so + p * 8 * LDW) : "memory");
            }
            if (TS > 0) vm_wait<4 * TS>();
        }
        if (TS > 0 && s >= TS && s < 8 + TS) lds_post(fill, base + s + 1 - TS);
    }
}
template <int NST, int SLOT0, int TS> DI void ring_refill(lptr lp, u32x4 (&rg)[8][4], lptr prog, lptr fill, unsigned base) {
    unsigned have = 0;
#pragma unroll
    for (int s = 8; s < NST; ++s) {
        lds_need<1>(prog, have, base + s - 7);
        if (s == NST - TS) asm volatile("s_waitcnt vmcnt(0)" : "+v"(rg[s & 7][0]), "+v"(rg[s & 7][1]), "+v"(rg[s & 7][2]), "+v"(rg[s & 7][3]) :: "memory");
        else asm volatile("" : "+v"(rg[s & 7][0]), "+v"(rg[s & 7][1]), "+v"(rg[s & 7][2]), "+v"(rg[s & 7][3]) :: "memory");
#pragma unroll
        for (int p = 0; p < 4; ++p) *(LAS u32x4*)(lp + ((s + SLOT0) & 7) * 16384 + p * 1024) = rg[s & 7][p];
        asm volatile("" ::: "memory");
        lds_post(fill, base + s + 1);
    }
}
template <int NST, int SLOT0, bool ACT_A, bool RING>
DI void gemm_compute(lptr lds, int lane, int kh, int ah, rsrc_t ws, unsigned act_soff, unsigned base, lptr done, unsigned done_need, f32x16& acc0, f32x16& acc1) {
    constexpr int NK = NST * 4, LA = 2, AN0 = 6, APD = 12;
    const unsigned av = lane * 16;
    bf16x8 f[NK];
    s16x4 wh[NK][4];
#define ALOAD(i) f[i] = ld_frag(ws, av + ((i) & 3) * 1024, act_soff + ((i) >> 2) * 4096)
#pragma unroll
    for (int i = 0; i < AN0; ++i) ALOAD(i);
    SBAR();
    const int hh = lane >> 5, gq = (lane >> 4) & 1, q = (lane >> 2) & 3, p = lane & 3;
    const unsigned a0 = kh * 8192 + (4 * hh + q) * 128 + (((4 * kh + 2 * gq + (p >> 1)) ^ (((q >> 1) & 1) << 2)) << 4) + (p & 1) * 8;
    const lptr b0 = lds + a0, b1 = lds + (a0 ^ 64), b0h = b0 + 65536, b1h = b1 + 65536;
    const lptr fill = lds + L_FILL + kh * 8, prog = lds + L_PROG + kh * 8 + ah * 4;
    unsigned have = 0;
#define WREAD(k, j) do { const int sl_ = (((k) >> 2) + SLOT0) & 7, o_ = (sl_ & 3) * 16384 + ((k) & 3) * 2048 + ((j) & 1) * 1024; \
        wh[k][j] = lds_tr((((j) >> 1) ? ((sl_ & 4) ? b0h : b0) : ((sl_ & 4) ? b1h : b1)) + o_); } while (0)
#define WFRAG(k, t) __builtin_bit_cast(bf16x8, __builtin_shufflevector(wh[k][2 * (t)], wh[k][2 * (t) + 1], 0, 1, 2, 3, 4, 5, 6, 7))
    acc1 = f32x16{};
    if (RING) lds_need<0>(fill, have, base + 1);
    unsigned dv = 0;
#pragma unroll
    for (int k = 0; k < LA; ++k) { WREAD(k, 0); WREAD(k, 1); WREAD(k, 2); WREAD(k, 3); }
    int nl = AN0;
#pragma unroll
    for (int k = 0; k < NK; ++k) {
        if (RING && ((k + LA) & 3) == 0 && k + LA < NK) lds_need<0>(fill, have, base + ((k + LA) >> 2) + 1);
        SBAR();
        if (k + 1 < NK) { __builtin_amdgcn_s_waitcnt(0xC07F | (4 << 8)); SBAR(); }
        acc1 = ACT_A ? MFMA(f[k], WFRAG(k, 0), acc1) : MFMA(WFRAG(k, 0), f[k], acc1);
        SBAR();
        if (k + LA < NK) { WREAD(k + LA, 0); WREAD(k + LA, 1); WREAD(k + LA, 2); }
        if (nl < NK && nl < k + APD) { ALOAD(nl); ++nl; }
        SBAR();
        acc0 = ACT_A ? MFMA(f[k], WFRAG(k, 1), acc0) : MFMA(WFRAG(k, 1), f[k], acc0);
        SBAR();
        if (RING && (k & 3) == 3 && (k >> 2) + 8 < NST) lds_post(prog, base + (k >> 2) + 1);
        if (k + LA < NK) WREAD(k + LA, 3);
        if (k == NK - LA) dv = *(volatile LAS unsigned*)done;
        SBAR();
        if (nl < NK && nl < k + 1 + APD) { ALOAD(nl); ++nl; }
    }
    SBAR();
        PIN(acc1); PIN(acc0);
#pragma unroll 1
    for (unsigned i = 0; !__all(dv >= done_need) && i < (SPIN_CAP << 4); ++i) dv = *(volatile LAS unsigned*)done;
    asm volatile("" ::: "memory");
#undef WFRAG
#undef WREAD
#undef ALOAD
}
DI void exchange(lptr lds, int lane, int w, int partner, f32x16& acc0, const f32x16& acc1) {
    LAS f32x4* dst = (LAS f32x4*)(lds + L_SCR + w * 4096 + lane * 16);
#pragma unroll
    for (int i = 0; i < 4; ++i) dst[i * 64] = f32x4{acc1[4 * i], acc1[4 * i + 1], acc1[4 * i + 2], acc1[4 * i + 3]};
    BAR();
    LAS const f32x4* src = (LAS const f32x4*)(lds + L_SCR + partner * 4096 + lane * 16);
#pragma unroll
    for (int i = 0; i < 4; ++i) { const f32x4 v = src[i * 64]; acc0[4 * i] += v[0]; acc0[4 * i + 1] += v[1]; acc0[4 * i + 2] += v[2]; acc0[4 * i + 3] += v[3]; }
}

#define MFMA16(a, b, c) __builtin_amdgcn_mfma_f32_16x16x32_bf16(a, b, c, 0, 0, 0)
struct QkvAcc { f32x4 q[2], k[2], v[2], cq, ck, cv; };
struct WFrag { s16x4 h[3][2]; };
DI bf16x8 wpart(const WFrag& f, int i) { return __builtin_bit_cast(bf16x8, __builtin_shufflevector(f.h[i][0], f.h[i][1], 0, 1, 2, 3, 4, 5, 6, 7)); }
DI void qkv_wread(WFrag& n, lptr p0, lptr p1, int o) {
#pragma unroll
    for (int i = 0; i < 3; ++i) { n.h[i][0] = lds_tr(p0 + o + i * 128); n.h[i][1] = lds_tr(p1 + o + i * 128); }
}
DI void qkv_step(QkvAcc& a, bf16x8 x0, bf16x8 x1, const WFrag& f, WFrag& n, bool more, lptr p0, lptr p1, int o) {
    const bf16x8 wq = wpart(f, 0), wk = wpart(f, 1), wv = wpart(f, 2);
    SBAR(); a.q[0] = MFMA16(wq, x0, a.q[0]); SBAR(); if (more) n.h[0][0] = lds_tr(p0 + o);
    SBAR(); a.q[1] = MFMA16(wq, x1, a.q[1]); SBAR(); if (more) n.h[0][1] = lds_tr(p1 + o);
    SBAR(); a.k[0] = MFMA16(wk, x0, a.k[0]); SBAR(); if (more) n.h[1][0] = lds_tr(p0 + o + 128);
    SBAR(); a.k[1] = MFMA16(wk, x1, a.k[1]); SBAR(); if (more) n.h[1][1] = lds_tr(p1 + o + 128);
    SBAR(); a.v[0] = MFMA16(x0, wv, a.v[0]); SBAR(); if (more) n.h[2][0] = lds_tr(p0 + o + 256);
    SBAR(); a.v[1] = MFMA16(x1, wv, a.v[1]); SBAR(); if (more) n.h[2][1] = lds_tr(p1 + o + 256);
    SBAR();
}
DI u32x4 join16(f32x4 a, f32x4 b) {
    const auto s0 = __builtin_amdgcn_permlane16_swap(pk2(a[0], a[1]), pk2(b[0], b[1]), false, false);
    const auto s1 = __builtin_amdgcn_permlane16_swap(pk2(a[2], a[3]), pk2(b[2], b[3]), false, false);
    return u32x4{s0[0], s1[0], s0[1], s1[1]};
}
DI u32x2 row_partial(const f32x16& x) {
    f32x2 s0 = {x[0], x[1]}, s1 = {x[2], x[3]}, q0 = s0 * s0, q1 = s1 * s1;
#pragma unroll
    for (int i = 4; i < 16; i += 4) {
        const f32x2 a = {x[i], x[i + 1]}, b = {x[i + 2], x[i + 3]};
        s0 += a; s1 += b; q0 = __builtin_elementwise_fma(a, a, q0); q1 = __builtin_elementwise_fma(b, b, q1);
    }
    s0 += s1; q0 += q1;
    const unsigned t = __float_as_uint(half_sum2(s0[0] + s0[1], q0[0] + q0[1]));
    const auto sw = __builtin_amdgcn_permlane32_swap(t, t, false, false);
    return u32x2{sw[0], sw[1]};
}
DI f32x16 unpack16(const unsigned (&x)[8]) {
    f32x16 v;
#pragma unroll
    for (int i = 0; i < 8; ++i) { v[2 * i] = bf_lo(x[i]); v[2 * i + 1] = bf_hi(x[i]); }
    return v;
}
DI f32x4 record_total(const u32x4 (&p)[4]) {
    f32x4 t;
#pragma unroll
    for (int j = 0; j < 4; ++j) {
        const unsigned v = __float_as_uint((__uint_as_float(p[0][j]) + __uint_as_float(p[1][j])) + (__uint_as_float(p[2][j]) + __uint_as_float(p[3][j])));
        const auto a = __builtin_amdgcn_permlane32_swap(v, v, false, false);
        const unsigned u = __float_as_uint(__uint_as_float(a[0]) + __uint_as_float(a[1]));
        const auto b = __builtin_amdgcn_permlane16_swap(u, u, false, false);
        t[j] = __uint_as_float(b[0]) + __uint_as_float(b[1]);
    }
    return t;
}
#define XE(r) "v_exp_f32 v" #r ", v" #r "\n\t"
#define SMF(d, a, b) "v_smfmac_f32_32x32x32_bf16 v[" d "], v[" a "], v[" b "], v173\n\t"
#define ATT_HEAD XE(66) XE(67) XE(68) XE(69) "v_mfma_f32_32x32x16_bf16 v[80:95], v[160:163], v[156:159], v[0:15]\n\t"
#define ATT_FULL(C0, C1, N0, N1) ATT_HEAD \
    "ds_read_b128 v[160:163], %[kp] offset:%[ko]\n\t" XE(70) XE(71) "ds_read_b128 v[" N0 "], %[vp] offset:%[vo]\n\t" XE(72) XE(73) SMF("32:47", C0, "64:71") \
    "ds_read_b128 v[" N1 "], %[vp] offset:%[vo2]\n\t" XE(74) XE(75) XE(76) XE(77) XE(78) XE(79) XE(80) XE(81) SMF("32:47", C1, "72:79") \
    XE(82) XE(83) XE(84) "s_waitcnt lgkmcnt(0)\n\tv_mfma_f32_32x32x16_bf16 v[64:79], v[160:163], v[152:155], v[48:63]\n\t" \
    XE(85) XE(86) XE(87) XE(88) XE(89) SMF("16:31", C0, "80:87") \
    XE(90) XE(91) XE(92) XE(93) XE(94) XE(95) XE(64) XE(65) SMF("16:31", C1, "88:95")
#define ATT_LAST(C0, C1) ATT_HEAD \
    XE(70) XE(71) XE(72) XE(73) SMF("32:47", C0, "64:71") XE(74) XE(75) XE(76) XE(77) XE(78) XE(79) "s_nop 0\n\t" XE(80) XE(81) SMF("32:47", C1, "72:79") \
    XE(82) XE(83) XE(84) XE(85) XE(86) XE(87) XE(88) XE(89) SMF("16:31", C0, "80:87") \
    XE(90) XE(91) XE(92) XE(93) XE(94) XE(95) "s_nop 1\n\t" SMF("16:31", C1, "88:95") "s_nop 11"
#define ATT_OPS(KO, VO) \
    : "+{v[64:79]}"(sA), "+{v[80:95]}"(sB), "+{v[32:47]}"(o), "+{v[16:31]}"(o2), "+{v[160:163]}"(kf), \
      "+{v[164:167]}"(v0), "+{v[168:171]}"(v1), "+{v[176:179]}"(v2), "+{v[180:183]}"(v3) \
    : "{v[48:63]}"(cneg), "{v[0:15]}"(cneg2), "{v[152:155]}"(qf), "{v[156:159]}"(qf2), "{v173}"(idx), \
      [kp] "v"(kp), [vp] "v"(vp), [ko] "i"(KO), [vo] "i"(VO), [vo2] "i"((VO) + 512) : "memory"
#define ATT_EVEN(KO, VO) asm volatile(ATT_FULL("164:167", "168:171", "176:179", "180:183") ATT_OPS(KO, VO))
#define ATT_ODD(KO, VO) asm volatile(ATT_FULL("176:179", "180:183", "164:167", "168:171") ATT_OPS(KO, VO))
#define ATT_ODD_LAST() asm volatile(ATT_LAST("176:179", "180:183") ATT_OPS(0, 0))
DI float neg_row_max(f32x16 s) {
    float m0 = __builtin_fmaxf(s[0], s[1]), m1 = __builtin_fmaxf(s[2], s[3]);
#pragma unroll
    for (int i = 4; i < 16; i += 4) { m0 = __builtin_fmaxf(__builtin_fmaxf(m0, s[i]), s[i + 1]); m1 = __builtin_fmaxf(__builtin_fmaxf(m1, s[i + 2]), s[i + 3]); }
    const float m = __builtin_fmaxf(m0, m1);
    const auto sw = __builtin_amdgcn_permlane32_swap(__float_as_uint(m), __float_as_uint(m), false, false);
    return -__builtin_fmaxf(__uint_as_float(sw[0]), __uint_as_float(sw[1]));
}
DI f32x16 splat16(float m) {
    f32x2 m2 = {m, m};
    PIN(m2);
    f32x16 c;
#pragma unroll
    for (int i = 0; i < 8; ++i) { f32x2 t; asm volatile("v_mov_b64 %0, %1" : "=v"(t) : "v"(m2)); c[2 * i] = t[0]; c[2 * i + 1] = t[1]; }
    PIN(c);
    return c;
}

__global__ __launch_bounds__(512, 2) void fused_fwd(const void* __restrict__ xin, const void* __restrict__ wqkv, const void* __restrict__ wo,
                                                    const void* __restrict__ w1, const unsigned short* __restrict__ b1, const void* __restrict__ w2,
                                                    const unsigned short* __restrict__ b2, unsigned short* __restrict__ out, char* wsp) {
    __shared__ __attribute__((aligned(16))) char lds_raw[L_TOTAL];
    const lptr lds = (lptr)lds_raw;
    const unsigned lds0 = (unsigned)(uintptr_t)lds;
    const int w_ = __builtin_amdgcn_readfirstlane(threadIdx.x >> 6);
#define PHASE_IDS \
    const int lane = opaque(threadIdx.x & 63), l31 = lane & 31, hh = lane >> 5; \
    const int w = sopaque(w_), bid = sopaque(blockIdx.x), g = bid & 7, r = bid >> 3, hd = (r + 12) & 31; \
    const int st64 = g * 4 + (r >> 3), dt64 = r & 7; \
    const int dt1 = (g & 1) * 4 + (r & 3), ft1 = (g >> 1) * 7 + (r >> 2); \
    const int own_s32 = st64 * 2 + (w & 1), own_d32 = dt64 * 2 + ((w >> 1) & 1); \
    (void)l31; (void)hh; (void)hd; (void)dt1; (void)ft1; (void)own_s32; (void)own_d32;
    const bool w1_on = (blockIdx.x >> 3) < 28;
    const rsrc_t ws = mk_rsrc(wsp, WS_END);

    unsigned xres[8] = {};
    unsigned tag_lo = 0, tag_hi = 0;
    u32x2 nonce = {};
    u32x4 wc[12];
#define UNDEF_WC() do { _Pragma("unroll") for (int i_ = 0; i_ < 12; ++i_) asm volatile("" : "=v"(wc[i_])); } while (0)
    UNDEF_WC();

#pragma unroll 1
    for (int L = 0; L < 2; ++L) {
        bf16x8 qf;
        float nm;
        {
            PHASE_IDS
            const int gq = (lane >> 4) & 1, q = (lane >> 2) & 3, p = lane & 3, l15 = lane & 15, kg = lane >> 4;
            const unsigned kv_soff = WS_KV + (L * 32 + hd) * 131072;
            QkvAcc a = {};
            u32x4 sp[4];
            float rt[2], mt[2];
            f32x4 rs[2], ms[2];
            asm volatile("" : "=v"(rt[0]), "=v"(rt[1]), "=v"(mt[0]), "=v"(mt[1]), "=v"(rs[0]), "=v"(rs[1]), "=v"(ms[0]), "=v"(ms[1]));
#define LN_PREP() do { \
                const f32x4 tot = record_total(sp); \
                const float m0 = tot[0] * (1.f / 512.f), m1 = tot[2] * (1.f / 512.f); \
                const float r0 = __builtin_amdgcn_rsqf(__builtin_fmaxf(tot[1] * (1.f / 512.f) - m0 * m0, 0.f) + LN_EPS); \
                const float r1 = __builtin_amdgcn_rsqf(__builtin_fmaxf(tot[3] * (1.f / 512.f) - m1 * m1, 0.f) + LN_EPS); \
                const lptr sst = lds + L_SST + w * 256; \
                *(LAS f32x2*)(sst + hh * 128 + l15 * 8) = hh ? f32x2{m0 * r0, m1 * r1} : f32x2{r0, r1}; \
                _Pragma("unroll") for (int t = 0; t < 2; ++t) { \
                    rt[t] = *(LAS const float*)(sst + t * 64 + l15 * 4); mt[t] = *(LAS const float*)(sst + 128 + t * 64 + l15 * 4); \
                    rs[t] = *(LAS const f32x4*)(sst + t * 64 + kg * 16); ms[t] = *(LAS const f32x4*)(sst + 128 + t * 64 + kg * 16); } \
                SBAR(); } while (0)
#define CS_READ() do { a.cq = *(LAS const f32x4*)(lds + L_CSP + kg * 16); a.ck = *(LAS const f32x4*)(lds + L_CSP + 64 + kg * 16); \
                a.cv[0] = *(LAS const float*)(lds + L_CSP + 128 + l15 * 4); } while (0)
            const unsigned sp_soff = WS_SP + (L * 64 + g * 8 + w) * 4096;
            if (L == 0) {
                const rsrc_t rx = mk_rsrc(xin, 2048 * 1024), rw = mk_rsrc(wqkv, 512 * 3072);
                unsigned wv[3];
                const int rho = (hd >> 2) & 3;
#define WQUART(j) do { if (w >= 4) { const int m_ = 4 * (((j) + rho) & 3) + w - 4; _Pragma("unroll") for (int ty = 0; ty < 3; ++ty) \
                       dma<1, 0, 0>(lds0 + L_WIMG + m_ * 3072 + ty * 1024, wv[ty], rw, m_ * 98304 + hd * 32); } } while (0)
                const unsigned tokb = (g * 256 + w * 32) * 1024, stg = w < 5 ? 65536 + w * 12288 : (w == 5 ? 0 : L_REG + (w - 6) * 12288);
                const unsigned vA = (lane >> 3) * 1024 + (((lane & 7) ^ (lane >> 4)) << 4), vB = vA ^ 64;
#define XCHUNK(c) do { dma<2, 2048, 16384>(lds0 + stg + ((c) % 3) * 4096, vA, rx, tokb + (((c) + 2 * rho) & 7) * 128); \
                       dma<2, 2048, 16384>(lds0 + stg + ((c) % 3) * 4096 + 1024, vB, rx, tokb + 8192 + (((c) + 2 * rho) & 7) * 128); } while (0)
#define ROLE_WAIT(a, b) do { if (w >= 4) vm_wait<a>(); else vm_wait<b>(); } while (0)
                u32x2 xr[4];
                XCHUNK(0);
#pragma unroll
                for (int ty = 0; ty < 3; ++ty) {
                    const unsigned u = ty * 64 + lane, rg = __umul24(u, 171) >> 12, rem = u - __umul24(rg, 24);
                    wv[ty] = __umul24(4 * (rg ^ ((rg >> 1) & 1)) + ((rem >> 1) & 3), 3072) + (rem >> 3) * 1024 + (rem & 1) * 16;
                }
                WQUART(0);
                asm volatile("s_nop 4\n\tbuffer_load_dwordx2 %0, off, %1, 0 sc1" : "=v"(nonce) : "s"(ws) : "memory");
                if (w < 4) {
                    asm volatile("s_nop 4\n\tbuffer_load_dwordx2 %0, %4, %5, %6 offen\n\tbuffer_load_dwordx2 %1, %4, %5, %6 offen offset:16\n\t"
                                 "buffer_load_dwordx2 %2, %4, %5, %6 offen offset:32\n\tbuffer_load_dwordx2 %3, %4, %5, %6 offen offset:48"
                                 : "=&v"(xr[0]), "=&v"(xr[1]), "=&v"(xr[2]), "=&v"(xr[3]) : "v"(l31 * 1024 + hh * 8), "s"(rx), "s"(own_s32 * 32768 + own_d32 * 64) : "memory");
                } else {
#pragma unroll
                    for (int i = 0; i < 4; ++i) asm volatile("" : "=v"(xr[i]));
                }
                XCHUNK(1); WQUART(1); XCHUNK(2);
                const lptr p0 = lds + L_WIMG + (4 * hh + 3 * gq) * 384 + q * 32 + p * 8, p1 = lds + L_WIMG + (4 * hh + 1 + gq) * 384 + q * 32 + p * 8;
                lptr px[4];
#pragma unroll
                for (int t = 0; t < 4; ++t) px[t] = lds + stg + ((t & 1) * 16 + l15) * 128 + (((4 * (t >> 1) + kg) ^ (l15 >> 1)) << 4);
                WFrag wf[2];
                const bf16x8 ones = __builtin_bit_cast(bf16x8, u32x4{0x3f803f80u, 0x3f803f80u, 0x3f803f80u, 0x3f803f80u});
                u32x2 fl;
                const unsigned f0r = opaque(WS_F0 + hd * 128 + (lane & 7) * 8), f5r = opaque(WS_F5 + (g * 8 + w) * 128 + (lane & 15) * 8), flr = hh ? f0r : f5r;
#pragma unroll
                for (int c = 0; c < 8; ++c) {
                    if (c <= 5) ROLE_WAIT(11, 8); else if (c == 6) vm_wait<0>();
                    if (c == 0) { BAR(); qkv_wread(wf[0], p0 + rho * 12288, p1 + rho * 12288, 0); }
                    bf16x8 xf[4];
#pragma unroll
                    for (int t = 0; t < 4; ++t) xf[t] = lds_frag(px[t] + (c % 3) * 4096);
                    if (c == 5) asm volatile("s_nop 4\n\tbuffer_load_dwordx2 %0, %1, %2, 0 offen sc1" : "=v"(fl) : "v"(flr), "s"(ws) : "memory");
                    if (c + 3 < 8) { asm volatile("s_waitcnt lgkmcnt(0)" : "+v"(xf[0]), "+v"(xf[1]), "+v"(xf[2]), "+v"(xf[3]) :: "memory"); XCHUNK(c + 3); }
                    if (c == 1) WQUART(2);
                    if (c == 3) WQUART(3);
                    if (c == 0) {
                        asm volatile("" : "+v"(nonce), "+v"(xr[0]), "+v"(xr[1]), "+v"(xr[2]), "+v"(xr[3]));
                        unsigned long long z = ((unsigned long long)__builtin_amdgcn_readfirstlane(nonce[1]) << 32) | (unsigned)__builtin_amdgcn_readfirstlane(nonce[0]);
                        z = z * 0x9E3779B97F4A7C15ull + 0x632BE59BD9B4E019ull;
                        z ^= z >> 29; z *= 0xBF58476D1CE4E5B9ull; z ^= z >> 32;
                        tag_lo = (unsigned)z; tag_hi = (unsigned)(z >> 32);
#pragma unroll
                        for (int i = 0; i < 4; ++i) { xres[2 * i] = xr[i][0]; xres[2 * i + 1] = xr[i][1]; }
                        if (w < 4) {
                            __builtin_amdgcn_raw_buffer_store_b64(row_partial(unpack16(xres)), ws, hh ? OOB : l31 * 8u, WS_SP + (own_s32 * 16 + own_d32) * 256, AUX_SC1);
                            __builtin_amdgcn_raw_buffer_store_b64(u32x2{}, ws, l31 * 8 + hh * 0x40000, WS_ST + (own_s32 * 16 + own_d32) * 256, AUX_SC1);
                            const unsigned c1a = opaque(WS_C1 + (hd * 8 + g) * 128 + lane * 32768), c3a = opaque(WS_C3 + bid * 256 + (lane - 3) * 128);
                            const unsigned c2a = sopaque(WS_C2 + (((r >> 4) * 64 + g * 8) * 2 + (r & 15)) * 128);
                            const unsigned ca = lane < 2 ? c1a : (lane == 2 ? c2a : c3a);
                            __builtin_amdgcn_raw_buffer_store_b32(0u, ws, (w == 0 && lane < 5) ? ca : OOB, 0, AUX_SC1);
                        }
                    }
                    if (c == 6) {
                        asm volatile("" : "+v"(fl));
                        if (!__all(((fl[0] ^ tag_lo) | (fl[1] ^ tag_hi)) == 0)) wait64(ws, flr, 0, tag_lo, tag_hi);
                        asm volatile("" ::: "memory");
#pragma unroll
                        for (int i = 0; i < 4; ++i) sp[i] = __builtin_amdgcn_raw_buffer_load_b128(ws, lane * 16 + i * 1024, sp_soff, AUX_SC1);
                        SBAR();
                    }
                    if (c == 6) {
                        f32x4 c3[3] = {};
#pragma unroll
                        for (int j = 0; j < 2; ++j) {
                            WFrag e;
                            qkv_wread(e, p0 + w * 6144, p1 + w * 6144, j * 3072);
#pragma unroll
                            for (int i = 0; i < 3; ++i) c3[i] = MFMA16(ones, wpart(e, i), c3[i]);
                        }
                        *(LAS float*)(lds + L_CSP + 256 + ((kg < 3 ? kg : 2) * 16 + l15) * 32 + w * 4) = kg == 0 ? c3[0][0] : (kg == 1 ? c3[1][0] : c3[2][0]);
                    }
                    if (c == 7) {
                        BAR();
                        if (w == 0) {
                            const int n = lane < 48 ? lane : lane - 16;
                            const f32x4 s0 = *(LAS const f32x4*)(lds + L_CSP + 256 + n * 32), s1 = *(LAS const f32x4*)(lds + L_CSP + 256 + n * 32 + 16);
                            *(LAS float*)(lds + L_CSP + n * 4) = ((s0[0] + s0[1]) + (s0[2] + s0[3])) + ((s1[0] + s1[1]) + (s1[2] + s1[3]));
                        }
                    }
                    if (c == 6) {
                        if (w >= 4) {
#pragma unroll
                            for (int i = 0; i < 12; ++i) wc[i] = *(LAS const u32x4*)(lds + L_WIMG + (w - 4) * 12288 + i * 1024 + lane * 16);
                        }
                    }
#pragma unroll
                    for (int t = 0; t < 2; ++t) {
                        const int k = 2 * c + t;
                        if (k == 3) { ROLE_WAIT(15, 63); BAR(); }
                        if (k == 7) { ROLE_WAIT(11, 63); BAR(); }
                        if (k == 11) { ROLE_WAIT(4, 63); BAR(); }
                        qkv_step(a, xf[2 * t], xf[2 * t + 1], wf[k & 1], wf[(k + 1) & 1], k < 15,
                                        p0 + ((((k + 1) >> 2) + rho) & 3) * 12288, p1 + ((((k + 1) >> 2) + rho) & 3) * 12288, ((k + 1) & 3) * 3072);
                        if ((k == 13 && w >= 4) || (k == 14 && w < 4)) LN_PREP();
                    }
                    if (c == 1 && w < 4) {
                        VMWAIT(0);
                        const unsigned f5a = sopaque(WS_F5 + own_s32 * 128 + own_d32 * 8), f0a = sopaque(w == 0 ? WS_F0 + hd * 128 + g * 8 : OOB);
                        __builtin_amdgcn_raw_buffer_store_b64(u32x2{tag_lo, tag_hi}, ws, lane == 0 ? f5a : (lane == 1 ? f0a : OOB), 0, AUX_SC1);
                    }
                }
                BAR();
                CS_READ();
#undef WQUART
#undef ROLE_WAIT
#undef XCHUNK
            } else {
                if (w >= 4) {
#pragma unroll
                    for (int i = 0; i < 12; ++i) *(LAS u32x4*)(lds + L_WIMG + (w - 4) * 12288 + i * 1024 + lane * 16) = wc[i];
                }
                BAR();
                wait_cnt(ws, WS_C5 + ((g * 8 + w) * 2 + (r & 1)) * 128, 16, lane);
                constexpr int XN0 = 6, XPD = 12;
                bf16x8 xf[32];
                const unsigned x_soff = WS_X2 + (g * 8 + w) * 32768;
#define XLOAD(k) xf[k] = ld_frag(ws, lane * 16 + ((k) & 3) * 1024, x_soff + ((k) >> 2) * 4096)
#pragma unroll
                for (int i = 0; i < 4; ++i) sp[i] = __builtin_amdgcn_raw_buffer_load_b128(ws, lane * 16 + i * 1024, sp_soff, AUX_SC1);
#pragma unroll
                for (int k = 0; k < XN0; ++k) XLOAD(k);
                SBAR();
                const lptr p0 = lds + L_WIMG + (4 * hh + gq) * 384 + q * 32 + p * 8, p1 = lds + L_WIMG + (4 * hh + 3 - gq) * 384 + q * 32 + p * 8;
                WFrag wf[3];
                qkv_wread(wf[0], p0, p1, 0);
                qkv_wread(wf[1], p0, p1, 3072);
                int nl = XN0;
                __builtin_amdgcn_s_setprio(3);
#pragma unroll
                for (int k = 0; k < 16; ++k) {
                    if (k < 15) { SBAR(); __builtin_amdgcn_s_waitcnt(0xC07F | (6 << 8)); }
                    qkv_step(a, xf[2 * k], xf[2 * k + 1], wf[k % 3], wf[(k + 2) % 3], k < 14, p0, p1, (k + 2) * 3072);
#pragma unroll
                    for (int i = 0; i < 3; ++i) if (nl < 32 && nl < 2 * k + 2 + XPD) { XLOAD(nl); ++nl; }
                    if (k == 5) __builtin_amdgcn_s_setprio(2);
                    if (k == 10) __builtin_amdgcn_s_setprio(1);
                    if (k == 13) __builtin_amdgcn_s_setprio(0);
                    if (k == 6 && w < 4) LN_PREP();
                    if (k == 2 && w >= 4) LN_PREP();
                    if (k == 12) CS_READ();
                }
#undef XLOAD
                UNDEF_WC();
            }
            __builtin_amdgcn_s_setprio(3);
            constexpr float QS = 0.25f * 1.4426950408889634f;
            const u32x4 kpk = join16(a.k[0] * rt[0] - mt[0] * a.ck, a.k[1] * rt[1] - mt[1] * a.ck);
            st_frag(kpk, ws, lane * 16, kv_soff + (g * 8 + w) * 2048);
            const u32x4 qpk = join16((a.q[0] * rt[0] - mt[0] * a.cq) * QS, (a.q[1] * rt[1] - mt[1] * a.cq) * QS);
            qf = __builtin_bit_cast(bf16x8, qpk);
            const f32x16 sd = MFMA(__builtin_bit_cast(bf16x8, kpk), qf, f32x16{});
            SBAR();
            const u32x4 vpk = join16(a.v[0] * rs[0] - ms[0] * a.cv[0], a.v[1] * rs[1] - ms[1] * a.cv[0]);
            const unsigned voff = gq * 512 + hh * 256 + l15 * 16;
            st_frag(vpk, ws, voff, kv_soff + (g * 8 + w) * 2048 + 1024);
            *(LAS u32x4*)(lds + w * 1024 + lane * 16) = kpk;
            *(LAS u32x4*)(lds + 8192 + w * 1024 + voff) = vpk;
            *(LAS u32x4*)(lds + L_REG + w * 3072 + lane * 16) = qpk;
            SBAR();
            nm = neg_row_max(sd);
            *(LAS float*)(lds + L_REG + w * 3072 + 1024 + lane * 4) = nm;
            if (L == 0) {
                if (w == 0) *(LAS unsigned*)(lds + L_PROG + (lane & 15) * 4) = 0u;
                const unsigned one = l15 ? 0u : 0x3f803f80u;
                *(LAS u32x4*)(lds + L_ONES + w * 512 + hh * 256 + l15 * 16) = u32x4{one, one, one, one};
            }
            BAR();
        }
        {
            PHASE_IDS
            const int gq = (lane >> 4) & 1;
            const unsigned kv_soff = WS_KV + (L * 32 + hd) * 131072;
            const int idx = opaque(0xDDDD);
            lptr kp = lds + (w & 1) * 4096 + lane * 16;
            lptr vp = lds + (gq ? L_ONES : 8192 + (w & 1) * 4096) + hh * 256 + (lane & 15) * 16;
            const int vinc = gq ? 0 : 16384;
            bf16x8 kf = lds_frag(kp);
            const float nm2 = *(LAS const float*)(lds + L_REG + (w ^ 1) * 3072 + 1024 + lane * 4);
            const bf16x8 qf2 = lds_frag(lds + L_REG + (w ^ 1) * 3072 + lane * 16);
            bf16x8 v0 = lds_frag(vp), v1 = lds_frag(vp + 512), v2, v3;
            f32x16 sA, sB;
            asm volatile("" : "=v"(v2), "=v"(v3), "=v"(sB));
            SBAR();
            const f32x16 cneg = splat16(nm);
#define KVCHUNK(j) dma_sc1<2, 8192, 1024>(lds0 + (j) * 16384 + w * 1024, lane * 16, ws, kv_soff + (((g + (j)) & 7) * 8 + w) * 2048)
            sA = MFMA(kf, qf, cneg);
            SBAR();
            f32x16 o = {}, o2 = {};
            asm volatile("" : "+{v[32:47]}"(o), "+{v[16:31]}"(o2));
            const f32x16 cneg2 = splat16(nm2);
            publish<1>(ws, WS_C1 + ((L * 32 + hd) * 8 + w) * 128, lane);
            sA[0] = EX(sA[0]); sA[1] = EX(sA[1]);
#define PRIO(n) __builtin_amdgcn_s_setprio(n)
            {
                const unsigned c1o = WS_C1 + ((L * 32 + hd) * 8 + w) * 128, pv0 = lane == 0 ? 0u : OOB;
                int pc = 0;
#define POLL_ISSUE() asm volatile("s_nop 4\n\tbuffer_atomic_add %0, %1, %2, %3 offen sc0 sc1" : "+v"(pc) : "v"(pv0), "s"(ws), "s"(c1o) : "memory")
#define POLL_READ() asm volatile("s_waitcnt vmcnt(0)" : "+v"(pc) :: "memory")
                PRIO(2);
                ATT_EVEN(1024, 1024);
                POLL_ISSUE();
                ATT_ODD(2048, 2048);
                POLL_READ();
                if (__builtin_amdgcn_readfirstlane(pc) < 8) wait_cnt(ws, c1o, 8, lane);
                KVCHUNK(1);
                PRIO(1);
                ATT_EVEN(3072, 3072);
                PRIO(0);
                ATT_ODD_LAST();
                vm_wait_bar<0>();
                kp += 16384; vp += vinc;
                kf = lds_frag(kp); v0 = lds_frag(vp); v1 = lds_frag(vp + 512);
                KVCHUNK(2);
                sA = MFMA(kf, qf, cneg);
                sA[0] = EX(sA[0]); sA[1] = EX(sA[1]);
            }
#pragma unroll
            for (int c = 1; c < 8; ++c) {
                if (c == 1 || c == 3 || c == 4 || c == 7) PRIO(2);
                ATT_EVEN(1024, 1024);
                if (c == 1) KVCHUNK(3);
                if (c == 2) KVCHUNK(5);
                if (c == 1 || c == 3 || c == 4) PRIO(1);
                ATT_ODD(2048, 2048);
                if (c == 2) KVCHUNK(6);
                if (c == 1 || c == 3 || c == 4) PRIO(0); else if (c == 7) PRIO(1);
                ATT_EVEN(3072, 3072);
                if (c == 1) { vm_wait_bar<0>(); KVCHUNK(4); }
                if (c == 2) KVCHUNK(7);
                if (c == 3) vm_wait_bar<0>();
                if (c == 4) {
                    BAR();
                    dma<8, 1024, 8192>(lds0 + w * 8192, wvoff(lane, 1024), mk_rsrc(wo, 512 * 1024), ((w & 1) * 256 + (w >> 1) * 64) * 1024 + dt64 * 128);
                }
                if (c == 5) touch<1, 0>(lds0 + L_SST + w * 256, lane * 3584, mk_rsrc(w1, 2048 * 3584), ((r & 3) * 512 + w * 64) * 3584 + (w1_on ? ft1 : 0) * 128);
                if (c == 2 && L == 0) touch<1, 0>(lds0 + L_SST + w * 256, (lane < 28 ? lane : 0) * 4096, mk_rsrc(w2, 1792 * 4096), (dt64 * 224 + w * 28) * 4096 + st64 * 128);
                if (c < 7) { if (c != 2 && c < 5) PRIO(3); vp += vinc; ATT_ODD(16384, 0); kp += 16384; }
                else { PRIO(0); ATT_ODD_LAST(); }
            }
            VMWAIT(0);
            {
                const lptr mine = lds + L_REG + w * 3072 + lane * 16, other = lds + L_REG + (w ^ 1) * 3072 + lane * 16;
                *(LAS f32x4*)mine = f32x4{o2[0], o2[1], o2[2], o2[3]};
                *(LAS f32x4*)(mine + 1024) = f32x4{o2[4], o2[5], o2[6], o2[7]};
                *(LAS float*)(lds + L_REG + w * 3072 + 2048 + lane * 4) = o2[8];
                BAR();
                const f32x4 a0 = *(LAS const f32x4*)other, a1 = *(LAS const f32x4*)(other + 1024);
                o[8] += *(LAS const float*)(lds + L_REG + (w ^ 1) * 3072 + 2048 + lane * 4);
                asm volatile("" ::: "memory");
                lds_post(lds + L_DONE + w * 4, 3 * L + 1);
#pragma unroll
                for (int i = 0; i < 4; ++i) { o[i] += a0[i]; o[4 + i] += a1[i]; }
            }
#define O_STORE() do { const float il = __builtin_amdgcn_rcpf(__uint_as_float(__builtin_amdgcn_permlane32_swap(__float_as_uint(o[8]), __float_as_uint(o[8]), false, false)[0])); \
                st_frag(u32x4{pk2(o[0] * il, o[1] * il), pk2(o[2] * il, o[3] * il), pk2(o[4] * il, o[5] * il), pk2(o[6] * il, o[7] * il)}, ws, lane * 16, \
                        WS_O + L * 0x200000 + ((g * 8 + w) * 32 + hd) * 1024); SBAR(); } while (0)
            O_STORE();
            float chk = __builtin_fabsf(o[8]);
#pragma unroll
            for (int i = 0; i < 8; ++i) chk += __builtin_fabsf(o[i]);
            if (__builtin_expect(__any(!(chk < 1e36f)), 0)) {
                float m = -__builtin_inff();
                o = f32x16{};
                const u32x4 one4 = {0x3f803f80u, 0x3f803f80u, 0x3f803f80u, 0x3f803f80u};
#pragma unroll 1
                for (int t = 0; t < 64; ++t) {
                    const bf16x8 kk = ld_frag(ws, lane * 16, kv_soff + t * 2048);
                    u32x4 a0 = __builtin_amdgcn_raw_buffer_load_b128(ws, hh * 256 + (lane & 15) * 16, kv_soff + t * 2048 + 1024, AUX_SC1);
                    u32x4 a1 = __builtin_amdgcn_raw_buffer_load_b128(ws, hh * 256 + (lane & 15) * 16, kv_soff + t * 2048 + 1536, AUX_SC1);
                    a0 = gq ? one4 : a0; a1 = gq ? one4 : a1;
                    f32x16 s = MFMA(kk, qf, f32x16{});
                    float tm = m;
#pragma unroll
                    for (int i = 0; i < 16; ++i) tm = __builtin_fmaxf(tm, s[i]);
                    const auto sw = __builtin_amdgcn_permlane32_swap(__float_as_uint(tm), __float_as_uint(tm), false, false);
                    tm = __builtin_fmaxf(__uint_as_float(sw[0]), __uint_as_float(sw[1]));
                    const float al = EX(m - tm);
                    m = tm;
#pragma unroll
                    for (int i = 0; i < 16; ++i) { o[i] *= al; s[i] = EX(s[i] - m); }
                    o = smf(__builtin_bit_cast(bf16x8, a0), lo8(s), o, idx);
                    o = smf(__builtin_bit_cast(bf16x8, a1), hi8(s), o, idx);
                }
                VMWAIT(0);
                O_STORE();
            }
            publish<1>(ws, WS_C2 + ((L * 64 + g * 8 + w) * 2 + (hd >> 4)) * 128, lane);
            touch<1, 0>(lds0 + L_SST + w * 256, (lane & 7) * 128, ws, WS_O + L * 0x200000 + ((g * 8 + w) * 32 + hd) * 1024);
        }
        if (w_ < 4) {
            f32x16 acc0, acc1;
            u32x2 bb[4];
            unsigned b2u;
            __builtin_amdgcn_s_setprio(2);
            {
                PHASE_IDS
                const int kh = w >> 1;
#pragma unroll
                for (int i = 0; i < 4; ++i) bb[i] = *(const u32x2*)(b1 + ((w1_on ? ft1 : 0) * 2 + kh) * 32 + 8 * i + 4 * hh);
                b2u = b2[own_s32 * 32 + l31];
                acc0 = unpack16(xres);
                PIN(acc0);
                wait_cnt(ws, WS_C2 + ((L * 64 + own_s32) * 2 + kh) * 128, 16, lane);
                gemm_compute<4, 0, false, false>(lds, lane, kh, 0, ws, WS_O + L * 0x200000 + (own_s32 * 32 + kh * 16) * 1024, 0, lds + L_DONE + (lane & 7) * 4, 3 * L + 1, acc0, acc1);
                exchange(lds, lane, w, w ^ 2, acc0, acc1);
#pragma unroll
                for (int i = 0; i < 8; ++i) xres[i] = pk2(acc0[2 * i], acc0[2 * i + 1]);
                const unsigned st_soff = WS_ST + (L * 64 + own_s32) * 4096, tg = L + 1;
                {
                    const u32x2 rp = row_partial(unpack16(xres));
                    __builtin_amdgcn_raw_buffer_store_b64(u32x2{rp[0], (rp[1] & ~3u) | tg}, ws, hh ? OOB : l31 * 8u, st_soff + own_d32 * 256, AUX_SC1);
                    arrive<1>(ws, WS_C6 + (L * 64 + own_s32) * 128, lane);
                }
                const lptr tp = lds + L_SCR + (w ^ 2) * 4096;
                *(LAS u32x4*)(tp + l31 * 64 + hh * 32) = u32x4{xres[0], xres[1], xres[2], xres[3]};
                *(LAS u32x4*)(tp + l31 * 64 + hh * 32 + 16) = u32x4{xres[4], xres[5], xres[6], xres[7]};
                const lptr rp = tp + (4 * hh + ((lane >> 2) & 3)) * 64 + ((lane >> 4) & 1) * 32 + (lane & 3) * 8;
                float xt[16];
#pragma unroll
                for (int t = 0; t < 2; ++t) {
                    const u32x4 v = __builtin_bit_cast(u32x4, tr_frag(rp + t * 1024, rp + t * 1024 + 512));
#pragma unroll
                    for (int i = 0; i < 4; ++i) { xt[8 * t + 2 * i] = bf_lo(v[i]); xt[8 * t + 2 * i + 1] = bf_hi(v[i]); }
                }
#pragma unroll
                for (int i = 0; i < 16; ++i) PIN(xt[i]);
                wait_cnt(ws, WS_C6 + (L * 64 + own_s32) * 128, 16, lane);
                u32x4 gv[4];
#pragma unroll 1
                for (unsigned it = 0; it < SPIN_CAP; ++it) {
                    unsigned bad = 0;
#pragma unroll
                    for (int i = 0; i < 4; ++i) gv[i] = __builtin_amdgcn_raw_buffer_load_b128(ws, lane * 16 + i * 1024, st_soff, AUX_SC1);
#pragma unroll
                    for (int i = 0; i < 4; ++i) bad |= (gv[i][1] ^ tg) | (gv[i][3] ^ tg);
                    if (__all((bad & 3) == 0)) break;
                    asm volatile("" ::: "memory");
                }
                {
                    const f32x4 tot = record_total(gv);
                    const float m0 = tot[0] * (1.f / 512.f), m1 = tot[2] * (1.f / 512.f);
                    const float r0 = __builtin_amdgcn_rsqf(__builtin_fmaxf(tot[1] * (1.f / 512.f) - m0 * m0, 0.f) * (1.f + LN_EPS) + LN_EPS * LN_EPS);
                    const float r1 = __builtin_amdgcn_rsqf(__builtin_fmaxf(tot[3] * (1.f / 512.f) - m1 * m1, 0.f) * (1.f + LN_EPS) + LN_EPS * LN_EPS);
                    *(LAS f32x2*)(tp + 2048 + hh * 128 + (lane & 15) * 8) = hh ? f32x2{m0 * r0, m1 * r1} : f32x2{r0, r1};
                }
                const unsigned hv = (hh * 32 + ((l31 & 3) | ((l31 & 12) << 1) | ((l31 & 16) >> 2))) * 16;
#pragma unroll
                for (int t = 0; t < 2; ++t) {
                    u32x4 h;
#pragma unroll
                    for (int j = 0; j < 2; ++j) {
                        const f32x4 sc = *(LAS const f32x4*)(tp + 2048 + (16 * t + 8 * j + 4 * hh) * 4), ms = *(LAS const f32x4*)(tp + 2176 + (16 * t + 8 * j + 4 * hh) * 4);
                        const float* x = xt + 8 * t + 4 * j;
                        h[2 * j] = pk2(x[0] * sc[0] - ms[0], x[1] * sc[1] - ms[1]); h[2 * j + 1] = pk2(x[2] * sc[2] - ms[2], x[3] * sc[3] - ms[3]);
                    }
                    st_frag(h, ws, hv, WS_H2 + L * 0x200000 + (own_d32 * 128 + own_s32 * 2 + t) * 1024);
                }
                lds_post(lds + L_DONE + w * 4, 3 * L + 2);
                publish<2>(ws, WS_C3 + ((L * 16 + own_d32) * 2 + (own_s32 >> 5)) * 256, lane);
                if (L == 0 && bid == 0 && w == 0)
                    __builtin_amdgcn_raw_buffer_store_b64(u32x2{nonce[0] + 1u, nonce[1] + (nonce[0] == 0xffffffffu)}, ws, lane == 0 ? 0u : OOB, WS_NONCE, AUX_SC1);
            }
            if (w1_on) {
                PHASE_IDS
                const int kh = w >> 1, d32 = dt1 * 2 + (w & 1), f32t = ft1 * 2 + kh;
#pragma unroll
                for (int i = 0; i < 16; ++i) { const unsigned bw = bb[i >> 2][(i >> 1) & 1]; acc0[i] = (i & 1) ? bf_hi(bw) : bf_lo(bw); }
                PIN(acc0);
                wait_cnt(ws, WS_C3 + ((L * 16 + d32) * 2 + kh) * 256 + (g & 2) * 64, 32, lane);
                gemm_compute<16, 4, false, true>(lds, lane, kh, w & 1, ws, WS_H2 + L * 0x200000 + (d32 * 128 + kh * 64) * 1024, L * 32, lds + L_DONE + (w ^ 2) * 4, 3 * L + 2, acc0, acc1);
                exchange(lds, lane, w, w ^ 2, acc0, acc1);
                float u[16];
#pragma unroll
                for (int i = 0; i < 16; ++i) {
                    const float z = acc0[i];
                    const float e = EX(z * __builtin_fmaf(z * z, -0.10294324f, -2.30220818f));
                    u[i] = z * __builtin_amdgcn_rcpf(1.f + e);
                }
#pragma unroll
                for (int t = 0; t < 2; ++t)
                    st_frag(u32x4{pk2(u[8 * t], u[8 * t + 1]), pk2(u[8 * t + 2], u[8 * t + 3]), pk2(u[8 * t + 4], u[8 * t + 5]), pk2(u[8 * t + 6], u[8 * t + 7])}, ws, lane * 16,
                            WS_U + L * 0x200000 + (d32 * 112 + f32t * 2 + t) * 1024);
                lds_post(lds + L_DONE + w * 4, 3 * L + 3);
                publish<2>(ws, WS_C4 + ((L * 16 + d32) * 2 + ((f32t + 4) >> 5)) * 256, lane);
            }
            {
                PHASE_IDS
                const int kh = w & 1;
                const float bias = __uint_as_float(b2u << 16);
                acc0 = unpack16(xres) + bias;
                PIN(acc0);
                wait_cnt(ws, WS_C4 + ((L * 16 + own_d32) * 2 + kh) * 256 + (g & 1) * 128, 28, lane);
                gemm_compute<14, 4, true, true>(lds, lane, kh, w >> 1, ws, WS_U + L * 0x200000 + (own_d32 * 112 + kh * 56) * 1024, L * 32 + 16, lds + L_DONE + (w ^ 2) * 4, 3 * L + (w1_on ? 3 : 2), acc0, acc1);
                exchange(lds, lane, w, w ^ 1, acc0, acc1);
#pragma unroll
                for (int i = 0; i < 8; ++i) xres[i] = pk2(acc0[2 * i], acc0[2 * i + 1]);
                if (L == 0) {
#pragma unroll
                    for (int t = 0; t < 2; ++t)
                        st_frag(u32x4{xres[4 * t], xres[4 * t + 1], xres[4 * t + 2], xres[4 * t + 3]}, ws, (l31 >> 4) * 1024 + ((lane & 15) + 16 * hh) * 16 + t * 512,
                                WS_X2 + (own_s32 * 32 + own_d32 * 2) * 1024);
                    __builtin_amdgcn_raw_buffer_store_b64(row_partial(unpack16(xres)), ws, hh ? OOB : l31 * 8u, WS_SP + ((64 + own_s32) * 16 + own_d32) * 256, AUX_SC1);
                    publish<2>(ws, WS_C5 + own_s32 * 256, lane);
                    {
                        const unsigned xa = opaque(WS_X2 + (own_s32 * 32 + own_d32 * 2) * 1024 + (lane & 15) * 128), sa = opaque(WS_SP + ((64 + own_s32) * 16 + own_d32) * 256 + (lane & 1) * 128);
                        touch<1, 0>(lds0 + L_SST + w * 256, hh ? sa : xa, ws, 0);
                    }
                } else {
                    char* op = (char*)out + (own_s32 * 32 + l31) * 1024 + own_d32 * 64 + hh * 16;
#pragma unroll
                    for (int t = 0; t < 2; ++t) {
                        const auto s0 = __builtin_amdgcn_permlane32_swap(xres[4 * t], xres[4 * t + 2], false, false);
                        const auto s1 = __builtin_amdgcn_permlane32_swap(xres[4 * t + 1], xres[4 * t + 3], false, false);
                        *(u32x4*)(op + t * 32) = u32x4{s0[0], s1[0], s0[1], s1[1]};
                    }
                }
            }
            __builtin_amdgcn_s_setprio(0);
            UNDEF_WC();
        } else {
            PHASE_IDS
            const int lw = w - 4;
            const rsrc_t r1 = mk_rsrc(w1, 2048 * 3584), r2 = mk_rsrc(w2, 1792 * 4096);
            const unsigned v1o = wvoff(lane, 3584), v2o = wvoff(lane, 4096), lb = lds0 + lw * 4096;
            const unsigned g1 = ((lw >> 1) * 1024 + (lw & 1) * 32) * 3584 + ft1 * 128, g2 = ((lw >> 1) * 896 + (lw & 1) * 32) * 4096 + st64 * 128;
            const lptr lp = lds + lw * 4096 + lane * 16, fill = lds + L_FILL + lw * 4, prog = lds + L_PROG + (lw >> 1) * 8;
            u32x4 rg[8][4];
            if (w1_on) {
                ring_prefetch<0, 4, 4, 3584, 1>(lb, rg, r1, v1o, g1, fill, L * 32);
                BAR();
                ring_prefetch<4, 16, 4, 3584, 1>(lb, rg, r1, v1o, g1, fill, L * 32);
                ring_refill<16, 4, 1>(lp, rg, prog, fill, L * 32);
            }
            asm volatile("s_waitcnt lgkmcnt(0)" ::: "memory");
            ring_prefetch<8, 14, 4, 4096, 0>(lb, rg, r2, v2o, g2, fill, 0);
            BAR();
            ring_prefetch<0, 8, 4, 4096, 1>(lb, rg, r2, v2o, g2, fill, L * 32 + 16);
            VMWAIT(0);
            lds_post(fill, L * 32 + 24);
            ring_refill<14, 4, 0>(lp, rg, prog, fill, L * 32 + 16);
            BAR();
        }
    }
}

extern "C" void kernel_launch(void* const* d_in, const int* in_sizes, int n_in, void* d_out, int out_size, void* d_ws, size_t ws_size,
                              hipStream_t stream) {
    fused_fwd<<<256, 512, 0, stream>>>(d_in[0], d_in[1], d_in[2], d_in[4], (const unsigned short*)d_in[5], d_in[6], (const unsigned short*)d_in[7],
                                       (unsigned short*)d_out, (char*)d_ws);
}
```
